```python
import torch

def _softmax(x, axis=-1):
    return torch.softmax(x, dim=axis)
def _gelu(x):
    return torch.nn.functional.gelu(x, approximate="tanh")
def _silu(x):
    return torch.nn.functional.silu(x)
def _sigmoid(x):
    return torch.sigmoid(x)
def _layernorm(x, axis=-1, eps=1e-5):
    m = x.mean(dim=axis, keepdim=True)
    v = x.var(dim=axis, keepdim=True, unbiased=False)
    return (x - m) / torch.sqrt(v + eps)
def _scatter_add(tgt, idx, src, axis=0):
    return tgt.clone().index_add_(axis, idx, src)
def _segment_sum(src, seg, n):
    out = torch.zeros((n,) + src.shape[1:], dtype=src.dtype)
    return out.index_add_(0, seg, src)
def _pad(x, pw):
    flat = tuple(v for pair in reversed(pw) for v in pair)
    return torch.nn.functional.pad(x, flat)

kernel_name = 'gated_mix__linear_attn_block__linear_attn_block__prompt'

def setup_inputs():

    _w = (lambda a: (lambda t: t.to(torch.bfloat16) if t.is_floating_point() else t)(torch.as_tensor(a)))
    def _A_setup():
        B, S, D = 1, 2048, 1024
        return {
            "x": _w((lambda *s: torch.randn(*s, dtype=torch.bfloat16) * 0.05)(B, S, D)),
            "wqkv": _w((lambda *s: torch.randn(*s, dtype=torch.bfloat16) * 0.8)(D, 3 * D)),
            "wo": _w((lambda *s: torch.randn(*s, dtype=torch.bfloat16) * 0.05)(D, D)),
        }
    _A_in = _A_setup()

    def _B_setup():
        B, S, D = 1, 2048, 1024
        return {
            "x": _w((lambda *s: torch.randn(*s, dtype=torch.bfloat16) * 0.05)(B, S, D)),
            "wqkv": _w((lambda *s: torch.randn(*s, dtype=torch.bfloat16) * 0.8)(D, 3 * D)),
            "wo": _w((lambda *s: torch.randn(*s, dtype=torch.bfloat16) * 0.05)(D, D)),
        }
    _B_in = _B_setup()

    return {
        "A_x": _A_in["x"],
        "A_wqkv": _A_in["wqkv"],
        "A_wo": _A_in["wo"],
        "B_x": _B_in["x"],
        "B_wqkv": _B_in["wqkv"],
        "B_wo": _B_in["wo"],
        "gate_w": _w((lambda *s: torch.randn(*s, dtype=torch.bfloat16) * 0.05)(1024) * 16.0),
    }

def reference(A_x, A_wqkv, A_wo, B_x, B_wqkv, B_wo, gate_w):
    def _A_ref(x, wqkv, wo):
        B, S, D = x.shape
        H = 32; Dh = D // H
        qkv = (lambda x, *d: x.permute(*d))((x @ wqkv).reshape(B, S, 3, H, Dh), 2, 0, 3, 1, 4)
        q, k, v = qkv[0], qkv[1], qkv[2]
        phi = lambda t: torch.where(t > 0, t + 1, torch.exp(t)) + 1e-6
        pq, pk = phi(q), phi(k)
        kv = torch.einsum("bhsd,bhse->bhde", pk, v)
        z = torch.einsum("bhsd->bhd", pk)
        num = torch.einsum("bhqd,bhde->bhqe", pq, kv)
        den = torch.einsum("bhqd,bhd->bhq", pq, z)[..., None] + 1e-6
        o = num / den
        return (lambda x, *d: x.permute(*d))(o, 0, 2, 1, 3).reshape(B, S, D) @ wo

    def _B_ref(x, wqkv, wo):
        B, S, D = x.shape
        H = 32; Dh = D // H
        qkv = (lambda x, *d: x.permute(*d))((x @ wqkv).reshape(B, S, 3, H, Dh), 2, 0, 3, 1, 4)
        q, k, v = qkv[0], qkv[1], qkv[2]
        phi = lambda t: torch.where(t > 0, t + 1, torch.exp(t)) + 1e-6
        pq, pk = phi(q), phi(k)
        kv = torch.einsum("bhsd,bhse->bhde", pk, v)
        z = torch.einsum("bhsd->bhd", pk)
        num = torch.einsum("bhqd,bhde->bhqe", pq, kv)
        den = torch.einsum("bhqd,bhd->bhq", pq, z)[..., None] + 1e-6
        o = num / den
        return (lambda x, *d: x.permute(*d))(o, 0, 2, 1, 3).reshape(B, S, D) @ wo

    x = A_x
    g = _sigmoid((x * gate_w).sum(-1, keepdims=True))
    a = _A_ref(x, A_wqkv, A_wo)
    b = _B_ref(x, B_wqkv, B_wo)
    return g * a + (1 - g) * b
```

```cpp
#include <hip/hip_runtime.h>
#include <hip/hip_bf16.h>
typedef unsigned short u16;
__device__ inline float bf2f(u16 b){ return __uint_as_float(((unsigned)b)<<16); }
__device__ inline u16 f2bf(float f){ unsigned u=__float_as_uint(f); u+=0x7fffu+((u>>16)&1u); return (u16)(u>>16); }
constexpr int S=2048,D=1024,H=32,DH=32;
__global__ void k_qkv(const u16* x,const u16* wa,const u16* wb,float* qkv){
  int n=blockIdx.x*16+threadIdx.x, s=blockIdx.y*16+threadIdx.y, br=blockIdx.z;
  const u16* w=br?wb:wa;
  __shared__ float xs[16][17], ws[16][17];
  float acc=0;
  for(int k0=0;k0<D;k0+=16){
    xs[threadIdx.y][threadIdx.x]=bf2f(x[s*D+k0+threadIdx.x]);
    ws[threadIdx.y][threadIdx.x]=bf2f(w[(k0+threadIdx.y)*3072+n]);
    __syncthreads();
    for(int k=0;k<16;k++) acc+=xs[threadIdx.y][k]*ws[k][threadIdx.x];
    __syncthreads();
  }
  if(n<2048) acc=(acc>0?acc+1.f:__expf(acc))+1e-6f;
  qkv[((size_t)br*S+s)*3072+n]=acc;
}
__global__ void k_kv(const float* qkv,float* kv,float* z){
  int h=blockIdx.x, br=blockIdx.y; int d=threadIdx.y,e=threadIdx.x;
  float acc=0,zz=0;
  for(int s=0;s<S;s++){ const float* r=qkv+((size_t)br*S+s)*3072; float pk=r[1024+h*32+d]; acc+=pk*r[2048+h*32+e]; zz+=pk; }
  kv[((br*H+h)*32+d)*32+e]=acc; if(e==0) z[(br*H+h)*32+d]=zz;
}
__global__ void k_o(const u16* x,const u16* gw,const float* qkv,const float* kv,const float* z,float* o){
  int s=blockIdx.x; int t=threadIdx.x;
  __shared__ float red[1024];
  red[t]=bf2f(x[s*D+t])*bf2f(gw[t]); __syncthreads();
  for(int st=512;st>0;st>>=1){ if(t<st) red[t]+=red[t+st]; __syncthreads(); }
  float g=1.f/(1.f+__expf(-red[0]));
  int h=t/32,e=t%32;
  for(int br=0;br<2;br++){
    const float* r=qkv+((size_t)br*S+s)*3072+h*32; float num=0,den=0;
    for(int d=0;d<32;d++){ num+=r[d]*kv[((br*H+h)*32+d)*32+e]; den+=r[d]*z[(br*H+h)*32+d]; }
    o[(size_t)s*2048+br*1024+t]=num/(den+1e-6f)*(br?1.f-g:g);
  }
}
__global__ void k_out(const float* o,const u16* woa,const u16* wob,u16* out){
  int n=blockIdx.x*16+threadIdx.x, s=blockIdx.y*16+threadIdx.y;
  __shared__ float xs[16][17], ws[16][17];
  float acc=0;
  for(int k0=0;k0<2048;k0+=16){
    xs[threadIdx.y][threadIdx.x]=o[(size_t)s*2048+k0+threadIdx.x];
    int k=k0+threadIdx.y; const u16* w=k<1024?woa:wob;
    ws[threadIdx.y][threadIdx.x]=bf2f(w[(k&1023)*1024+n]);
    __syncthreads();
    for(int kk=0;kk<16;kk++) acc+=xs[threadIdx.y][kk]*ws[kk][threadIdx.x];
    __syncthreads();
  }
  out[(size_t)s*D+n]=f2bf(acc);
}
extern "C" void kernel_launch(void* const* d_in, const int* in_sizes, int n_in, void* d_out, int out_size, void* d_ws, size_t ws_size, hipStream_t stream){
  const u16* x=(const u16*)d_in[0]; const u16* wa=(const u16*)d_in[1]; const u16* woa=(const u16*)d_in[2];
  const u16* wb=(const u16*)d_in[4]; const u16* wob=(const u16*)d_in[5]; const u16* gw=(const u16*)d_in[6];
  float* qkv=(float*)d_ws; float* kv=qkv+(size_t)2*S*3072; float* z=kv+2*H*32*32; float* o=z+2*H*32;
  k_qkv<<<dim3(3072/16,S/16,2),dim3(16,16),0,stream>>>(x,wa,wb,qkv);
  k_kv<<<dim3(H,2),dim3(32,32),0,stream>>>(qkv,kv,z);
  k_o<<<S,1024,0,stream>>>(x,gw,qkv,kv,z,o);
  k_out<<<dim3(D/16,S/16),dim3(16,16),0,stream>>>(o,woa,wob,(u16*)d_out);
}
```

```cpp
#include <hip/hip_runtime.h>
#include <hip/hip_bf16.h>

__global__ __launch_bounds__(256, 1) void gmla_fused(const void* x, const void* wa, const void* woa, const void* wb, const void* wob,
                                                     const void* gw, void* out, void* ws) {
  __shared__ __attribute__((aligned(16))) char lds[163840];
  const unsigned wid = __builtin_amdgcn_readfirstlane(threadIdx.x >> 6);
  asm volatile(
    "s_load_dwordx16 s[4:19], %[ka], 0x0\n"
    "s_mov_b32 s48, %[wid]\n"
    "s_mov_b32 s53, %[bid]\n"
    "v_mbcnt_lo_u32_b32 v192, -1, 0\n"
    "v_mbcnt_hi_u32_b32 v192, -1, v192\n"
    "s_getpc_b64 s[70:71]\n"
    "Lpc%=:\n"
    "s_lshr_b32 s75, s53, 3\n"
    "s_lshl_b32 s75, s75, 2\n"
    "s_add_u32 s75, s75, s48\n"
    "s_mul_i32 s75, s75, 512\n"
    "s_and_b32 s75, s75, 0xffff\n"
    "v_lshlrev_b32 v195, 7, v192\n"
    "v_add_u32 v195, s75, v195\n"
    "v_min_u32 v195, Lfin%=-Lpc%=, v195\n"
    "s_mov_b64 exec, 15\n"
    "global_load_dword v249, v195, s[70:71]\n"
    "s_mov_b64 exec, -1\n"
    "s_lshr_b32 s77, s53, 3\n"
    "s_lshl2_add_u32 s77, s77, s48\n"
    "s_mul_i32 s83, s77, 6144\n"
    "s_and_b32 s75, s53, 3\n"
    "s_lshl_b32 s75, s75, 9\n"
    "s_add_u32 s83, s83, s75\n"
    "s_and_b32 s75, s53, 7\n"
    "s_lshl_b32 s75, s75, 7\n"
    "s_add_u32 s75, s75, s77\n"
    "s_lshl_b32 s84, s75, 12\n"
    "v_lshrrev_b32 v234, 2, v192\n"
    "v_mul_u32_u24 v234, 1536, v234\n"
    "v_lshl_add_u32 v234, v192, 7, v234\n"
    "v_and_b32 v235, 1, v192\n"
    "v_mul_u32_u24 v235, 1984, v235\n"
    "v_lshl_add_u32 v235, v192, 6, v235\n"
    "s_mov_b32 s22, 0x600000\n"
    "s_mov_b32 s23, 0x20000\n"
    "s_mov_b32 s26, 0x400000\n"
    "s_mov_b32 s27, 0x20000\n"
    "s_bitcmp0_b32 s53, 2\n"
    "s_waitcnt lgkmcnt(0)\n"
    "s_cselect_b32 s20, s6, s10\n"
    "s_cselect_b32 s21, s7, s11\n"
    "s_mov_b32 s73, 32768\n"
    "s_cselect_b32 s73, s73, 49152\n"
    "s_cselect_b32 s32, s8, s12\n"
    "s_cselect_b32 s33, s9, s13\n"
    "s_and_b32 s21, s21, 0xffff\n"
    "s_mov_b32 s24, s4\n"
    "s_and_b32 s25, s5, 0xffff\n"
    "s_mov_b64 exec, 4095\n"
    "buffer_load_dword v249, v234, s[20:23], s83 offen\n"
    "s_mov_b64 exec, 15\n"
    "buffer_load_dword v249, v235, s[24:27], s84 offen\n"
    "s_mov_b64 exec, -1\n"
    "s_bfe_u32 s81, s53, 0x20003\n"
    "s_lshr_b32 s76, s53, 5\n"
    "s_bfe_u32 s49, s53, 0x30003\n"
    "s_and_b32 s75, s53, 7\n"
    "s_lshl_b32 s75, s75, 2\n"
    "s_lshr_b32 s50, s53, 6\n"
    "s_or_b32 s50, s50, s75\n"
    "s_lshr_b32 s93, s50, 2\n"
    "s_xor_b32 s93, s93, 7\n"
    "v_lshrrev_b32 v193, 3, v192\n"
    "v_and_b32 v194, 7, v192\n"
    "v_and_b32 v195, 2, v193\n"
    "v_xor_b32 v195, v194, v195\n"
    "v_lshlrev_b32 v195, 4, v195\n"
    "v_mul_u32_u24 v198, 6144, v193\n"
    "v_add_u32 v198, v198, v195\n"
    "v_xor_b32 v199, 64, v198\n"
    "v_add_u32 v199, 48128, v199\n"
    "v_lshrrev_b32 v195, 4, v192\n"
    "v_xor_b32 v195, v194, v195\n"
    "v_lshlrev_b32 v195, 4, v195\n"
    "v_lshl_add_u32 v196, v193, 11, v195\n"
    "v_xor_b32 v197, 64, v196\n"
    "v_add_u32 v197, 15360, v197\n"
    "s_lshl_b32 s61, s48, 11\n"
    "s_mul_i32 s62, s48, 98304\n"
    "s_and_b32 s75, s50, 15\n"
    "s_lshl_b32 s75, s75, 7\n"
    "s_add_u32 s62, s62, s75\n"
    "s_lshl_b32 s59, s48, 13\n"
    "s_lshl_b32 s60, s48, 17\n"
    "s_lshl_b32 s75, s49, 19\n"
    "s_add_u32 s60, s60, s75\n"
    "s_lshl_b32 s75, s93, 15\n"
    "s_xor_b32 s60, s60, s75\n"
    "s_mov_b32 s22, 0x600000\n"
    "s_mov_b32 s23, 0x20000\n"
    "s_mov_b32 s26, 0x400000\n"
    "s_mov_b32 s27, 0x20000\n"
    "s_lshr_b32 s54, s48, 1\n"
    "s_and_b32 s55, s48, 1\n"
    "v_and_b32 v193, 15, v192\n"
    "v_lshrrev_b32 v194, 4, v192\n"
    "v_lshrrev_b32 v195, 1, v193\n"
    "v_xor_b32 v195, v194, v195\n"
    "v_lshlrev_b32 v195, 4, v195\n"
    "v_lshl_add_u32 v195, v193, 7, v195\n"
    "s_lshl_b32 s75, s54, 14\n"
    "v_add_u32 v200, s75, v195\n"
    "v_xor_b32 v201, 64, v200\n"
    "v_add_u32 v202, 0x10000, v200\n"
    "v_add_u32 v203, 0x10000, v201\n"
    "v_lshlrev_b32 v253, 2, v193\n"
    "v_bfe_u32 v193, v192, 2, 2\n"
    "v_lshlrev_b32 v195, 10, v194\n"
    "v_lshl_add_u32 v195, v193, 7, v195\n"
    "v_and_b32 v251, 3, v192\n"
    "v_lshl_add_u32 v195, v251, 3, v195\n"
    "v_lshrrev_b32 v205, 1, v193\n"
    "v_and_b32 v206, 1, v194\n"
    "v_lshl_or_b32 v205, v206, 1, v205\n"
    "s_lshl_b32 s75, s55, 1\n"
    "v_xor_b32 v205, s75, v205\n"
    "v_lshl_add_u32 v195, v205, 5, v195\n"
    "v_add_u32 v204, 98304, v195\n"
    "v_xor_b32 v205, 32, v204\n"
    "s_lshl_b32 s75, s54, 5\n"
    "v_xor_b32 v206, s75, v204\n"
    "v_xor_b32 v207, 32, v206\n"
    "s_mov_b32 s28, s18\n"
    "s_and_b32 s29, s19, 0xffff\n"
    "s_mov_b32 s30, 0x1100000\n"
    "s_mov_b32 s31, 0x20000\n"
    "s_mov_b32 s40, s14\n"
    "s_and_b32 s41, s15, 0xffff\n"
    "s_mov_b32 s42, 0x800\n"
    "s_mov_b32 s43, 0x20000\n"
    "v_lshlrev_b32 v216, 4, v192\n"
    "v_mov_b32 v242, 0\n"
    "v_mov_b32 v243, 0\n"
    "s_add_u32 m0, s61, 98304\n"
    "s_add_u32 s63, s62, 0\n"
    "buffer_load_dwordx4 v198, s[20:23], s63 offen lds\n"
    "buffer_load_dwordx4 v199, s[20:23], s63 offen offset:1024 lds\n"
    "s_add_u32 m0, s61, 106496\n"
    "s_add_u32 s63, s62, 2048\n"
    "buffer_load_dwordx4 v198, s[20:23], s63 offen lds\n"
    "buffer_load_dwordx4 v199, s[20:23], s63 offen offset:1024 lds\n"
    "s_add_u32 m0, s61, 114688\n"
    "s_add_u32 s63, s62, 4096\n"
    "buffer_load_dwordx4 v198, s[20:23], s63 offen lds\n"
    "buffer_load_dwordx4 v199, s[20:23], s63 offen offset:1024 lds\n"
    "s_add_u32 m0, s59, 0\n"
    "s_xor_b32 s63, s60, 0\n"
    "buffer_load_dwordx4 v196, s[24:27], s63 offen lds\n"
    "buffer_load_dwordx4 v197, s[24:27], s63 offen offset:1024 lds\n"
    "s_add_u32 m0, s59, 2048\n"
    "s_xor_b32 s63, s60, 32768\n"
    "buffer_load_dwordx4 v196, s[24:27], s63 offen lds\n"
    "buffer_load_dwordx4 v197, s[24:27], s63 offen offset:1024 lds\n"
    "s_add_u32 m0, s59, 4096\n"
    "s_xor_b32 s63, s60, 65536\n"
    "buffer_load_dwordx4 v196, s[24:27], s63 offen lds\n"
    "buffer_load_dwordx4 v197, s[24:27], s63 offen offset:1024 lds\n"
    "s_add_u32 m0, s59, 6144\n"
    "s_xor_b32 s63, s60, 98304\n"
    "buffer_load_dwordx4 v196, s[24:27], s63 offen lds\n"
    "buffer_load_dwordx4 v197, s[24:27], s63 offen offset:1024 lds\n"
    "s_xor_b32 s52, s76, 2\n"
    "buffer_load_dword v250, v242, s[28:31], 0 offen sc1\n"
    "buffer_load_dwordx4 v[218:221], v216, s[40:43], 0 offen offset:0\n"
    "buffer_load_dwordx4 v[222:225], v216, s[40:43], 0 offen offset:1024\n"
    "s_add_u32 s83, s83, 786432\n"
    "s_add_u32 s84, s84, 256\n"
    "s_mov_b64 exec, 4095\n"
    "buffer_load_dword v249, v234, s[20:23], s83 offen\n"
    "s_mov_b64 exec, 15\n"
    "buffer_load_dword v249, v235, s[24:27], s84 offen\n"
    "s_mov_b64 exec, -1\n"
    "s_waitcnt vmcnt(11)\n"
    "s_barrier\n"
    "ds_read_b128 a[96:99], v200 offset:0\n"
    "ds_read_b128 a[100:103], v201 offset:0\n"
    "ds_read_b64_tr_b16 a[0:1], v204 offset:0\n"
    "ds_read_b64_tr_b16 a[2:3], v204 offset:512\n"
    "ds_read_b64_tr_b16 a[4:5], v204 offset:4096\n"
    "ds_read_b64_tr_b16 a[6:7], v204 offset:4608\n"
    "ds_read_b64_tr_b16 a[8:9], v205 offset:0\n"
    "ds_read_b64_tr_b16 a[10:11], v205 offset:512\n"
    "ds_read_b64_tr_b16 a[12:13], v205 offset:4096\n"
    "ds_read_b64_tr_b16 a[14:15], v205 offset:4608\n"
    "ds_read_b128 a[104:107], v200 offset:8192\n"
    "ds_read_b128 a[108:111], v201 offset:8192\n"
    "s_waitcnt lgkmcnt(10)\n"
    "s_waitcnt lgkmcnt(8)\n"
    "v_mfma_f32_16x16x32_bf16 v[0:3], a[0:3], a[96:99], 1.0\n"
    "ds_read_b64_tr_b16 a[16:17], v204 offset:8192\n"
    "ds_read_b64_tr_b16 a[18:19], v204 offset:8704\n"
    "ds_read_b64_tr_b16 a[20:21], v204 offset:12288\n"
    "s_waitcnt lgkmcnt(9)\n"
    "v_mfma_f32_16x16x32_bf16 v[0:3], a[4:7], a[100:103], v[0:3]\n"
    "ds_read_b64_tr_b16 a[22:23], v204 offset:12800\n"
    "ds_read_b64_tr_b16 a[24:25], v205 offset:8192\n"
    "ds_read_b64_tr_b16 a[26:27], v205 offset:8704\n"
    "s_waitcnt lgkmcnt(10)\n"
    "v_mfma_f32_16x16x32_bf16 v[4:7], a[8:11], a[96:99], 1.0\n"
    "ds_read_b64_tr_b16 a[28:29], v205 offset:12288\n"
    "ds_read_b64_tr_b16 a[30:31], v205 offset:12800\n"
    "ds_read_b64_tr_b16 a[32:33], v206 offset:16384\n"
    "s_waitcnt lgkmcnt(11)\n"
    "v_mfma_f32_16x16x32_bf16 v[4:7], a[12:15], a[100:103], v[4:7]\n"
    "ds_read_b64_tr_b16 a[34:35], v206 offset:16896\n"
    "ds_read_b64_tr_b16 a[36:37], v206 offset:20480\n"
    "ds_read_b64_tr_b16 a[38:39], v206 offset:20992\n"
    "s_waitcnt lgkmcnt(10)\n"
    "v_mfma_f32_16x16x32_bf16 v[8:11], a[96:99], a[16:19], 1.0\n"
    "ds_read_b64_tr_b16 a[40:41], v207 offset:16384\n"
    "ds_read_b64_tr_b16 a[42:43], v207 offset:16896\n"
    "ds_read_b64_tr_b16 a[44:45], v207 offset:20480\n"
    "s_waitcnt lgkmcnt(11)\n"
    "v_mfma_f32_16x16x32_bf16 v[8:11], a[100:103], a[20:23], v[8:11]\n"
    "ds_read_b64_tr_b16 a[46:47], v207 offset:20992\n"
    "s_add_u32 m0, s61, 122880\n"
    "s_waitcnt lgkmcnt(10)\n"
    "v_mfma_f32_16x16x32_bf16 v[12:15], a[96:99], a[24:27], 1.0\n"
    "s_add_u32 s63, s62, 393216\n"
    "buffer_load_dwordx4 v198, s[20:23], s63 offen lds\n"
    "s_waitcnt lgkmcnt(8)\n"
    "v_mfma_f32_16x16x32_bf16 v[12:15], a[100:103], a[28:31], v[12:15]\n"
    "buffer_load_dwordx4 v199, s[20:23], s63 offen offset:1024 lds\n"
    "s_add_u32 m0, s61, 131072\n"
    "s_waitcnt lgkmcnt(6)\n"
    "v_mfma_f32_16x16x32_bf16 v[16:19], a[96:99], a[32:35], 0\n"
    "s_add_u32 s63, s62, 395264\n"
    "buffer_load_dwordx4 v198, s[20:23], s63 offen lds\n"
    "s_waitcnt lgkmcnt(4)\n"
    "v_mfma_f32_16x16x32_bf16 v[16:19], a[100:103], a[36:39], v[16:19]\n"
    "buffer_load_dwordx4 v199, s[20:23], s63 offen offset:1024 lds\n"
    "s_add_u32 m0, s61, 139264\n"
    "s_waitcnt lgkmcnt(2)\n"
    "v_mfma_f32_16x16x32_bf16 v[20:23], a[96:99], a[40:43], 0\n"
    "s_add_u32 s63, s62, 397312\n"
    "buffer_load_dwordx4 v198, s[20:23], s63 offen lds\n"
    "s_waitcnt lgkmcnt(0)\n"
    "v_mfma_f32_16x16x32_bf16 v[20:23], a[100:103], a[44:47], v[20:23]\n"
    "s_waitcnt vmcnt(14)\n"
    "s_barrier\n"
    "v_mfma_f32_16x16x32_bf16 v[96:99], a[0:3], a[104:107], 1.0\n"
    "ds_read_b128 a[112:115], v200 offset:2048\n"
    "ds_read_b128 a[116:119], v201 offset:2048\n"
    "v_mfma_f32_16x16x32_bf16 v[96:99], a[4:7], a[108:111], v[96:99]\n"
    "ds_read_b128 a[96:99], v200 offset:10240\n"
    "ds_read_b128 a[100:103], v201 offset:10240\n"
    "v_mfma_f32_16x16x32_bf16 v[100:103], a[8:11], a[104:107], 1.0\n"
    "buffer_load_dwordx4 v199, s[20:23], s63 offen offset:1024 lds\n"
    "s_add_u32 m0, s59, 32768\n"
    "v_mfma_f32_16x16x32_bf16 v[100:103], a[12:15], a[108:111], v[100:103]\n"
    "s_xor_b32 s63, s60, 128\n"
    "buffer_load_dwordx4 v196, s[24:27], s63 offen lds\n"
    "v_mfma_f32_16x16x32_bf16 v[104:107], a[104:107], a[16:19], 1.0\n"
    "buffer_load_dwordx4 v197, s[24:27], s63 offen offset:1024 lds\n"
    "v_lshrrev_b32 v217, 3, v192\n"
    "v_mfma_f32_16x16x32_bf16 v[104:107], a[108:111], a[20:23], v[104:107]\n"
    "s_add_u32 m0, s59, 34816\n"
    "v_and_b32 v252, 7, v192\n"
    "v_mfma_f32_16x16x32_bf16 v[108:111], a[104:107], a[24:27], 1.0\n"
    "s_xor_b32 s63, s60, 32896\n"
    "s_lshl_b32 s77, s76, 2\n"
    "v_mfma_f32_16x16x32_bf16 v[108:111], a[108:111], a[28:31], v[108:111]\n"
    "buffer_load_dwordx4 v196, s[24:27], s63 offen lds\n"
    "s_or_b32 s77, s77, s48\n"
    "v_mfma_f32_16x16x32_bf16 v[112:115], a[104:107], a[32:35], 0\n"
    "buffer_load_dwordx4 v197, s[24:27], s63 offen offset:1024 lds\n"
    "s_mul_i32 s75, s77, 12288\n"
    "v_mfma_f32_16x16x32_bf16 v[112:115], a[108:111], a[36:39], v[112:115]\n"
    "s_add_u32 m0, s59, 36864\n"
    "s_and_b32 s78, s50, 12\n"
    "v_mfma_f32_16x16x32_bf16 v[116:119], a[104:107], a[40:43], 0\n"
    "s_xor_b32 s63, s60, 65664\n"
    "s_lshl_b32 s78, s78, 7\n"
    "v_mfma_f32_16x16x32_bf16 v[116:119], a[108:111], a[44:47], v[116:119]\n"
    "buffer_load_dwordx4 v196, s[24:27], s63 offen lds\n"
    "s_waitcnt lgkmcnt(2)\n"
    "v_mfma_f32_16x16x32_bf16 v[24:27], a[0:3], a[112:115], 1.0\n"
    "buffer_load_dwordx4 v197, s[24:27], s63 offen offset:1024 lds\n"
    "s_add_u32 s75, s75, s78\n"
    "v_mfma_f32_16x16x32_bf16 v[24:27], a[4:7], a[116:119], v[24:27]\n"
    "s_add_u32 m0, s59, 38912\n"
    "s_mul_i32 s78, s81, 393216\n"
    "v_mfma_f32_16x16x32_bf16 v[28:31], a[8:11], a[112:115], 1.0\n"
    "s_xor_b32 s63, s60, 98432\n"
    "s_add_u32 s75, s75, s78\n"
    "v_mfma_f32_16x16x32_bf16 v[28:31], a[12:15], a[116:119], v[28:31]\n"
    "buffer_load_dwordx4 v196, s[24:27], s63 offen lds\n"
    "v_mfma_f32_16x16x32_bf16 v[32:35], a[112:115], a[16:19], 1.0\n"
    "buffer_load_dwordx4 v197, s[24:27], s63 offen offset:1024 lds\n"
    "v_mfma_f32_16x16x32_bf16 v[32:35], a[116:119], a[20:23], v[32:35]\n"
    "s_add_u32 s44, s20, s75\n"
    "s_addc_u32 s45, s21, 0\n"
    "v_mfma_f32_16x16x32_bf16 v[36:39], a[112:115], a[24:27], 1.0\n"
    "s_add_u32 m0, s59, 65536\n"
    "s_and_b32 s75, s53, 7\n"
    "v_mfma_f32_16x16x32_bf16 v[36:39], a[116:119], a[28:31], v[36:39]\n"
    "s_xor_b32 s63, s60, 256\n"
    "s_lshl_b32 s75, s75, 5\n"
    "v_mfma_f32_16x16x32_bf16 v[40:43], a[112:115], a[32:35], 0\n"
    "buffer_load_dwordx4 v196, s[24:27], s63 offen lds\n"
    "s_add_u32 s75, s75, s77\n"
    "v_mfma_f32_16x16x32_bf16 v[40:43], a[116:119], a[36:39], v[40:43]\n"
    "s_lshl_b32 s75, s75, 14\n"
    "s_lshl_b32 s78, s81, 7\n"
    "v_mfma_f32_16x16x32_bf16 v[44:47], a[112:115], a[40:43], 0\n"
    "s_add_u32 s75, s75, s78\n"
    "v_mfma_f32_16x16x32_bf16 v[44:47], a[116:119], a[44:47], v[44:47]\n"
    "s_waitcnt lgkmcnt(0)\n"
    "s_waitcnt vmcnt(9)\n"
    "s_barrier\n"
    "v_mfma_f32_16x16x32_bf16 v[120:123], a[0:3], a[96:99], 1.0\n"
    "ds_read_b128 a[104:107], v200 offset:4096\n"
    "ds_read_b128 a[108:111], v201 offset:4096\n"
    "v_mfma_f32_16x16x32_bf16 v[120:123], a[4:7], a[100:103], v[120:123]\n"
    "ds_read_b128 a[112:115], v200 offset:12288\n"
    "ds_read_b128 a[116:119], v201 offset:12288\n"
    "v_mfma_f32_16x16x32_bf16 v[124:127], a[8:11], a[96:99], 1.0\n"
    "ds_read_b64_tr_b16 a[48:49], v204 offset:24576\n"
    "ds_read_b64_tr_b16 a[50:51], v204 offset:25088\n"
    "v_mfma_f32_16x16x32_bf16 v[124:127], a[12:15], a[100:103], v[124:127]\n"
    "ds_read_b64_tr_b16 a[52:53], v204 offset:28672\n"
    "ds_read_b64_tr_b16 a[54:55], v204 offset:29184\n"
    "v_mfma_f32_16x16x32_bf16 v[128:131], a[96:99], a[16:19], 1.0\n"
    "ds_read_b64_tr_b16 a[56:57], v205 offset:24576\n"
    "buffer_load_dwordx4 v197, s[24:27], s63 offen offset:1024 lds\n"
    "v_mfma_f32_16x16x32_bf16 v[128:131], a[100:103], a[20:23], v[128:131]\n"
    "ds_read_b64_tr_b16 a[58:59], v205 offset:25088\n"
    "s_add_u32 m0, s59, 67584\n"
    "v_mfma_f32_16x16x32_bf16 v[132:135], a[96:99], a[24:27], 1.0\n"
    "s_xor_b32 s63, s60, 33024\n"
    "buffer_load_dwordx4 v196, s[24:27], s63 offen lds\n"
    "v_mfma_f32_16x16x32_bf16 v[132:135], a[100:103], a[28:31], v[132:135]\n"
    "ds_read_b64_tr_b16 a[60:61], v205 offset:28672\n"
    "buffer_load_dwordx4 v197, s[24:27], s63 offen offset:1024 lds\n"
    "v_mfma_f32_16x16x32_bf16 v[136:139], a[96:99], a[32:35], 0\n"
    "ds_read_b64_tr_b16 a[62:63], v205 offset:29184\n"
    "s_add_u32 m0, s61, 98304\n"
    "v_mfma_f32_16x16x32_bf16 v[136:139], a[100:103], a[36:39], v[136:139]\n"
    "ds_read_b64_tr_b16 a[64:65], v204 offset:32768\n"
    "s_add_u32 s63, s62, 786432\n"
    "v_mfma_f32_16x16x32_bf16 v[140:143], a[96:99], a[40:43], 0\n"
    "ds_read_b64_tr_b16 a[66:67], v204 offset:33280\n"
    "buffer_load_dwordx4 v198, s[20:23], s63 offen lds\n"
    "v_mfma_f32_16x16x32_bf16 v[140:143], a[100:103], a[44:47], v[140:143]\n"
    "ds_read_b64_tr_b16 a[68:69], v204 offset:36864\n"
    "s_waitcnt lgkmcnt(13)\n"
    "v_mfma_f32_16x16x32_bf16 v[48:51], a[0:3], a[104:107], 1.0\n"
    "ds_read_b128 a[96:99], v200 offset:6144\n"
    "ds_read_b64_tr_b16 a[70:71], v204 offset:37376\n"
    "v_mfma_f32_16x16x32_bf16 v[48:51], a[4:7], a[108:111], v[48:51]\n"
    "ds_read_b128 a[100:103], v201 offset:6144\n"
    "buffer_load_dwordx4 v199, s[20:23], s63 offen offset:1024 lds\n"
    "v_mfma_f32_16x16x32_bf16 v[52:55], a[8:11], a[104:107], 1.0\n"
    "ds_read_b64_tr_b16 a[72:73], v205 offset:32768\n"
    "v_mfma_f32_16x16x32_bf16 v[52:55], a[12:15], a[108:111], v[52:55]\n"
    "ds_read_b64_tr_b16 a[74:75], v205 offset:33280\n"
    "v_mfma_f32_16x16x32_bf16 v[56:59], a[104:107], a[16:19], 1.0\n"
    "ds_read_b64_tr_b16 a[76:77], v205 offset:36864\n"
    "v_mfma_f32_16x16x32_bf16 v[56:59], a[108:111], a[20:23], v[56:59]\n"
    "ds_read_b64_tr_b16 a[78:79], v205 offset:37376\n"
    "v_mfma_f32_16x16x32_bf16 v[60:63], a[104:107], a[24:27], 1.0\n"
    "ds_read_b64_tr_b16 a[80:81], v206 offset:40960\n"
    "s_add_u32 m0, s59, 69632\n"
    "v_mfma_f32_16x16x32_bf16 v[60:63], a[108:111], a[28:31], v[60:63]\n"
    "ds_read_b64_tr_b16 a[82:83], v206 offset:41472\n"
    "s_xor_b32 s63, s60, 65792\n"
    "v_mfma_f32_16x16x32_bf16 v[64:67], a[104:107], a[32:35], 0\n"
    "buffer_load_dwordx4 v196, s[24:27], s63 offen lds\n"
    "v_mfma_f32_16x16x32_bf16 v[64:67], a[108:111], a[36:39], v[64:67]\n"
    "ds_read_b64_tr_b16 a[84:85], v206 offset:45056\n"
    "v_mfma_f32_16x16x32_bf16 v[68:71], a[104:107], a[40:43], 0\n"
    "ds_read_b64_tr_b16 a[86:87], v206 offset:45568\n"
    "v_mfma_f32_16x16x32_bf16 v[68:71], a[108:111], a[44:47], v[68:71]\n"
    "ds_read_b64_tr_b16 a[88:89], v207 offset:40960\n"
    "s_waitcnt lgkmcnt(14)\n"
    "v_mfma_f32_16x16x32_bf16 v[144:147], a[0:3], a[112:115], 1.0\n"
    "ds_read_b128 a[104:107], v200 offset:14336\n"
    "ds_read_b64_tr_b16 a[90:91], v207 offset:41472\n"
    "v_mfma_f32_16x16x32_bf16 v[144:147], a[4:7], a[116:119], v[144:147]\n"
    "ds_read_b128 a[108:111], v201 offset:14336\n"
    "ds_read_b64_tr_b16 a[92:93], v207 offset:45056\n"
    "v_mfma_f32_16x16x32_bf16 v[148:151], a[8:11], a[112:115], 1.0\n"
    "ds_read_b64_tr_b16 a[94:95], v207 offset:45568\n"
    "buffer_load_dwordx4 v197, s[24:27], s63 offen offset:1024 lds\n"
    "v_mfma_f32_16x16x32_bf16 v[148:151], a[12:15], a[116:119], v[148:151]\n"
    "s_add_u32 m0, s61, 106496\n"
    "s_add_u32 s63, s62, 788480\n"
    "v_mfma_f32_16x16x32_bf16 v[152:155], a[112:115], a[16:19], 1.0\n"
    "buffer_load_dwordx4 v198, s[20:23], s63 offen lds\n"
    "v_mfma_f32_16x16x32_bf16 v[152:155], a[116:119], a[20:23], v[152:155]\n"
    "buffer_load_dwordx4 v199, s[20:23], s63 offen offset:1024 lds\n"
    "v_mfma_f32_16x16x32_bf16 v[156:159], a[112:115], a[24:27], 1.0\n"
    "s_add_u32 m0, s61, 114688\n"
    "v_mfma_f32_16x16x32_bf16 v[156:159], a[116:119], a[28:31], v[156:159]\n"
    "s_add_u32 s63, s62, 790528\n"
    "v_mfma_f32_16x16x32_bf16 v[160:163], a[112:115], a[32:35], 0\n"
    "buffer_load_dwordx4 v198, s[20:23], s63 offen lds\n"
    "v_mfma_f32_16x16x32_bf16 v[160:163], a[116:119], a[36:39], v[160:163]\n"
    "s_add_u32 s46, s24, s75\n"
    "s_addc_u32 s47, s25, 0\n"
    "v_mfma_f32_16x16x32_bf16 v[164:167], a[112:115], a[40:43], 0\n"
    "s_add_u32 s75, s52, s81\n"
    "s_and_b32 s75, s75, 7\n"
    "v_mfma_f32_16x16x32_bf16 v[164:167], a[116:119], a[44:47], v[164:167]\n"
    "s_waitcnt vmcnt(11) lgkmcnt(0)\n"
    "s_barrier\n"
    "v_mfma_f32_16x16x32_bf16 v[72:75], a[0:3], a[96:99], 1.0\n"
    "ds_read_b128 a[112:115], v200 offset:32768\n"
    "buffer_load_dwordx4 v199, s[20:23], s63 offen offset:1024 lds\n"
    "v_mfma_f32_16x16x32_bf16 v[72:75], a[4:7], a[100:103], v[72:75]\n"
    "ds_read_b128 a[116:119], v201 offset:32768\n"
    "s_and_b32 s82, s75, 4\n"
    "v_mfma_f32_16x16x32_bf16 v[76:79], a[8:11], a[96:99], 1.0\n"
    "s_add_u32 m0, s61, 122880\n"
    "s_lshl_b32 s82, s82, 17\n"
    "v_mfma_f32_16x16x32_bf16 v[76:79], a[12:15], a[100:103], v[76:79]\n"
    "s_add_u32 s63, s62, 1179648\n"
    "s_sub_u32 s82, 0x40000, s82\n"
    "v_mfma_f32_16x16x32_bf16 v[80:83], a[96:99], a[16:19], 1.0\n"
    "buffer_load_dwordx4 v198, s[20:23], s63 offen lds\n"
    "s_lshl_b32 s75, s75, 2\n"
    "v_mfma_f32_16x16x32_bf16 v[80:83], a[100:103], a[20:23], v[80:83]\n"
    "s_and_b32 s78, s53, 3\n"
    "s_add_u32 s75, s75, s78\n"
    "v_mfma_f32_16x16x32_bf16 v[84:87], a[96:99], a[24:27], 1.0\n"
    "s_lshl_b32 s78, s48, 5\n"
    "s_add_u32 s75, s75, s78\n"
    "v_mfma_f32_16x16x32_bf16 v[84:87], a[100:103], a[28:31], v[84:87]\n"
    "buffer_load_dwordx4 v199, s[20:23], s63 offen offset:1024 lds\n"
    "s_lshl_b32 s75, s75, 14\n"
    "v_mfma_f32_16x16x32_bf16 v[88:91], a[96:99], a[32:35], 0\n"
    "s_lshl_b32 s78, s52, 8\n"
    "s_add_u32 s75, s75, s78\n"
    "v_mfma_f32_16x16x32_bf16 v[88:91], a[100:103], a[36:39], v[88:91]\n"
    "s_add_u32 s79, s32, s75\n"
    "s_addc_u32 s80, s33, 0\n"
    "v_mfma_f32_16x16x32_bf16 v[92:95], a[96:99], a[40:43], 0\n"
    "v_lshrrev_b32 v195, 2, v192\n"
    "v_lshlrev_b32 v195, 11, v195\n"
    "v_mfma_f32_16x16x32_bf16 v[92:95], a[100:103], a[44:47], v[92:95]\n"
    "v_lshl_add_u32 v195, v251, 7, v195\n"
    "v_mfma_f32_16x16x32_bf16 v[168:171], a[0:3], a[104:107], 1.0\n"
    "ds_read_b128 a[96:99], v200 offset:34816\n"
    "v_and_b32 v254, 1, v217\n"
    "v_mfma_f32_16x16x32_bf16 v[168:171], a[4:7], a[108:111], v[168:171]\n"
    "ds_read_b128 a[100:103], v201 offset:34816\n"
    "s_add_u32 m0, s59, 71680\n"
    "v_mfma_f32_16x16x32_bf16 v[172:175], a[8:11], a[104:107], 1.0\n"
    "s_xor_b32 s63, s60, 98560\n"
    "v_cmp_gt_u32 vcc, 32, v192\n"
    "v_mfma_f32_16x16x32_bf16 v[172:175], a[12:15], a[108:111], v[172:175]\n"
    "buffer_load_dwordx4 v196, s[24:27], s63 offen lds\n"
    "v_lshlrev_b32 v254, 7, v254\n"
    "v_mfma_f32_16x16x32_bf16 v[176:179], a[104:107], a[16:19], 1.0\n"
    "v_cndmask_b32 v254, v254, 0, vcc\n"
    "v_lshl_add_u32 v254, v252, 11, v254\n"
    "v_mfma_f32_16x16x32_bf16 v[176:179], a[108:111], a[20:23], v[176:179]\n"
    "v_mov_b32 v244, s79\n"
    "v_mov_b32 v245, s80\n"
    "v_mfma_f32_16x16x32_bf16 v[180:183], a[104:107], a[24:27], 1.0\n"
    "buffer_load_dwordx4 v197, s[24:27], s63 offen offset:1024 lds\n"
    "v_mov_b32 v246, s46\n"
    "v_mfma_f32_16x16x32_bf16 v[180:183], a[108:111], a[28:31], v[180:183]\n"
    "v_mov_b32 v247, s47\n"
    "v_mov_b32 v248, 512\n"
    "v_mfma_f32_16x16x32_bf16 v[184:187], a[104:107], a[32:35], 0\n"
    "v_cndmask_b32 v244, v244, v246, vcc\n"
    "v_cndmask_b32 v245, v245, v247, vcc\n"
    "v_mfma_f32_16x16x32_bf16 v[184:187], a[108:111], a[36:39], v[184:187]\n"
    "v_cndmask_b32 v248, 0, v248, vcc\n"
    "v_cmp_gt_u32 vcc, 24, v192\n"
    "v_mfma_f32_16x16x32_bf16 v[188:191], a[104:107], a[40:43], 0\n"
    "v_mov_b32 v246, s44\n"
    "v_mov_b32 v247, s45\n"
    "v_mfma_f32_16x16x32_bf16 v[188:191], a[108:111], a[44:47], v[188:191]\n"
    "v_cndmask_b32 v244, v244, v246, vcc\n"
    "v_cndmask_b32 v245, v245, v247, vcc\n"
    "v_cndmask_b32 v246, v254, v195, vcc\n"
    "v_mov_b32 v247, 0\n"
    "v_lshl_add_u64 v[244:245], v[246:247], 0, v[244:245]\n"
    "v_mov_b32 v195, 1572864\n"
    "v_cndmask_b32 v246, v248, v195, vcc\n"
    "s_cmp_eq_u32 s81, 0\n"
    "s_cbranch_scc0 Lpf0%=\n"
    "v_lshl_add_u64 v[244:245], v[246:247], 0, v[244:245]\n"
    "global_load_dword v249, v[244:245], off\n"
    "Lpf0%=:\n"
    "s_waitcnt lgkmcnt(2)\n"
    "v_mfma_f32_16x16x32_bf16 v[0:3], a[48:51], a[112:115], v[0:3]\n"
    "ds_read_b128 a[104:107], v200 offset:36864\n"
    "v_mfma_f32_16x16x32_bf16 v[0:3], a[52:55], a[116:119], v[0:3]\n"
    "ds_read_b128 a[108:111], v201 offset:36864\n"
    "v_mfma_f32_16x16x32_bf16 v[4:7], a[56:59], a[112:115], v[4:7]\n"
    "s_sub_u32 s32, s32, 2048\n"
    "s_subb_u32 s33, s33, 0\n"
    "v_mfma_f32_16x16x32_bf16 v[4:7], a[60:63], a[116:119], v[4:7]\n"
    "s_and_b32 s33, s33, 0xffff\n"
    "v_mfma_f32_16x16x32_bf16 v[8:11], a[112:115], a[64:67], v[8:11]\n"
    "s_mov_b32 s34, 0x200800\n"
    "v_mfma_f32_16x16x32_bf16 v[8:11], a[116:119], a[68:71], v[8:11]\n"
    "s_mov_b32 s35, 0x20000\n"
    "v_mfma_f32_16x16x32_bf16 v[12:15], a[112:115], a[72:75], v[12:15]\n"
    "s_and_b32 s75, s53, 3\n"
    "v_mfma_f32_16x16x32_bf16 v[12:15], a[116:119], a[76:79], v[12:15]\n"
    "s_lshl_b32 s75, s75, 2\n"
    "v_mfma_f32_16x16x32_bf16 v[16:19], a[112:115], a[80:83], v[16:19]\n"
    "s_add_u32 m0, s61, 131072\n"
    "s_or_b32 s51, s75, s81\n"
    "v_mfma_f32_16x16x32_bf16 v[16:19], a[116:119], a[84:87], v[16:19]\n"
    "s_add_u32 s63, s62, 1181696\n"
    "s_lshl_b32 s57, s50, 1\n"
    "v_mfma_f32_16x16x32_bf16 v[20:23], a[112:115], a[88:91], v[20:23]\n"
    "buffer_load_dwordx4 v198, s[20:23], s63 offen lds\n"
    "s_add_u32 s57, s57, s55\n"
    "v_mfma_f32_16x16x32_bf16 v[20:23], a[116:119], a[92:95], v[20:23]\n"
    "s_lshl_b32 s58, s49, 1\n"
    "s_waitcnt lgkmcnt(2)\n"
    "v_mfma_f32_16x16x32_bf16 v[24:27], a[48:51], a[96:99], v[24:27]\n"
    "ds_read_b128 a[112:115], v200 offset:38912\n"
    "s_add_u32 s58, s58, s54\n"
    "v_mfma_f32_16x16x32_bf16 v[24:27], a[52:55], a[100:103], v[24:27]\n"
    "ds_read_b128 a[116:119], v201 offset:38912\n"
    "buffer_load_dwordx4 v199, s[20:23], s63 offen offset:1024 lds\n"
    "v_mfma_f32_16x16x32_bf16 v[28:31], a[56:59], a[96:99], v[28:31]\n"
    "s_lshl_b32 s64, s48, 12\n"
    "v_mfma_f32_16x16x32_bf16 v[28:31], a[60:63], a[100:103], v[28:31]\n"
    "s_mov_b32 s36, s16\n"
    "v_mfma_f32_16x16x32_bf16 v[32:35], a[96:99], a[64:67], v[32:35]\n"
    "s_and_b32 s37, s17, 0xffff\n"
    "v_mfma_f32_16x16x32_bf16 v[32:35], a[100:103], a[68:71], v[32:35]\n"
    "s_mov_b32 s38, 0x400000\n"
    "v_mfma_f32_16x16x32_bf16 v[36:39], a[96:99], a[72:75], v[36:39]\n"
    "s_mov_b32 s39, 0x20000\n"
    "v_mfma_f32_16x16x32_bf16 v[36:39], a[100:103], a[76:79], v[36:39]\n"
    "v_lshl_add_u32 v195, v194, 2, v193\n"
    "v_mfma_f32_16x16x32_bf16 v[40:43], a[96:99], a[80:83], v[40:43]\n"
    "v_bfe_u32 v213, v195, 1, 2\n"
    "v_mfma_f32_16x16x32_bf16 v[40:43], a[100:103], a[84:87], v[40:43]\n"
    "v_lshlrev_b32 v213, 1, v213\n"
    "v_mfma_f32_16x16x32_bf16 v[44:47], a[96:99], a[88:91], v[44:47]\n"
    "v_xor_b32 v213, v213, v251\n"
    "v_mfma_f32_16x16x32_bf16 v[44:47], a[100:103], a[92:95], v[44:47]\n"
    "s_waitcnt lgkmcnt(2)\n"
    "s_waitcnt vmcnt(6)\n"
    "s_barrier\n"
    "v_mfma_f32_16x16x32_bf16 v[48:51], a[48:51], a[104:107], v[48:51]\n"
    "ds_read_b128 a[96:99], v200 offset:40960\n"
    "ds_read_b64_tr_b16 a[0:1], v204 offset:0\n"
    "v_mfma_f32_16x16x32_bf16 v[48:51], a[52:55], a[108:111], v[48:51]\n"
    "ds_read_b128 a[100:103], v201 offset:40960\n"
    "ds_read_b64_tr_b16 a[2:3], v204 offset:512\n"
    "v_mfma_f32_16x16x32_bf16 v[52:55], a[56:59], a[104:107], v[52:55]\n"
    "s_add_u32 m0, s59, 0\n"
    "v_lshlrev_b32 v213, 4, v213\n"
    "v_mfma_f32_16x16x32_bf16 v[52:55], a[60:63], a[108:111], v[52:55]\n"
    "ds_read_b64_tr_b16 a[4:5], v204 offset:4096\n"
    "s_xor_b32 s63, s60, 384\n"
    "v_mfma_f32_16x16x32_bf16 v[56:59], a[104:107], a[64:67], v[56:59]\n"
    "buffer_load_dwordx4 v196, s[24:27], s63 offen lds\n"
    "v_lshl_add_u32 v195, v195, 7, v213\n"
    "v_mfma_f32_16x16x32_bf16 v[56:59], a[108:111], a[68:71], v[56:59]\n"
    "ds_read_b64_tr_b16 a[6:7], v204 offset:4608\n"
    "s_add_u32 s75, s64, 98304\n"
    "v_mfma_f32_16x16x32_bf16 v[60:63], a[104:107], a[72:75], v[60:63]\n"
    "ds_read_b64_tr_b16 a[8:9], v205 offset:0\n"
    "v_add_u32 v212, s75, v195\n"
    "v_mfma_f32_16x16x32_bf16 v[60:63], a[108:111], a[76:79], v[60:63]\n"
    "buffer_load_dwordx4 v197, s[24:27], s63 offen offset:1024 lds\n"
    "v_xor_b32 v213, 8, v212\n"
    "v_mfma_f32_16x16x32_bf16 v[64:67], a[104:107], a[80:83], v[64:67]\n"
    "ds_read_b64_tr_b16 a[10:11], v205 offset:512\n"
    "v_xor_b32 v214, 64, v212\n"
    "v_mfma_f32_16x16x32_bf16 v[64:67], a[108:111], a[84:87], v[64:67]\n"
    "v_xor_b32 v215, 72, v212\n"
    "v_mfma_f32_16x16x32_bf16 v[68:71], a[104:107], a[88:91], v[68:71]\n"
    "ds_read_b64_tr_b16 a[12:13], v205 offset:4096\n"
    "v_and_b32 v193, 7, v192\n"
    "v_mfma_f32_16x16x32_bf16 v[68:71], a[108:111], a[92:95], v[68:71]\n"
    "ds_read_b64_tr_b16 a[14:15], v205 offset:4608\n"
    "s_waitcnt lgkmcnt(10)\n"
    "v_mfma_f32_16x16x32_bf16 v[72:75], a[48:51], a[112:115], v[72:75]\n"
    "ds_read_b128 a[104:107], v200 offset:43008\n"
    "v_lshlrev_b32 v195, 1, v194\n"
    "v_mfma_f32_16x16x32_bf16 v[72:75], a[52:55], a[116:119], v[72:75]\n"
    "ds_read_b128 a[108:111], v201 offset:43008\n"
    "ds_read_b64_tr_b16 a[16:17], v204 offset:8192\n"
    "v_mfma_f32_16x16x32_bf16 v[76:79], a[56:59], a[112:115], v[76:79]\n"
    "s_add_u32 m0, s61, 139264\n"
    "v_xor_b32 v193, v193, v195\n"
    "v_mfma_f32_16x16x32_bf16 v[76:79], a[60:63], a[116:119], v[76:79]\n"
    "ds_read_b64_tr_b16 a[18:19], v204 offset:8704\n"
    "s_add_u32 s63, s62, 1183744\n"
    "v_mfma_f32_16x16x32_bf16 v[80:83], a[112:115], a[64:67], v[80:83]\n"
    "ds_read_b64_tr_b16 a[20:21], v204 offset:12288\n"
    "buffer_load_dwordx4 v198, s[20:23], s63 offen lds\n"
    "v_mfma_f32_16x16x32_bf16 v[80:83], a[116:119], a[68:71], v[80:83]\n"
    "v_lshlrev_b32 v193, 4, v193\n"
    "v_mfma_f32_16x16x32_bf16 v[84:87], a[112:115], a[72:75], v[84:87]\n"
    "ds_read_b64_tr_b16 a[22:23], v204 offset:12800\n"
    "v_lshrrev_b32 v195, 3, v192\n"
    "v_mfma_f32_16x16x32_bf16 v[84:87], a[116:119], a[76:79], v[84:87]\n"
    "buffer_load_dwordx4 v199, s[20:23], s63 offen offset:1024 lds\n"
    "v_lshl_add_u32 v208, v195, 11, v193\n"
    "v_mfma_f32_16x16x32_bf16 v[88:91], a[112:115], a[80:83], v[88:91]\n"
    "ds_read_b64_tr_b16 a[24:25], v205 offset:8192\n"
    "s_lshl_b32 s75, s48, 19\n"
    "v_mfma_f32_16x16x32_bf16 v[88:91], a[116:119], a[84:87], v[88:91]\n"
    "ds_read_b64_tr_b16 a[26:27], v205 offset:8704\n"
    "s_lshl_b32 s76, s52, 8\n"
    "v_mfma_f32_16x16x32_bf16 v[92:95], a[112:115], a[88:91], v[92:95]\n"
    "s_add_u32 s75, s75, s76\n"
    "v_mfma_f32_16x16x32_bf16 v[92:95], a[116:119], a[92:95], v[92:95]\n"
    "ds_read_b64_tr_b16 a[28:29], v205 offset:12288\n"
    "s_waitcnt lgkmcnt(14)\n"
    "v_mfma_f32_16x16x32_bf16 v[96:99], a[48:51], a[96:99], v[96:99]\n"
    "ds_read_b128 a[112:115], v200 offset:45056\n"
    "v_add_u32 v208, s75, v208\n"
    "v_mfma_f32_16x16x32_bf16 v[96:99], a[52:55], a[100:103], v[96:99]\n"
    "ds_read_b128 a[116:119], v201 offset:45056\n"
    "ds_read_b64_tr_b16 a[30:31], v205 offset:12800\n"
    "v_mfma_f32_16x16x32_bf16 v[100:103], a[56:59], a[96:99], v[100:103]\n"
    "ds_read_b64_tr_b16 a[32:33], v206 offset:16384\n"
    "s_add_u32 m0, s59, 2048\n"
    "v_mfma_f32_16x16x32_bf16 v[100:103], a[60:63], a[100:103], v[100:103]\n"
    "s_xor_b32 s63, s60, 33152\n"
    "s_and_b32 s75, s51, 1\n"
    "v_mfma_f32_16x16x32_bf16 v[104:107], a[96:99], a[64:67], v[104:107]\n"
    "ds_read_b64_tr_b16 a[34:35], v206 offset:16896\n"
    "buffer_load_dwordx4 v196, s[24:27], s63 offen lds\n"
    "v_mfma_f32_16x16x32_bf16 v[104:107], a[100:103], a[68:71], v[104:107]\n"
    "s_lshl_b32 s75, s75, 15\n"
    "v_mfma_f32_16x16x32_bf16 v[108:111], a[96:99], a[72:75], v[108:111]\n"
    "ds_read_b64_tr_b16 a[36:37], v206 offset:20480\n"
    "s_xor_b32 s76, s75, 0x8000\n"
    "v_mfma_f32_16x16x32_bf16 v[108:111], a[100:103], a[76:79], v[108:111]\n"
    "ds_read_b64_tr_b16 a[38:39], v206 offset:20992\n"
    "buffer_load_dwordx4 v197, s[24:27], s63 offen offset:1024 lds\n"
    "v_mfma_f32_16x16x32_bf16 v[112:115], a[96:99], a[80:83], v[112:115]\n"
    "s_add_u32 s75, s75, 2048\n"
    "v_mfma_f32_16x16x32_bf16 v[112:115], a[100:103], a[84:87], v[112:115]\n"
    "ds_read_b64_tr_b16 a[40:41], v207 offset:16384\n"
    "v_add_u32 v210, s76, v208\n"
    "v_mfma_f32_16x16x32_bf16 v[116:119], a[96:99], a[88:91], v[116:119]\n"
    "v_add_u32 v208, s75, v208\n"
    "v_mfma_f32_16x16x32_bf16 v[116:119], a[100:103], a[92:95], v[116:119]\n"
    "ds_read_b64_tr_b16 a[42:43], v207 offset:16896\n"
    "s_waitcnt lgkmcnt(14)\n"
    "v_mfma_f32_16x16x32_bf16 v[120:123], a[48:51], a[104:107], v[120:123]\n"
    "ds_read_b128 a[96:99], v200 offset:47104\n"
    "ds_read_b64_tr_b16 a[44:45], v207 offset:20480\n"
    "v_mfma_f32_16x16x32_bf16 v[120:123], a[52:55], a[108:111], v[120:123]\n"
    "ds_read_b128 a[100:103], v201 offset:47104\n"
    "s_add_u32 m0, s59, 4096\n"
    "v_mfma_f32_16x16x32_bf16 v[124:127], a[56:59], a[104:107], v[124:127]\n"
    "ds_read_b64_tr_b16 a[46:47], v207 offset:20992\n"
    "s_xor_b32 s63, s60, 65920\n"
    "v_mfma_f32_16x16x32_bf16 v[124:127], a[60:63], a[108:111], v[124:127]\n"
    "buffer_load_dwordx4 v196, s[24:27], s63 offen lds\n"
    "v_add_u32 v209, 15360, v208\n"
    "v_mfma_f32_16x16x32_bf16 v[128:131], a[104:107], a[64:67], v[128:131]\n"
    "v_add_u32 v211, 15360, v210\n"
    "v_mfma_f32_16x16x32_bf16 v[128:131], a[108:111], a[68:71], v[128:131]\n"
    "buffer_load_dwordx4 v197, s[24:27], s63 offen offset:1024 lds\n"
    "s_lshl_b32 s65, s52, 16\n"
    "v_mfma_f32_16x16x32_bf16 v[132:135], a[104:107], a[72:75], v[132:135]\n"
    "s_mov_b32 s66, 0x3fb8aa3b\n"
    "v_mfma_f32_16x16x32_bf16 v[132:135], a[108:111], a[76:79], v[132:135]\n"
    "s_mov_b32 s67, 0x3fb8aa3b\n"
    "v_mfma_f32_16x16x32_bf16 v[136:139], a[104:107], a[80:83], v[136:139]\n"
    "s_mov_b32 s68, 0x358637bd\n"
    "v_mfma_f32_16x16x32_bf16 v[136:139], a[108:111], a[84:87], v[136:139]\n"
    "s_mov_b32 s69, 0x358637bd\n"
    "v_mfma_f32_16x16x32_bf16 v[140:143], a[104:107], a[88:91], v[140:143]\n"
    "s_mov_b32 s74, 131072\n"
    "v_mfma_f32_16x16x32_bf16 v[140:143], a[108:111], a[92:95], v[140:143]\n"
    "s_waitcnt vmcnt(10) lgkmcnt(0)\n"
    "s_barrier\n"
    "v_mfma_f32_16x16x32_bf16 v[144:147], a[48:51], a[112:115], v[144:147]\n"
    "ds_read_b128 a[104:107], v202 offset:0\n"
    "s_mov_b32 s96, 0\n"
    "v_mfma_f32_16x16x32_bf16 v[144:147], a[52:55], a[116:119], v[144:147]\n"
    "ds_read_b128 a[108:111], v203 offset:0\n"
    "v_mfma_f32_16x16x32_bf16 v[148:151], a[56:59], a[112:115], v[148:151]\n"
    "s_add_u32 m0, s61, 98304\n"
    "v_mfma_f32_16x16x32_bf16 v[148:151], a[60:63], a[116:119], v[148:151]\n"
    "s_add_u32 s63, s62, 1572864\n"
    "v_mfma_f32_16x16x32_bf16 v[152:155], a[112:115], a[64:67], v[152:155]\n"
    "buffer_load_dwordx4 v198, s[20:23], s63 offen lds\n"
    "v_mfma_f32_16x16x32_bf16 v[152:155], a[116:119], a[68:71], v[152:155]\n"
    "v_mfma_f32_16x16x32_bf16 v[156:159], a[112:115], a[72:75], v[156:159]\n"
    "v_mfma_f32_16x16x32_bf16 v[156:159], a[116:119], a[76:79], v[156:159]\n"
    "buffer_load_dwordx4 v199, s[20:23], s63 offen offset:1024 lds\n"
    "v_mfma_f32_16x16x32_bf16 v[160:163], a[112:115], a[80:83], v[160:163]\n"
    "v_mfma_f32_16x16x32_bf16 v[160:163], a[116:119], a[84:87], v[160:163]\n"
    "v_mfma_f32_16x16x32_bf16 v[164:167], a[112:115], a[88:91], v[164:167]\n"
    "v_mfma_f32_16x16x32_bf16 v[164:167], a[116:119], a[92:95], v[164:167]\n"
    "v_mfma_f32_16x16x32_bf16 v[168:171], a[48:51], a[96:99], v[168:171]\n"
    "ds_read_b128 a[112:115], v202 offset:2048\n"
    "v_mfma_f32_16x16x32_bf16 v[168:171], a[52:55], a[100:103], v[168:171]\n"
    "ds_read_b128 a[116:119], v203 offset:2048\n"
    "s_add_u32 m0, s59, 6144\n"
    "v_mfma_f32_16x16x32_bf16 v[172:175], a[56:59], a[96:99], v[172:175]\n"
    "s_xor_b32 s63, s60, 98688\n"
    "v_mfma_f32_16x16x32_bf16 v[172:175], a[60:63], a[100:103], v[172:175]\n"
    "buffer_load_dwordx4 v196, s[24:27], s63 offen lds\n"
    "v_mfma_f32_16x16x32_bf16 v[176:179], a[96:99], a[64:67], v[176:179]\n"
    "v_mfma_f32_16x16x32_bf16 v[176:179], a[100:103], a[68:71], v[176:179]\n"
    "v_mfma_f32_16x16x32_bf16 v[180:183], a[96:99], a[72:75], v[180:183]\n"
    "buffer_load_dwordx4 v197, s[24:27], s63 offen offset:1024 lds\n"
    "v_mfma_f32_16x16x32_bf16 v[180:183], a[100:103], a[76:79], v[180:183]\n"
    "s_cmp_eq_u32 s81, 1\n"
    "s_cbranch_scc0 Lpf1%=\n"
    "v_lshl_add_u64 v[244:245], v[246:247], 0, v[244:245]\n"
    "global_load_dword v249, v[244:245], off\n"
    "Lpf1%=:\n"
    "v_mfma_f32_16x16x32_bf16 v[184:187], a[96:99], a[80:83], v[184:187]\n"
    "v_mfma_f32_16x16x32_bf16 v[184:187], a[100:103], a[84:87], v[184:187]\n"
    "v_mfma_f32_16x16x32_bf16 v[188:191], a[96:99], a[88:91], v[188:191]\n"
    "v_mfma_f32_16x16x32_bf16 v[188:191], a[100:103], a[92:95], v[188:191]\n"
    "s_waitcnt lgkmcnt(2)\n"
    "v_mfma_f32_16x16x32_bf16 v[0:3], a[0:3], a[104:107], v[0:3]\n"
    "ds_read_b128 a[96:99], v202 offset:4096\n"
    "v_mfma_f32_16x16x32_bf16 v[0:3], a[4:7], a[108:111], v[0:3]\n"
    "ds_read_b128 a[100:103], v203 offset:4096\n"
    "v_mfma_f32_16x16x32_bf16 v[4:7], a[8:11], a[104:107], v[4:7]\n"
    "v_readfirstlane_b32 s56, v250\n"
    "v_mfma_f32_16x16x32_bf16 v[4:7], a[12:15], a[108:111], v[4:7]\n"
    "s_nop 0\n"
    "s_mul_i32 s56, s56, 0x9E3779B1\n"
    "v_mfma_f32_16x16x32_bf16 v[8:11], a[104:107], a[16:19], v[8:11]\n"
    "s_add_u32 s56, s56, 0x7F4A7C15\n"
    "v_mfma_f32_16x16x32_bf16 v[8:11], a[108:111], a[20:23], v[8:11]\n"
    "v_mov_b32 v250, s56\n"
    "v_mfma_f32_16x16x32_bf16 v[12:15], a[104:107], a[24:27], v[12:15]\n"
    "s_lshl_b32 s75, s51, 3\n"
    "v_mfma_f32_16x16x32_bf16 v[12:15], a[108:111], a[28:31], v[12:15]\n"
    "s_bfe_u32 s76, s50, 0x10004\n"
    "v_mfma_f32_16x16x32_bf16 v[16:19], a[104:107], a[32:35], v[16:19]\n"
    "s_add_u32 m0, s61, 106496\n"
    "s_lshl_b32 s76, s76, 2\n"
    "v_mfma_f32_16x16x32_bf16 v[16:19], a[108:111], a[36:39], v[16:19]\n"
    "s_add_u32 s63, s62, 1574912\n"
    "s_add_u32 s75, s75, s76\n"
    "v_mfma_f32_16x16x32_bf16 v[20:23], a[104:107], a[40:43], v[20:23]\n"
    "buffer_load_dwordx4 v198, s[20:23], s63 offen lds\n"
    "s_add_u32 s75, s75, s48\n"
    "v_mfma_f32_16x16x32_bf16 v[20:23], a[108:111], a[44:47], v[20:23]\n"
    "s_lshl_b32 s90, s75, 15\n"
    "s_waitcnt lgkmcnt(2)\n"
    "v_mfma_f32_16x16x32_bf16 v[24:27], a[0:3], a[112:115], v[24:27]\n"
    "ds_read_b128 a[104:107], v202 offset:6144\n"
    "s_lshl_b32 s76, s52, 8\n"
    "v_mfma_f32_16x16x32_bf16 v[24:27], a[4:7], a[116:119], v[24:27]\n"
    "ds_read_b128 a[108:111], v203 offset:6144\n"
    "buffer_load_dwordx4 v199, s[20:23], s63 offen offset:1024 lds\n"
    "v_mfma_f32_16x16x32_bf16 v[28:31], a[8:11], a[112:115], v[28:31]\n"
    "s_add_u32 s90, s90, s76\n"
    "v_mfma_f32_16x16x32_bf16 v[28:31], a[12:15], a[116:119], v[28:31]\n"
    "s_lshl_b32 s95, s75, 16\n"
    "v_mfma_f32_16x16x32_bf16 v[32:35], a[112:115], a[16:19], v[32:35]\n"
    "s_add_u32 s95, s95, 4194304\n"
    "v_mfma_f32_16x16x32_bf16 v[32:35], a[116:119], a[20:23], v[32:35]\n"
    "s_lshl_b32 s86, s75, 10\n"
    "v_mfma_f32_16x16x32_bf16 v[36:39], a[112:115], a[24:27], v[36:39]\n"
    "s_add_u32 s86, s86, 65536\n"
    "v_mfma_f32_16x16x32_bf16 v[36:39], a[116:119], a[28:31], v[36:39]\n"
    "s_mov_b32 s91, s75\n"
    "v_mfma_f32_16x16x32_bf16 v[40:43], a[112:115], a[32:35], v[40:43]\n"
    "s_lshl_b32 s89, s58, 10\n"
    "v_mfma_f32_16x16x32_bf16 v[40:43], a[116:119], a[36:39], v[40:43]\n"
    "s_add_u32 s89, s89, s73\n"
    "v_mfma_f32_16x16x32_bf16 v[44:47], a[112:115], a[40:43], v[44:47]\n"
    "s_lshl_b32 s84, s57, 1\n"
    "v_mfma_f32_16x16x32_bf16 v[44:47], a[116:119], a[44:47], v[44:47]\n"
    "s_waitcnt lgkmcnt(2)\n"
    "s_waitcnt vmcnt(10)\n"
    "s_barrier\n"
    "v_mfma_f32_16x16x32_bf16 v[48:51], a[0:3], a[96:99], v[48:51]\n"
    "ds_read_b128 a[112:115], v202 offset:8192\n"
    "ds_read_b64_tr_b16 a[48:49], v204 offset:24576\n"
    "v_mfma_f32_16x16x32_bf16 v[48:51], a[4:7], a[100:103], v[48:51]\n"
    "ds_read_b128 a[116:119], v203 offset:8192\n"
    "ds_read_b64_tr_b16 a[50:51], v204 offset:25088\n"
    "v_mfma_f32_16x16x32_bf16 v[52:55], a[8:11], a[96:99], v[52:55]\n"
    "s_add_u32 m0, s59, 32768\n"
    "s_add_u32 s84, s84, s54\n"
    "v_mfma_f32_16x16x32_bf16 v[52:55], a[12:15], a[100:103], v[52:55]\n"
    "ds_read_b64_tr_b16 a[52:53], v204 offset:28672\n"
    "s_xor_b32 s63, s60, 512\n"
    "v_mfma_f32_16x16x32_bf16 v[56:59], a[96:99], a[16:19], v[56:59]\n"
    "buffer_load_dwordx4 v196, s[24:27], s63 offen lds\n"
    "s_mul_i32 s84, s84, 24576\n"
    "v_mfma_f32_16x16x32_bf16 v[56:59], a[100:103], a[20:23], v[56:59]\n"
    "ds_read_b64_tr_b16 a[54:55], v204 offset:29184\n"
    "s_add_u32 s84, s84, 262144\n"
    "v_mfma_f32_16x16x32_bf16 v[60:63], a[96:99], a[24:27], v[60:63]\n"
    "ds_read_b64_tr_b16 a[56:57], v205 offset:24576\n"
    "s_mul_i32 s85, s49, 3072\n"
    "v_mfma_f32_16x16x32_bf16 v[60:63], a[100:103], a[28:31], v[60:63]\n"
    "buffer_load_dwordx4 v197, s[24:27], s63 offen offset:1024 lds\n"
    "s_add_u32 s85, s85, s84\n"
    "v_mfma_f32_16x16x32_bf16 v[64:67], a[96:99], a[32:35], v[64:67]\n"
    "ds_read_b64_tr_b16 a[58:59], v205 offset:25088\n"
    "s_lshl_b32 s75, s58, 6\n"
    "v_mfma_f32_16x16x32_bf16 v[64:67], a[100:103], a[36:39], v[64:67]\n"
    "s_add_u32 s75, s75, s57\n"
    "v_mfma_f32_16x16x32_bf16 v[68:71], a[96:99], a[40:43], v[68:71]\n"
    "ds_read_b64_tr_b16 a[60:61], v205 offset:28672\n"
    "s_lshl_b32 s83, s75, 7\n"
    "v_mfma_f32_16x16x32_bf16 v[68:71], a[100:103], a[44:47], v[68:71]\n"
    "ds_read_b64_tr_b16 a[62:63], v205 offset:29184\n"
    "s_waitcnt lgkmcnt(10)\n"
    "v_mfma_f32_16x16x32_bf16 v[72:75], a[0:3], a[104:107], v[72:75]\n"
    "ds_read_b128 a[96:99], v202 offset:10240\n"
    "s_add_u32 s83, s83, 65536\n"
    "v_mfma_f32_16x16x32_bf16 v[72:75], a[4:7], a[108:111], v[72:75]\n"
    "ds_read_b128 a[100:103], v203 offset:10240\n"
    "ds_read_b64_tr_b16 a[64:65], v204 offset:32768\n"
    "v_mfma_f32_16x16x32_bf16 v[76:79], a[8:11], a[104:107], v[76:79]\n"
    "s_add_u32 m0, s61, 114688\n"
    "s_lshl_b32 s97, s75, 4\n"
    "v_mfma_f32_16x16x32_bf16 v[76:79], a[12:15], a[108:111], v[76:79]\n"
    "ds_read_b64_tr_b16 a[66:67], v204 offset:33280\n"
    "s_add_u32 s63, s62, 1576960\n"
    "v_mfma_f32_16x16x32_bf16 v[80:83], a[104:107], a[16:19], v[80:83]\n"
    "ds_read_b64_tr_b16 a[68:69], v204 offset:36864\n"
    "buffer_load_dwordx4 v198, s[20:23], s63 offen lds\n"
    "v_mfma_f32_16x16x32_bf16 v[80:83], a[108:111], a[20:23], v[80:83]\n"
    "s_add_u32 s97, s97, 4096\n"
    "v_mfma_f32_16x16x32_bf16 v[84:87], a[104:107], a[24:27], v[84:87]\n"
    "ds_read_b64_tr_b16 a[70:71], v204 offset:37376\n"
    "s_lshl_b32 s87, s75, 13\n"
    "v_mfma_f32_16x16x32_bf16 v[84:87], a[108:111], a[28:31], v[84:87]\n"
    "buffer_load_dwordx4 v199, s[20:23], s63 offen offset:1024 lds\n"
    "s_add_u32 s87, s87, 4194304\n"
    "v_mfma_f32_16x16x32_bf16 v[88:91], a[104:107], a[32:35], v[88:91]\n"
    "ds_read_b64_tr_b16 a[72:73], v205 offset:32768\n"
    "s_add_u32 s88, s87, 4096\n"
    "v_mfma_f32_16x16x32_bf16 v[88:91], a[108:111], a[36:39], v[88:91]\n"
    "ds_read_b64_tr_b16 a[74:75], v205 offset:33280\n"
    "v_lshlrev_b32 v252, 3, v192\n"
    "v_mfma_f32_16x16x32_bf16 v[92:95], a[104:107], a[40:43], v[92:95]\n"
    "v_cmp_ne_u32 vcc, 63, v192\n"
    "v_mfma_f32_16x16x32_bf16 v[92:95], a[108:111], a[44:47], v[92:95]\n"
    "ds_read_b64_tr_b16 a[76:77], v205 offset:36864\n"
    "s_waitcnt lgkmcnt(14)\n"
    "v_mfma_f32_16x16x32_bf16 v[96:99], a[0:3], a[112:115], v[96:99]\n"
    "ds_read_b128 a[104:107], v202 offset:12288\n"
    "v_mov_b32 v193, 0x7ffffff0\n"
    "v_mfma_f32_16x16x32_bf16 v[96:99], a[4:7], a[116:119], v[96:99]\n"
    "ds_read_b128 a[108:111], v203 offset:12288\n"
    "ds_read_b64_tr_b16 a[78:79], v205 offset:37376\n"
    "v_mfma_f32_16x16x32_bf16 v[100:103], a[8:11], a[112:115], v[100:103]\n"
    "ds_read_b64_tr_b16 a[80:81], v206 offset:40960\n"
    "s_add_u32 m0, s59, 34816\n"
    "v_mfma_f32_16x16x32_bf16 v[100:103], a[12:15], a[116:119], v[100:103]\n"
    "s_xor_b32 s63, s60, 33280\n"
    "v_cndmask_b32 v251, 0, v193, vcc\n"
    "v_mfma_f32_16x16x32_bf16 v[104:107], a[112:115], a[16:19], v[104:107]\n"
    "ds_read_b64_tr_b16 a[82:83], v206 offset:41472\n"
    "buffer_load_dwordx4 v196, s[24:27], s63 offen lds\n"
    "v_mfma_f32_16x16x32_bf16 v[104:107], a[116:119], a[20:23], v[104:107]\n"
    "s_lshr_b32 s78, s65, 3\n"
    "v_mfma_f32_16x16x32_bf16 v[108:111], a[112:115], a[24:27], v[108:111]\n"
    "ds_read_b64_tr_b16 a[84:85], v206 offset:45056\n"
    "v_mfma_f32_16x16x32_bf16 v[108:111], a[116:119], a[28:31], v[108:111]\n"
    "ds_read_b64_tr_b16 a[86:87], v206 offset:45568\n"
    "buffer_load_dwordx4 v197, s[24:27], s63 offen offset:1024 lds\n"
    "v_mfma_f32_16x16x32_bf16 v[112:115], a[112:115], a[32:35], v[112:115]\n"
    "v_mfma_f32_16x16x32_bf16 v[112:115], a[116:119], a[36:39], v[112:115]\n"
    "ds_read_b64_tr_b16 a[88:89], v207 offset:40960\n"
    "v_mfma_f32_16x16x32_bf16 v[116:119], a[112:115], a[40:43], v[116:119]\n"
    "v_mfma_f32_16x16x32_bf16 v[116:119], a[116:119], a[44:47], v[116:119]\n"
    "ds_read_b64_tr_b16 a[90:91], v207 offset:41472\n"
    "s_waitcnt lgkmcnt(14)\n"
    "v_mfma_f32_16x16x32_bf16 v[120:123], a[0:3], a[96:99], v[120:123]\n"
    "ds_read_b128 a[112:115], v202 offset:14336\n"
    "ds_read_b64_tr_b16 a[92:93], v207 offset:45056\n"
    "v_mfma_f32_16x16x32_bf16 v[120:123], a[4:7], a[100:103], v[120:123]\n"
    "ds_read_b128 a[116:119], v203 offset:14336\n"
    "s_add_u32 m0, s59, 36864\n"
    "v_mfma_f32_16x16x32_bf16 v[124:127], a[8:11], a[96:99], v[124:127]\n"
    "ds_read_b64_tr_b16 a[94:95], v207 offset:45568\n"
    "s_xor_b32 s63, s60, 66048\n"
    "v_mfma_f32_16x16x32_bf16 v[124:127], a[12:15], a[100:103], v[124:127]\n"
    "buffer_load_dwordx4 v196, s[24:27], s63 offen lds\n"
    "v_mfma_f32_16x16x32_bf16 v[128:131], a[96:99], a[16:19], v[128:131]\n"
    "v_mfma_f32_16x16x32_bf16 v[128:131], a[100:103], a[20:23], v[128:131]\n"
    "buffer_load_dwordx4 v197, s[24:27], s63 offen offset:1024 lds\n"
    "v_mfma_f32_16x16x32_bf16 v[132:135], a[96:99], a[24:27], v[132:135]\n"
    "v_mfma_f32_16x16x32_bf16 v[132:135], a[100:103], a[28:31], v[132:135]\n"
    "v_mfma_f32_16x16x32_bf16 v[136:139], a[96:99], a[32:35], v[136:139]\n"
    "v_mfma_f32_16x16x32_bf16 v[136:139], a[100:103], a[36:39], v[136:139]\n"
    "v_mfma_f32_16x16x32_bf16 v[140:143], a[96:99], a[40:43], v[140:143]\n"
    "v_mfma_f32_16x16x32_bf16 v[140:143], a[100:103], a[44:47], v[140:143]\n"
    "s_waitcnt vmcnt(10) lgkmcnt(0)\n"
    "s_barrier\n"
    "v_mfma_f32_16x16x32_bf16 v[144:147], a[0:3], a[104:107], v[144:147]\n"
    "ds_read_b128 a[96:99], v200 offset:0\n"
    "v_mfma_f32_16x16x32_bf16 v[144:147], a[4:7], a[108:111], v[144:147]\n"
    "ds_read_b128 a[100:103], v201 offset:0\n"
    "v_mfma_f32_16x16x32_bf16 v[148:151], a[8:11], a[104:107], v[148:151]\n"
    "s_add_u32 m0, s61, 122880\n"
    "v_mfma_f32_16x16x32_bf16 v[148:151], a[12:15], a[108:111], v[148:151]\n"
    "s_add_u32 s63, s62, 1966080\n"
    "v_mfma_f32_16x16x32_bf16 v[152:155], a[104:107], a[16:19], v[152:155]\n"
    "buffer_load_dwordx4 v198, s[20:23], s63 offen lds\n"
    "v_mfma_f32_16x16x32_bf16 v[152:155], a[108:111], a[20:23], v[152:155]\n"
    "v_mfma_f32_16x16x32_bf16 v[156:159], a[104:107], a[24:27], v[156:159]\n"
    "v_mfma_f32_16x16x32_bf16 v[156:159], a[108:111], a[28:31], v[156:159]\n"
    "buffer_load_dwordx4 v199, s[20:23], s63 offen offset:1024 lds\n"
    "v_mfma_f32_16x16x32_bf16 v[160:163], a[104:107], a[32:35], v[160:163]\n"
    "v_mfma_f32_16x16x32_bf16 v[160:163], a[108:111], a[36:39], v[160:163]\n"
    "v_mfma_f32_16x16x32_bf16 v[164:167], a[104:107], a[40:43], v[164:167]\n"
    "v_mfma_f32_16x16x32_bf16 v[164:167], a[108:111], a[44:47], v[164:167]\n"
    "v_mfma_f32_16x16x32_bf16 v[168:171], a[0:3], a[112:115], v[168:171]\n"
    "ds_read_b128 a[104:107], v200 offset:2048\n"
    "v_mfma_f32_16x16x32_bf16 v[168:171], a[4:7], a[116:119], v[168:171]\n"
    "ds_read_b128 a[108:111], v201 offset:2048\n"
    "s_add_u32 m0, s59, 38912\n"
    "v_mfma_f32_16x16x32_bf16 v[172:175], a[8:11], a[112:115], v[172:175]\n"
    "s_xor_b32 s63, s60, 98816\n"
    "v_mfma_f32_16x16x32_bf16 v[172:175], a[12:15], a[116:119], v[172:175]\n"
    "buffer_load_dwordx4 v196, s[24:27], s63 offen lds\n"
    "v_mfma_f32_16x16x32_bf16 v[176:179], a[112:115], a[16:19], v[176:179]\n"
    "v_mfma_f32_16x16x32_bf16 v[176:179], a[116:119], a[20:23], v[176:179]\n"
    "v_mfma_f32_16x16x32_bf16 v[180:183], a[112:115], a[24:27], v[180:183]\n"
    "buffer_load_dwordx4 v197, s[24:27], s63 offen offset:1024 lds\n"
    "v_mfma_f32_16x16x32_bf16 v[180:183], a[116:119], a[28:31], v[180:183]\n"
    "s_cmp_eq_u32 s81, 2\n"
    "s_cbranch_scc0 Lpf2%=\n"
    "v_lshl_add_u64 v[244:245], v[246:247], 0, v[244:245]\n"
    "global_load_dword v249, v[244:245], off\n"
    "Lpf2%=:\n"
    "v_mfma_f32_16x16x32_bf16 v[184:187], a[112:115], a[32:35], v[184:187]\n"
    "v_mfma_f32_16x16x32_bf16 v[184:187], a[116:119], a[36:39], v[184:187]\n"
    "v_mfma_f32_16x16x32_bf16 v[188:191], a[112:115], a[40:43], v[188:191]\n"
    "v_mfma_f32_16x16x32_bf16 v[188:191], a[116:119], a[44:47], v[188:191]\n"
    "s_waitcnt lgkmcnt(2)\n"
    "v_mfma_f32_16x16x32_bf16 v[0:3], a[48:51], a[96:99], v[0:3]\n"
    "ds_read_b128 a[112:115], v200 offset:4096\n"
    "v_mfma_f32_16x16x32_bf16 v[0:3], a[52:55], a[100:103], v[0:3]\n"
    "ds_read_b128 a[116:119], v201 offset:4096\n"
    "v_mfma_f32_16x16x32_bf16 v[4:7], a[56:59], a[96:99], v[4:7]\n"
    "s_add_u32 s75, s78, 0\n"
    "v_mfma_f32_16x16x32_bf16 v[4:7], a[60:63], a[100:103], v[4:7]\n"
    "s_and_b32 s75, s75, 0xffff\n"
    "v_mfma_f32_16x16x32_bf16 v[8:11], a[96:99], a[64:67], v[8:11]\n"
    "s_add_u32 s4, s75, s95\n"
    "v_mfma_f32_16x16x32_bf16 v[8:11], a[100:103], a[68:71], v[8:11]\n"
    "s_add_u32 s75, s78, 8192\n"
    "v_mfma_f32_16x16x32_bf16 v[12:15], a[96:99], a[72:75], v[12:15]\n"
    "s_and_b32 s75, s75, 0xffff\n"
    "v_mfma_f32_16x16x32_bf16 v[12:15], a[100:103], a[76:79], v[12:15]\n"
    "s_add_u32 s5, s75, s95\n"
    "v_mfma_f32_16x16x32_bf16 v[16:19], a[96:99], a[80:83], v[16:19]\n"
    "s_add_u32 m0, s61, 131072\n"
    "s_add_u32 s75, s78, 16384\n"
    "v_mfma_f32_16x16x32_bf16 v[16:19], a[100:103], a[84:87], v[16:19]\n"
    "s_add_u32 s63, s62, 1968128\n"
    "s_and_b32 s75, s75, 0xffff\n"
    "v_mfma_f32_16x16x32_bf16 v[20:23], a[96:99], a[88:91], v[20:23]\n"
    "buffer_load_dwordx4 v198, s[20:23], s63 offen lds\n"
    "s_add_u32 s6, s75, s95\n"
    "v_mfma_f32_16x16x32_bf16 v[20:23], a[100:103], a[92:95], v[20:23]\n"
    "s_add_u32 s75, s78, 24576\n"
    "s_waitcnt lgkmcnt(2)\n"
    "v_mfma_f32_16x16x32_bf16 v[24:27], a[48:51], a[104:107], v[24:27]\n"
    "ds_read_b128 a[96:99], v200 offset:6144\n"
    "s_and_b32 s75, s75, 0xffff\n"
    "v_mfma_f32_16x16x32_bf16 v[24:27], a[52:55], a[108:111], v[24:27]\n"
    "ds_read_b128 a[100:103], v201 offset:6144\n"
    "buffer_load_dwordx4 v199, s[20:23], s63 offen offset:1024 lds\n"
    "v_mfma_f32_16x16x32_bf16 v[28:31], a[56:59], a[104:107], v[28:31]\n"
    "s_add_u32 s7, s75, s95\n"
    "v_mfma_f32_16x16x32_bf16 v[28:31], a[60:63], a[108:111], v[28:31]\n"
    "s_add_u32 s75, s78, 32768\n"
    "v_mfma_f32_16x16x32_bf16 v[32:35], a[104:107], a[64:67], v[32:35]\n"
    "s_and_b32 s75, s75, 0xffff\n"
    "v_mfma_f32_16x16x32_bf16 v[32:35], a[108:111], a[68:71], v[32:35]\n"
    "s_add_u32 s8, s75, s95\n"
    "v_mfma_f32_16x16x32_bf16 v[36:39], a[104:107], a[72:75], v[36:39]\n"
    "s_add_u32 s75, s78, 40960\n"
    "v_mfma_f32_16x16x32_bf16 v[36:39], a[108:111], a[76:79], v[36:39]\n"
    "s_and_b32 s75, s75, 0xffff\n"
    "v_mfma_f32_16x16x32_bf16 v[40:43], a[104:107], a[80:83], v[40:43]\n"
    "s_add_u32 s9, s75, s95\n"
    "v_mfma_f32_16x16x32_bf16 v[40:43], a[108:111], a[84:87], v[40:43]\n"
    "s_add_u32 s75, s78, 49152\n"
    "v_mfma_f32_16x16x32_bf16 v[44:47], a[104:107], a[88:91], v[44:47]\n"
    "s_and_b32 s75, s75, 0xffff\n"
    "v_mfma_f32_16x16x32_bf16 v[44:47], a[108:111], a[92:95], v[44:47]\n"
    "s_waitcnt lgkmcnt(2)\n"
    "s_waitcnt vmcnt(10)\n"
    "s_barrier\n"
    "v_mfma_f32_16x16x32_bf16 v[48:51], a[48:51], a[112:115], v[48:51]\n"
    "ds_read_b128 a[104:107], v200 offset:8192\n"
    "ds_read_b64_tr_b16 a[0:1], v204 offset:0\n"
    "v_mfma_f32_16x16x32_bf16 v[48:51], a[52:55], a[116:119], v[48:51]\n"
    "ds_read_b128 a[108:111], v201 offset:8192\n"
    "ds_read_b64_tr_b16 a[2:3], v204 offset:512\n"
    "v_mfma_f32_16x16x32_bf16 v[52:55], a[56:59], a[112:115], v[52:55]\n"
    "s_add_u32 m0, s59, 65536\n"
    "s_add_u32 s10, s75, s95\n"
    "v_mfma_f32_16x16x32_bf16 v[52:55], a[60:63], a[116:119], v[52:55]\n"
    "ds_read_b64_tr_b16 a[4:5], v204 offset:4096\n"
    "s_xor_b32 s63, s60, 640\n"
    "v_mfma_f32_16x16x32_bf16 v[56:59], a[112:115], a[64:67], v[56:59]\n"
    "buffer_load_dwordx4 v196, s[24:27], s63 offen lds\n"
    "s_add_u32 s75, s78, 57344\n"
    "v_mfma_f32_16x16x32_bf16 v[56:59], a[116:119], a[68:71], v[56:59]\n"
    "ds_read_b64_tr_b16 a[6:7], v204 offset:4608\n"
    "s_and_b32 s75, s75, 0xffff\n"
    "v_mfma_f32_16x16x32_bf16 v[60:63], a[112:115], a[72:75], v[60:63]\n"
    "ds_read_b64_tr_b16 a[8:9], v205 offset:0\n"
    "s_add_u32 s11, s75, s95\n"
    "v_mfma_f32_16x16x32_bf16 v[60:63], a[116:119], a[76:79], v[60:63]\n"
    "buffer_load_dwordx4 v197, s[24:27], s63 offen offset:1024 lds\n"
    "s_lshl_b32 s75, s52, 7\n"
    "v_mfma_f32_16x16x32_bf16 v[64:67], a[112:115], a[80:83], v[64:67]\n"
    "ds_read_b64_tr_b16 a[10:11], v205 offset:512\n"
    "s_add_u32 s75, s75, s91\n"
    "v_mfma_f32_16x16x32_bf16 v[64:67], a[116:119], a[84:87], v[64:67]\n"
    "s_xor_b32 s76, s75, 4\n"
    "v_mfma_f32_16x16x32_bf16 v[68:71], a[112:115], a[88:91], v[68:71]\n"
    "ds_read_b64_tr_b16 a[12:13], v205 offset:4096\n"
    "s_lshl_b32 s12, s75, 12\n"
    "v_mfma_f32_16x16x32_bf16 v[68:71], a[116:119], a[92:95], v[68:71]\n"
    "ds_read_b64_tr_b16 a[14:15], v205 offset:4608\n"
    "s_waitcnt lgkmcnt(10)\n"
    "v_mfma_f32_16x16x32_bf16 v[72:75], a[48:51], a[96:99], v[72:75]\n"
    "ds_read_b128 a[112:115], v200 offset:10240\n"
    "s_add_u32 s12, s12, 13631488\n"
    "v_mfma_f32_16x16x32_bf16 v[72:75], a[52:55], a[100:103], v[72:75]\n"
    "ds_read_b128 a[116:119], v201 offset:10240\n"
    "ds_read_b64_tr_b16 a[16:17], v204 offset:8192\n"
    "v_mfma_f32_16x16x32_bf16 v[76:79], a[56:59], a[96:99], v[76:79]\n"
    "s_add_u32 m0, s61, 139264\n"
    "s_lshl_b32 s14, s76, 12\n"
    "v_mfma_f32_16x16x32_bf16 v[76:79], a[60:63], a[100:103], v[76:79]\n"
    "ds_read_b64_tr_b16 a[18:19], v204 offset:8704\n"
    "s_add_u32 s63, s62, 1970176\n"
    "v_mfma_f32_16x16x32_bf16 v[80:83], a[96:99], a[64:67], v[80:83]\n"
    "ds_read_b64_tr_b16 a[20:21], v204 offset:12288\n"
    "buffer_load_dwordx4 v198, s[20:23], s63 offen lds\n"
    "v_mfma_f32_16x16x32_bf16 v[80:83], a[100:103], a[68:71], v[80:83]\n"
    "s_add_u32 s14, s14, 13631488\n"
    "v_mfma_f32_16x16x32_bf16 v[84:87], a[96:99], a[72:75], v[84:87]\n"
    "ds_read_b64_tr_b16 a[22:23], v204 offset:12800\n"
    "s_lshl_b32 s16, s75, 8\n"
    "v_mfma_f32_16x16x32_bf16 v[84:87], a[100:103], a[76:79], v[84:87]\n"
    "buffer_load_dwordx4 v199, s[20:23], s63 offen offset:1024 lds\n"
    "s_add_u32 s16, s16, 12845056\n"
    "v_mfma_f32_16x16x32_bf16 v[88:91], a[96:99], a[80:83], v[88:91]\n"
    "ds_read_b64_tr_b16 a[24:25], v205 offset:8192\n"
    "s_lshl_b32 s17, s76, 8\n"
    "v_mfma_f32_16x16x32_bf16 v[88:91], a[100:103], a[84:87], v[88:91]\n"
    "ds_read_b64_tr_b16 a[26:27], v205 offset:8704\n"
    "s_add_u32 s17, s17, 12845056\n"
    "v_mfma_f32_16x16x32_bf16 v[92:95], a[96:99], a[88:91], v[92:95]\n"
    "v_mfma_f32_16x16x32_bf16 v[92:95], a[100:103], a[92:95], v[92:95]\n"
    "ds_read_b64_tr_b16 a[28:29], v205 offset:12288\n"
    "s_waitcnt lgkmcnt(14)\n"
    "v_mfma_f32_16x16x32_bf16 v[96:99], a[48:51], a[104:107], v[96:99]\n"
    "ds_read_b128 a[96:99], v200 offset:12288\n"
    "v_mfma_f32_16x16x32_bf16 v[96:99], a[52:55], a[108:111], v[96:99]\n"
    "ds_read_b128 a[100:103], v201 offset:12288\n"
    "ds_read_b64_tr_b16 a[30:31], v205 offset:12800\n"
    "v_mfma_f32_16x16x32_bf16 v[100:103], a[56:59], a[104:107], v[100:103]\n"
    "ds_read_b64_tr_b16 a[32:33], v206 offset:16384\n"
    "s_add_u32 m0, s59, 67584\n"
    "v_mfma_f32_16x16x32_bf16 v[100:103], a[60:63], a[108:111], v[100:103]\n"
    "s_xor_b32 s63, s60, 33408\n"
    "v_mfma_f32_16x16x32_bf16 v[104:107], a[104:107], a[64:67], v[104:107]\n"
    "ds_read_b64_tr_b16 a[34:35], v206 offset:16896\n"
    "buffer_load_dwordx4 v196, s[24:27], s63 offen lds\n"
    "v_mfma_f32_16x16x32_bf16 v[104:107], a[108:111], a[68:71], v[104:107]\n"
    "v_mfma_f32_16x16x32_bf16 v[108:111], a[104:107], a[72:75], v[108:111]\n"
    "ds_read_b64_tr_b16 a[36:37], v206 offset:20480\n"
    "v_mfma_f32_16x16x32_bf16 v[108:111], a[108:111], a[76:79], v[108:111]\n"
    "ds_read_b64_tr_b16 a[38:39], v206 offset:20992\n"
    "buffer_load_dwordx4 v197, s[24:27], s63 offen offset:1024 lds\n"
    "v_mfma_f32_16x16x32_bf16 v[112:115], a[104:107], a[80:83], v[112:115]\n"
    "v_mfma_f32_16x16x32_bf16 v[112:115], a[108:111], a[84:87], v[112:115]\n"
    "ds_read_b64_tr_b16 a[40:41], v207 offset:16384\n"
    "v_mfma_f32_16x16x32_bf16 v[116:119], a[104:107], a[88:91], v[116:119]\n"
    "v_mfma_f32_16x16x32_bf16 v[116:119], a[108:111], a[92:95], v[116:119]\n"
    "ds_read_b64_tr_b16 a[42:43], v207 offset:16896\n"
    "s_waitcnt lgkmcnt(14)\n"
    "v_mfma_f32_16x16x32_bf16 v[120:123], a[48:51], a[112:115], v[120:123]\n"
    "ds_read_b128 a[104:107], v200 offset:14336\n"
    "ds_read_b64_tr_b16 a[44:45], v207 offset:20480\n"
    "v_mfma_f32_16x16x32_bf16 v[120:123], a[52:55], a[116:119], v[120:123]\n"
    "ds_read_b128 a[108:111], v201 offset:14336\n"
    "s_add_u32 m0, s59, 69632\n"
    "v_mfma_f32_16x16x32_bf16 v[124:127], a[56:59], a[112:115], v[124:127]\n"
    "ds_read_b64_tr_b16 a[46:47], v207 offset:20992\n"
    "s_xor_b32 s63, s60, 66176\n"
    "v_mfma_f32_16x16x32_bf16 v[124:127], a[60:63], a[116:119], v[124:127]\n"
    "buffer_load_dwordx4 v196, s[24:27], s63 offen lds\n"
    "v_mfma_f32_16x16x32_bf16 v[128:131], a[112:115], a[64:67], v[128:131]\n"
    "v_mfma_f32_16x16x32_bf16 v[128:131], a[116:119], a[68:71], v[128:131]\n"
    "buffer_load_dwordx4 v197, s[24:27], s63 offen offset:1024 lds\n"
    "v_mfma_f32_16x16x32_bf16 v[132:135], a[112:115], a[72:75], v[132:135]\n"
    "v_mfma_f32_16x16x32_bf16 v[132:135], a[116:119], a[76:79], v[132:135]\n"
    "v_mfma_f32_16x16x32_bf16 v[136:139], a[112:115], a[80:83], v[136:139]\n"
    "v_mfma_f32_16x16x32_bf16 v[136:139], a[116:119], a[84:87], v[136:139]\n"
    "v_mfma_f32_16x16x32_bf16 v[140:143], a[112:115], a[88:91], v[140:143]\n"
    "v_mfma_f32_16x16x32_bf16 v[140:143], a[116:119], a[92:95], v[140:143]\n"
    "s_waitcnt vmcnt(10) lgkmcnt(0)\n"
    "s_barrier\n"
    "v_mfma_f32_16x16x32_bf16 v[144:147], a[48:51], a[96:99], v[144:147]\n"
    "ds_read_b128 a[112:115], v200 offset:32768\n"
    "v_mfma_f32_16x16x32_bf16 v[144:147], a[52:55], a[100:103], v[144:147]\n"
    "ds_read_b128 a[116:119], v201 offset:32768\n"
    "v_mfma_f32_16x16x32_bf16 v[148:151], a[56:59], a[96:99], v[148:151]\n"
    "s_add_u32 m0, s61, 98304\n"
    "v_mfma_f32_16x16x32_bf16 v[148:151], a[60:63], a[100:103], v[148:151]\n"
    "s_add_u32 s63, s62, 2359296\n"
    "v_mfma_f32_16x16x32_bf16 v[152:155], a[96:99], a[64:67], v[152:155]\n"
    "buffer_load_dwordx4 v198, s[20:23], s63 offen lds\n"
    "v_mfma_f32_16x16x32_bf16 v[152:155], a[100:103], a[68:71], v[152:155]\n"
    "v_mfma_f32_16x16x32_bf16 v[156:159], a[96:99], a[72:75], v[156:159]\n"
    "v_mfma_f32_16x16x32_bf16 v[156:159], a[100:103], a[76:79], v[156:159]\n"
    "buffer_load_dwordx4 v199, s[20:23], s63 offen offset:1024 lds\n"
    "v_mfma_f32_16x16x32_bf16 v[160:163], a[96:99], a[80:83], v[160:163]\n"
    "v_mfma_f32_16x16x32_bf16 v[160:163], a[100:103], a[84:87], v[160:163]\n"
    "v_mfma_f32_16x16x32_bf16 v[164:167], a[96:99], a[88:91], v[164:167]\n"
    "v_mfma_f32_16x16x32_bf16 v[164:167], a[100:103], a[92:95], v[164:167]\n"
    "v_mfma_f32_16x16x32_bf16 v[168:171], a[48:51], a[104:107], v[168:171]\n"
    "ds_read_b128 a[96:99], v200 offset:34816\n"
    "v_mfma_f32_16x16x32_bf16 v[168:171], a[52:55], a[108:111], v[168:171]\n"
    "ds_read_b128 a[100:103], v201 offset:34816\n"
    "s_add_u32 m0, s59, 71680\n"
    "v_mfma_f32_16x16x32_bf16 v[172:175], a[56:59], a[104:107], v[172:175]\n"
    "s_xor_b32 s63, s60, 98944\n"
    "v_mfma_f32_16x16x32_bf16 v[172:175], a[60:63], a[108:111], v[172:175]\n"
    "buffer_load_dwordx4 v196, s[24:27], s63 offen lds\n"
    "v_mfma_f32_16x16x32_bf16 v[176:179], a[104:107], a[64:67], v[176:179]\n"
    "v_mfma_f32_16x16x32_bf16 v[176:179], a[108:111], a[68:71], v[176:179]\n"
    "v_mfma_f32_16x16x32_bf16 v[180:183], a[104:107], a[72:75], v[180:183]\n"
    "buffer_load_dwordx4 v197, s[24:27], s63 offen offset:1024 lds\n"
    "v_mfma_f32_16x16x32_bf16 v[180:183], a[108:111], a[76:79], v[180:183]\n"
    "s_cmp_eq_u32 s81, 3\n"
    "s_cbranch_scc0 Lpf3%=\n"
    "v_lshl_add_u64 v[244:245], v[246:247], 0, v[244:245]\n"
    "global_load_dword v249, v[244:245], off\n"
    "Lpf3%=:\n"
    "v_mfma_f32_16x16x32_bf16 v[184:187], a[104:107], a[80:83], v[184:187]\n"
    "v_mfma_f32_16x16x32_bf16 v[184:187], a[108:111], a[84:87], v[184:187]\n"
    "v_mfma_f32_16x16x32_bf16 v[188:191], a[104:107], a[88:91], v[188:191]\n"
    "v_mfma_f32_16x16x32_bf16 v[188:191], a[108:111], a[92:95], v[188:191]\n"
    "s_waitcnt lgkmcnt(2)\n"
    "v_mfma_f32_16x16x32_bf16 v[0:3], a[0:3], a[112:115], v[0:3]\n"
    "ds_read_b128 a[104:107], v200 offset:36864\n"
    "v_mfma_f32_16x16x32_bf16 v[0:3], a[4:7], a[116:119], v[0:3]\n"
    "ds_read_b128 a[108:111], v201 offset:36864\n"
    "v_mfma_f32_16x16x32_bf16 v[4:7], a[8:11], a[112:115], v[4:7]\n"
    "s_lshl_b32 s75, s50, 3\n"
    "v_mfma_f32_16x16x32_bf16 v[4:7], a[12:15], a[116:119], v[4:7]\n"
    "s_lshl_b32 s76, s48, 1\n"
    "v_mfma_f32_16x16x32_bf16 v[8:11], a[112:115], a[16:19], v[8:11]\n"
    "s_add_u32 s75, s75, s76\n"
    "v_mfma_f32_16x16x32_bf16 v[8:11], a[116:119], a[20:23], v[8:11]\n"
    "s_lshl_b32 s76, s49, 8\n"
    "v_mfma_f32_16x16x32_bf16 v[12:15], a[112:115], a[24:27], v[12:15]\n"
    "s_add_u32 s76, s76, s75\n"
    "v_mfma_f32_16x16x32_bf16 v[12:15], a[116:119], a[28:31], v[12:15]\n"
    "s_lshl_b32 s72, s76, 11\n"
    "v_mfma_f32_16x16x32_bf16 v[16:19], a[112:115], a[32:35], v[16:19]\n"
    "s_add_u32 m0, s61, 106496\n"
    "s_lshr_b32 s76, s75, 7\n"
    "v_mfma_f32_16x16x32_bf16 v[16:19], a[116:119], a[36:39], v[16:19]\n"
    "s_add_u32 s63, s62, 2361344\n"
    "s_lshl_b32 s94, s49, 1\n"
    "v_mfma_f32_16x16x32_bf16 v[20:23], a[112:115], a[40:43], v[20:23]\n"
    "buffer_load_dwordx4 v198, s[20:23], s63 offen lds\n"
    "s_add_u32 s94, s94, s76\n"
    "v_mfma_f32_16x16x32_bf16 v[20:23], a[116:119], a[44:47], v[20:23]\n"
    "s_lshl_b32 s94, s94, 4\n"
    "s_waitcnt lgkmcnt(2)\n"
    "v_mfma_f32_16x16x32_bf16 v[24:27], a[0:3], a[96:99], v[24:27]\n"
    "ds_read_b128 a[112:115], v200 offset:38912\n"
    "s_and_b32 s76, s75, 15\n"
    "v_mfma_f32_16x16x32_bf16 v[24:27], a[4:7], a[100:103], v[24:27]\n"
    "ds_read_b128 a[116:119], v201 offset:38912\n"
    "buffer_load_dwordx4 v199, s[20:23], s63 offen offset:1024 lds\n"
    "v_mfma_f32_16x16x32_bf16 v[28:31], a[8:11], a[96:99], v[28:31]\n"
    "s_add_u32 s94, s94, s76\n"
    "v_mfma_f32_16x16x32_bf16 v[28:31], a[12:15], a[100:103], v[28:31]\n"
    "s_lshl_b32 s94, s94, 3\n"
    "v_mfma_f32_16x16x32_bf16 v[32:35], a[96:99], a[16:19], v[32:35]\n"
    "s_bfe_u32 s76, s75, 0x30004\n"
    "v_mfma_f32_16x16x32_bf16 v[32:35], a[100:103], a[20:23], v[32:35]\n"
    "s_add_u32 s94, s94, s76\n"
    "v_mfma_f32_16x16x32_bf16 v[36:39], a[96:99], a[24:27], v[36:39]\n"
    "s_lshl_b32 s94, s94, 3\n"
    "v_mfma_f32_16x16x32_bf16 v[36:39], a[100:103], a[28:31], v[36:39]\n"
    "s_add_u32 s94, s94, 32768\n"
    "v_mfma_f32_16x16x32_bf16 v[40:43], a[96:99], a[32:35], v[40:43]\n"
    "v_mfma_f32_16x16x32_bf16 v[40:43], a[100:103], a[36:39], v[40:43]\n"
    "v_mfma_f32_16x16x32_bf16 v[44:47], a[96:99], a[40:43], v[44:47]\n"
    "v_mfma_f32_16x16x32_bf16 v[44:47], a[100:103], a[44:47], v[44:47]\n"
    "s_waitcnt lgkmcnt(2)\n"
    "s_waitcnt vmcnt(10)\n"
    "s_barrier\n"
    "v_mfma_f32_16x16x32_bf16 v[48:51], a[0:3], a[104:107], v[48:51]\n"
    "ds_read_b128 a[96:99], v200 offset:40960\n"
    "ds_read_b64_tr_b16 a[48:49], v204 offset:24576\n"
    "v_mfma_f32_16x16x32_bf16 v[48:51], a[4:7], a[108:111], v[48:51]\n"
    "ds_read_b128 a[100:103], v201 offset:40960\n"
    "ds_read_b64_tr_b16 a[50:51], v204 offset:25088\n"
    "v_mfma_f32_16x16x32_bf16 v[52:55], a[8:11], a[104:107], v[52:55]\n"
    "s_add_u32 m0, s59, 0\n"
    "v_mfma_f32_16x16x32_bf16 v[52:55], a[12:15], a[108:111], v[52:55]\n"
    "ds_read_b64_tr_b16 a[52:53], v204 offset:28672\n"
    "s_xor_b32 s63, s60, 768\n"
    "v_mfma_f32_16x16x32_bf16 v[56:59], a[104:107], a[16:19], v[56:59]\n"
    "buffer_load_dwordx4 v196, s[24:27], s63 offen lds\n"
    "v_mfma_f32_16x16x32_bf16 v[56:59], a[108:111], a[20:23], v[56:59]\n"
    "ds_read_b64_tr_b16 a[54:55], v204 offset:29184\n"
    "v_mfma_f32_16x16x32_bf16 v[60:63], a[104:107], a[24:27], v[60:63]\n"
    "ds_read_b64_tr_b16 a[56:57], v205 offset:24576\n"
    "v_mfma_f32_16x16x32_bf16 v[60:63], a[108:111], a[28:31], v[60:63]\n"
    "buffer_load_dwordx4 v197, s[24:27], s63 offen offset:1024 lds\n"
    "v_mfma_f32_16x16x32_bf16 v[64:67], a[104:107], a[32:35], v[64:67]\n"
    "ds_read_b64_tr_b16 a[58:59], v205 offset:25088\n"
    "v_mfma_f32_16x16x32_bf16 v[64:67], a[108:111], a[36:39], v[64:67]\n"
    "v_mfma_f32_16x16x32_bf16 v[68:71], a[104:107], a[40:43], v[68:71]\n"
    "ds_read_b64_tr_b16 a[60:61], v205 offset:28672\n"
    "v_mfma_f32_16x16x32_bf16 v[68:71], a[108:111], a[44:47], v[68:71]\n"
    "ds_read_b64_tr_b16 a[62:63], v205 offset:29184\n"
    "s_waitcnt lgkmcnt(10)\n"
    "v_mfma_f32_16x16x32_bf16 v[72:75], a[0:3], a[112:115], v[72:75]\n"
    "ds_read_b128 a[104:107], v200 offset:43008\n"
    "v_mfma_f32_16x16x32_bf16 v[72:75], a[4:7], a[116:119], v[72:75]\n"
    "ds_read_b128 a[108:111], v201 offset:43008\n"
    "ds_read_b64_tr_b16 a[64:65], v204 offset:32768\n"
    "v_mfma_f32_16x16x32_bf16 v[76:79], a[8:11], a[112:115], v[76:79]\n"
    "s_add_u32 m0, s61, 114688\n"
    "v_mfma_f32_16x16x32_bf16 v[76:79], a[12:15], a[116:119], v[76:79]\n"
    "ds_read_b64_tr_b16 a[66:67], v204 offset:33280\n"
    "s_add_u32 s63, s62, 2363392\n"
    "v_mfma_f32_16x16x32_bf16 v[80:83], a[112:115], a[16:19], v[80:83]\n"
    "ds_read_b64_tr_b16 a[68:69], v204 offset:36864\n"
    "buffer_load_dwordx4 v198, s[20:23], s63 offen lds\n"
    "v_mfma_f32_16x16x32_bf16 v[80:83], a[116:119], a[20:23], v[80:83]\n"
    "v_mfma_f32_16x16x32_bf16 v[84:87], a[112:115], a[24:27], v[84:87]\n"
    "ds_read_b64_tr_b16 a[70:71], v204 offset:37376\n"
    "v_mfma_f32_16x16x32_bf16 v[84:87], a[116:119], a[28:31], v[84:87]\n"
    "buffer_load_dwordx4 v199, s[20:23], s63 offen offset:1024 lds\n"
    "v_mfma_f32_16x16x32_bf16 v[88:91], a[112:115], a[32:35], v[88:91]\n"
    "ds_read_b64_tr_b16 a[72:73], v205 offset:32768\n"
    "v_mfma_f32_16x16x32_bf16 v[88:91], a[116:119], a[36:39], v[88:91]\n"
    "ds_read_b64_tr_b16 a[74:75], v205 offset:33280\n"
    "v_mfma_f32_16x16x32_bf16 v[92:95], a[112:115], a[40:43], v[92:95]\n"
    "v_mfma_f32_16x16x32_bf16 v[92:95], a[116:119], a[44:47], v[92:95]\n"
    "ds_read_b64_tr_b16 a[76:77], v205 offset:36864\n"
    "s_waitcnt lgkmcnt(14)\n"
    "v_mfma_f32_16x16x32_bf16 v[96:99], a[0:3], a[96:99], v[96:99]\n"
    "ds_read_b128 a[112:115], v200 offset:45056\n"
    "v_mfma_f32_16x16x32_bf16 v[96:99], a[4:7], a[100:103], v[96:99]\n"
    "ds_read_b128 a[116:119], v201 offset:45056\n"
    "ds_read_b64_tr_b16 a[78:79], v205 offset:37376\n"
    "v_mfma_f32_16x16x32_bf16 v[100:103], a[8:11], a[96:99], v[100:103]\n"
    "ds_read_b64_tr_b16 a[80:81], v206 offset:40960\n"
    "s_add_u32 m0, s59, 2048\n"
    "v_mfma_f32_16x16x32_bf16 v[100:103], a[12:15], a[100:103], v[100:103]\n"
    "s_xor_b32 s63, s60, 33536\n"
    "v_mfma_f32_16x16x32_bf16 v[104:107], a[96:99], a[16:19], v[104:107]\n"
    "ds_read_b64_tr_b16 a[82:83], v206 offset:41472\n"
    "buffer_load_dwordx4 v196, s[24:27], s63 offen lds\n"
    "v_mfma_f32_16x16x32_bf16 v[104:107], a[100:103], a[20:23], v[104:107]\n"
    "v_mfma_f32_16x16x32_bf16 v[108:111], a[96:99], a[24:27], v[108:111]\n"
    "ds_read_b64_tr_b16 a[84:85], v206 offset:45056\n"
    "v_mfma_f32_16x16x32_bf16 v[108:111], a[100:103], a[28:31], v[108:111]\n"
    "ds_read_b64_tr_b16 a[86:87], v206 offset:45568\n"
    "buffer_load_dwordx4 v197, s[24:27], s63 offen offset:1024 lds\n"
    "v_mfma_f32_16x16x32_bf16 v[112:115], a[96:99], a[32:35], v[112:115]\n"
    "v_mfma_f32_16x16x32_bf16 v[112:115], a[100:103], a[36:39], v[112:115]\n"
    "ds_read_b64_tr_b16 a[88:89], v207 offset:40960\n"
    "v_mfma_f32_16x16x32_bf16 v[116:119], a[96:99], a[40:43], v[116:119]\n"
    "v_mfma_f32_16x16x32_bf16 v[116:119], a[100:103], a[44:47], v[116:119]\n"
    "ds_read_b64_tr_b16 a[90:91], v207 offset:41472\n"
    "s_waitcnt lgkmcnt(14)\n"
    "v_mfma_f32_16x16x32_bf16 v[120:123], a[0:3], a[104:107], v[120:123]\n"
    "ds_read_b128 a[96:99], v200 offset:47104\n"
    "ds_read_b64_tr_b16 a[92:93], v207 offset:45056\n"
    "v_mfma_f32_16x16x32_bf16 v[120:123], a[4:7], a[108:111], v[120:123]\n"
    "ds_read_b128 a[100:103], v201 offset:47104\n"
    "s_add_u32 m0, s59, 4096\n"
    "v_mfma_f32_16x16x32_bf16 v[124:127], a[8:11], a[104:107], v[124:127]\n"
    "ds_read_b64_tr_b16 a[94:95], v207 offset:45568\n"
    "s_xor_b32 s63, s60, 66304\n"
    "v_mfma_f32_16x16x32_bf16 v[124:127], a[12:15], a[108:111], v[124:127]\n"
    "buffer_load_dwordx4 v196, s[24:27], s63 offen lds\n"
    "v_mfma_f32_16x16x32_bf16 v[128:131], a[104:107], a[16:19], v[128:131]\n"
    "v_mfma_f32_16x16x32_bf16 v[128:131], a[108:111], a[20:23], v[128:131]\n"
    "buffer_load_dwordx4 v197, s[24:27], s63 offen offset:1024 lds\n"
    "v_mfma_f32_16x16x32_bf16 v[132:135], a[104:107], a[24:27], v[132:135]\n"
    "v_mfma_f32_16x16x32_bf16 v[132:135], a[108:111], a[28:31], v[132:135]\n"
    "v_mfma_f32_16x16x32_bf16 v[136:139], a[104:107], a[32:35], v[136:139]\n"
    "v_mfma_f32_16x16x32_bf16 v[136:139], a[108:111], a[36:39], v[136:139]\n"
    "v_mfma_f32_16x16x32_bf16 v[140:143], a[104:107], a[40:43], v[140:143]\n"
    "v_mfma_f32_16x16x32_bf16 v[140:143], a[108:111], a[44:47], v[140:143]\n"
    "s_waitcnt vmcnt(10) lgkmcnt(0)\n"
    "s_barrier\n"
    "v_mfma_f32_16x16x32_bf16 v[144:147], a[0:3], a[112:115], v[144:147]\n"
    "ds_read_b128 a[104:107], v202 offset:0\n"
    "v_mfma_f32_16x16x32_bf16 v[144:147], a[4:7], a[116:119], v[144:147]\n"
    "ds_read_b128 a[108:111], v203 offset:0\n"
    "v_mfma_f32_16x16x32_bf16 v[148:151], a[8:11], a[112:115], v[148:151]\n"
    "s_add_u32 m0, s61, 122880\n"
    "v_mfma_f32_16x16x32_bf16 v[148:151], a[12:15], a[116:119], v[148:151]\n"
    "s_add_u32 s63, s62, 2752512\n"
    "v_mfma_f32_16x16x32_bf16 v[152:155], a[112:115], a[16:19], v[152:155]\n"
    "buffer_load_dwordx4 v198, s[20:23], s63 offen lds\n"
    "v_mfma_f32_16x16x32_bf16 v[152:155], a[116:119], a[20:23], v[152:155]\n"
    "v_mfma_f32_16x16x32_bf16 v[156:159], a[112:115], a[24:27], v[156:159]\n"
    "v_mfma_f32_16x16x32_bf16 v[156:159], a[116:119], a[28:31], v[156:159]\n"
    "buffer_load_dwordx4 v199, s[20:23], s63 offen offset:1024 lds\n"
    "v_mfma_f32_16x16x32_bf16 v[160:163], a[112:115], a[32:35], v[160:163]\n"
    "v_mfma_f32_16x16x32_bf16 v[160:163], a[116:119], a[36:39], v[160:163]\n"
    "v_mfma_f32_16x16x32_bf16 v[164:167], a[112:115], a[40:43], v[164:167]\n"
    "v_mfma_f32_16x16x32_bf16 v[164:167], a[116:119], a[44:47], v[164:167]\n"
    "v_mfma_f32_16x16x32_bf16 v[168:171], a[0:3], a[96:99], v[168:171]\n"
    "ds_read_b128 a[112:115], v202 offset:2048\n"
    "v_mfma_f32_16x16x32_bf16 v[168:171], a[4:7], a[100:103], v[168:171]\n"
    "ds_read_b128 a[116:119], v203 offset:2048\n"
    "s_add_u32 m0, s59, 6144\n"
    "v_mfma_f32_16x16x32_bf16 v[172:175], a[8:11], a[96:99], v[172:175]\n"
    "s_xor_b32 s63, s60, 99072\n"
    "v_mfma_f32_16x16x32_bf16 v[172:175], a[12:15], a[100:103], v[172:175]\n"
    "buffer_load_dwordx4 v196, s[24:27], s63 offen lds\n"
    "v_mfma_f32_16x16x32_bf16 v[176:179], a[96:99], a[16:19], v[176:179]\n"
    "v_mfma_f32_16x16x32_bf16 v[176:179], a[100:103], a[20:23], v[176:179]\n"
    "v_mfma_f32_16x16x32_bf16 v[180:183], a[96:99], a[24:27], v[180:183]\n"
    "buffer_load_dwordx4 v197, s[24:27], s63 offen offset:1024 lds\n"
    "v_mfma_f32_16x16x32_bf16 v[180:183], a[100:103], a[28:31], v[180:183]\n"
    "s_cmp_eq_u32 s81, 0\n"
    "s_cbranch_scc0 Lpf4%=\n"
    "v_lshl_add_u64 v[244:245], v[246:247], 0, v[244:245]\n"
    "global_load_dword v249, v[244:245], off\n"
    "Lpf4%=:\n"
    "v_mfma_f32_16x16x32_bf16 v[184:187], a[96:99], a[32:35], v[184:187]\n"
    "v_mfma_f32_16x16x32_bf16 v[184:187], a[100:103], a[36:39], v[184:187]\n"
    "v_mfma_f32_16x16x32_bf16 v[188:191], a[96:99], a[40:43], v[188:191]\n"
    "v_mfma_f32_16x16x32_bf16 v[188:191], a[100:103], a[44:47], v[188:191]\n"
    "s_waitcnt lgkmcnt(2)\n"
    "v_mfma_f32_16x16x32_bf16 v[0:3], a[48:51], a[104:107], v[0:3]\n"
    "ds_read_b128 a[96:99], v202 offset:4096\n"
    "s_add_u32 s63, s65, 0\n"
    "v_mfma_f32_16x16x32_bf16 v[0:3], a[52:55], a[108:111], v[0:3]\n"
    "ds_read_b128 a[100:103], v203 offset:4096\n"
    "s_and_b32 s63, s63, 0x7ffff\n"
    "v_mfma_f32_16x16x32_bf16 v[4:7], a[56:59], a[104:107], v[4:7]\n"
    "s_add_u32 m0, s64, 147456\n"
    "v_mfma_f32_16x16x32_bf16 v[4:7], a[60:63], a[108:111], v[4:7]\n"
    "buffer_load_dwordx4 v208, s[32:35], s63 offen offset:0 lds\n"
    "v_mfma_f32_16x16x32_bf16 v[8:11], a[104:107], a[64:67], v[8:11]\n"
    "buffer_load_dwordx4 v209, s[32:35], s63 offen offset:1024 lds\n"
    "v_mfma_f32_16x16x32_bf16 v[8:11], a[108:111], a[68:71], v[8:11]\n"
    "v_mfma_f32_16x16x32_bf16 v[12:15], a[104:107], a[72:75], v[12:15]\n"
    "v_mfma_f32_16x16x32_bf16 v[12:15], a[108:111], a[76:79], v[12:15]\n"
    "v_mfma_f32_16x16x32_bf16 v[16:19], a[104:107], a[80:83], v[16:19]\n"
    "s_add_u32 m0, s61, 131072\n"
    "v_mfma_f32_16x16x32_bf16 v[16:19], a[108:111], a[84:87], v[16:19]\n"
    "s_add_u32 s63, s62, 2754560\n"
    "v_mfma_f32_16x16x32_bf16 v[20:23], a[104:107], a[88:91], v[20:23]\n"
    "buffer_load_dwordx4 v198, s[20:23], s63 offen lds\n"
    "v_mfma_f32_16x16x32_bf16 v[20:23], a[108:111], a[92:95], v[20:23]\n"
    "s_waitcnt lgkmcnt(2)\n"
    "v_mfma_f32_16x16x32_bf16 v[24:27], a[48:51], a[112:115], v[24:27]\n"
    "ds_read_b128 a[104:107], v202 offset:6144\n"
    "v_mfma_f32_16x16x32_bf16 v[24:27], a[52:55], a[116:119], v[24:27]\n"
    "ds_read_b128 a[108:111], v203 offset:6144\n"
    "buffer_load_dwordx4 v199, s[20:23], s63 offen offset:1024 lds\n"
    "v_mfma_f32_16x16x32_bf16 v[28:31], a[56:59], a[112:115], v[28:31]\n"
    "v_mfma_f32_16x16x32_bf16 v[28:31], a[60:63], a[116:119], v[28:31]\n"
    "v_mfma_f32_16x16x32_bf16 v[32:35], a[112:115], a[64:67], v[32:35]\n"
    "s_add_u32 s63, s65, 0\n"
    "v_mfma_f32_16x16x32_bf16 v[32:35], a[116:119], a[68:71], v[32:35]\n"
    "s_and_b32 s63, s63, 0x7ffff\n"
    "v_mfma_f32_16x16x32_bf16 v[36:39], a[112:115], a[72:75], v[36:39]\n"
    "s_add_u32 m0, s64, 147456\n"
    "v_mfma_f32_16x16x32_bf16 v[36:39], a[116:119], a[76:79], v[36:39]\n"
    "buffer_load_dwordx4 v210, s[32:35], s63 offen offset:2048 lds\n"
    "v_mfma_f32_16x16x32_bf16 v[40:43], a[112:115], a[80:83], v[40:43]\n"
    "buffer_load_dwordx4 v211, s[32:35], s63 offen offset:3072 lds\n"
    "v_mfma_f32_16x16x32_bf16 v[40:43], a[116:119], a[84:87], v[40:43]\n"
    "v_mfma_f32_16x16x32_bf16 v[44:47], a[112:115], a[88:91], v[44:47]\n"
    "v_mfma_f32_16x16x32_bf16 v[44:47], a[116:119], a[92:95], v[44:47]\n"
    "s_waitcnt lgkmcnt(2)\n"
    "s_waitcnt vmcnt(14)\n"
    "s_barrier\n"
    "v_mfma_f32_16x16x32_bf16 v[48:51], a[48:51], a[96:99], v[48:51]\n"
    "ds_read_b128 a[112:115], v202 offset:8192\n"
    "ds_read_b64_tr_b16 a[0:1], v204 offset:0\n"
    "v_mfma_f32_16x16x32_bf16 v[48:51], a[52:55], a[100:103], v[48:51]\n"
    "ds_read_b128 a[116:119], v203 offset:8192\n"
    "ds_read_b64_tr_b16 a[2:3], v204 offset:512\n"
    "v_mfma_f32_16x16x32_bf16 v[52:55], a[56:59], a[96:99], v[52:55]\n"
    "s_add_u32 m0, s59, 32768\n"
    "v_mfma_f32_16x16x32_bf16 v[52:55], a[60:63], a[100:103], v[52:55]\n"
    "ds_read_b64_tr_b16 a[4:5], v204 offset:4096\n"
    "s_xor_b32 s63, s60, 896\n"
    "v_mfma_f32_16x16x32_bf16 v[56:59], a[96:99], a[64:67], v[56:59]\n"
    "buffer_load_dwordx4 v196, s[24:27], s63 offen lds\n"
    "v_mfma_f32_16x16x32_bf16 v[56:59], a[100:103], a[68:71], v[56:59]\n"
    "ds_read_b64_tr_b16 a[6:7], v204 offset:4608\n"
    "v_mfma_f32_16x16x32_bf16 v[60:63], a[96:99], a[72:75], v[60:63]\n"
    "ds_read_b64_tr_b16 a[8:9], v205 offset:0\n"
    "v_mfma_f32_16x16x32_bf16 v[60:63], a[100:103], a[76:79], v[60:63]\n"
    "buffer_load_dwordx4 v197, s[24:27], s63 offen offset:1024 lds\n"
    "v_mfma_f32_16x16x32_bf16 v[64:67], a[96:99], a[80:83], v[64:67]\n"
    "ds_read_b64_tr_b16 a[10:11], v205 offset:512\n"
    "v_mfma_f32_16x16x32_bf16 v[64:67], a[100:103], a[84:87], v[64:67]\n"
    "v_mfma_f32_16x16x32_bf16 v[68:71], a[96:99], a[88:91], v[68:71]\n"
    "ds_read_b64_tr_b16 a[12:13], v205 offset:4096\n"
    "v_mfma_f32_16x16x32_bf16 v[68:71], a[100:103], a[92:95], v[68:71]\n"
    "ds_read_b64_tr_b16 a[14:15], v205 offset:4608\n"
    "s_waitcnt lgkmcnt(10)\n"
    "v_mfma_f32_16x16x32_bf16 v[72:75], a[48:51], a[104:107], v[72:75]\n"
    "ds_read_b128 a[96:99], v202 offset:10240\n"
    "v_mfma_f32_16x16x32_bf16 v[72:75], a[52:55], a[108:111], v[72:75]\n"
    "ds_read_b128 a[100:103], v203 offset:10240\n"
    "ds_read_b64_tr_b16 a[16:17], v204 offset:8192\n"
    "v_mfma_f32_16x16x32_bf16 v[76:79], a[56:59], a[104:107], v[76:79]\n"
    "s_add_u32 m0, s61, 139264\n"
    "v_mfma_f32_16x16x32_bf16 v[76:79], a[60:63], a[108:111], v[76:79]\n"
    "ds_read_b64_tr_b16 a[18:19], v204 offset:8704\n"
    "s_add_u32 s63, s62, 2756608\n"
    "v_mfma_f32_16x16x32_bf16 v[80:83], a[104:107], a[64:67], v[80:83]\n"
    "ds_read_b64_tr_b16 a[20:21], v204 offset:12288\n"
    "buffer_load_dwordx4 v198, s[20:23], s63 offen lds\n"
    "v_mfma_f32_16x16x32_bf16 v[80:83], a[108:111], a[68:71], v[80:83]\n"
    "v_mfma_f32_16x16x32_bf16 v[84:87], a[104:107], a[72:75], v[84:87]\n"
    "ds_read_b64_tr_b16 a[22:23], v204 offset:12800\n"
    "v_mfma_f32_16x16x32_bf16 v[84:87], a[108:111], a[76:79], v[84:87]\n"
    "buffer_load_dwordx4 v199, s[20:23], s63 offen offset:1024 lds\n"
    "v_mfma_f32_16x16x32_bf16 v[88:91], a[104:107], a[80:83], v[88:91]\n"
    "ds_read_b64_tr_b16 a[24:25], v205 offset:8192\n"
    "v_mfma_f32_16x16x32_bf16 v[88:91], a[108:111], a[84:87], v[88:91]\n"
    "ds_read_b64_tr_b16 a[26:27], v205 offset:8704\n"
    "v_mfma_f32_16x16x32_bf16 v[92:95], a[104:107], a[88:91], v[92:95]\n"
    "v_mfma_f32_16x16x32_bf16 v[92:95], a[108:111], a[92:95], v[92:95]\n"
    "ds_read_b64_tr_b16 a[28:29], v205 offset:12288\n"
    "s_waitcnt lgkmcnt(14)\n"
    "v_mfma_f32_16x16x32_bf16 v[96:99], a[48:51], a[112:115], v[96:99]\n"
    "ds_read_b128 a[104:107], v202 offset:12288\n"
    "v_mfma_f32_16x16x32_bf16 v[96:99], a[52:55], a[116:119], v[96:99]\n"
    "ds_read_b128 a[108:111], v203 offset:12288\n"
    "ds_read_b64_tr_b16 a[30:31], v205 offset:12800\n"
    "v_mfma_f32_16x16x32_bf16 v[100:103], a[56:59], a[112:115], v[100:103]\n"
    "ds_read_b64_tr_b16 a[32:33], v206 offset:16384\n"
    "s_add_u32 m0, s59, 34816\n"
    "v_mfma_f32_16x16x32_bf16 v[100:103], a[60:63], a[116:119], v[100:103]\n"
    "s_xor_b32 s63, s60, 33664\n"
    "v_mfma_f32_16x16x32_bf16 v[104:107], a[112:115], a[64:67], v[104:107]\n"
    "ds_read_b64_tr_b16 a[34:35], v206 offset:16896\n"
    "buffer_load_dwordx4 v196, s[24:27], s63 offen lds\n"
    "v_mfma_f32_16x16x32_bf16 v[104:107], a[116:119], a[68:71], v[104:107]\n"
    "v_mfma_f32_16x16x32_bf16 v[108:111], a[112:115], a[72:75], v[108:111]\n"
    "ds_read_b64_tr_b16 a[36:37], v206 offset:20480\n"
    "v_mfma_f32_16x16x32_bf16 v[108:111], a[116:119], a[76:79], v[108:111]\n"
    "ds_read_b64_tr_b16 a[38:39], v206 offset:20992\n"
    "buffer_load_dwordx4 v197, s[24:27], s63 offen offset:1024 lds\n"
    "v_mfma_f32_16x16x32_bf16 v[112:115], a[112:115], a[80:83], v[112:115]\n"
    "v_mfma_f32_16x16x32_bf16 v[112:115], a[116:119], a[84:87], v[112:115]\n"
    "ds_read_b64_tr_b16 a[40:41], v207 offset:16384\n"
    "v_mfma_f32_16x16x32_bf16 v[116:119], a[112:115], a[88:91], v[116:119]\n"
    "v_mfma_f32_16x16x32_bf16 v[116:119], a[116:119], a[92:95], v[116:119]\n"
    "ds_read_b64_tr_b16 a[42:43], v207 offset:16896\n"
    "s_waitcnt lgkmcnt(14)\n"
    "v_mfma_f32_16x16x32_bf16 v[120:123], a[48:51], a[96:99], v[120:123]\n"
    "ds_read_b128 a[112:115], v202 offset:14336\n"
    "ds_read_b64_tr_b16 a[44:45], v207 offset:20480\n"
    "v_mfma_f32_16x16x32_bf16 v[120:123], a[52:55], a[100:103], v[120:123]\n"
    "ds_read_b128 a[116:119], v203 offset:14336\n"
    "s_add_u32 m0, s59, 36864\n"
    "v_mfma_f32_16x16x32_bf16 v[124:127], a[56:59], a[96:99], v[124:127]\n"
    "ds_read_b64_tr_b16 a[46:47], v207 offset:20992\n"
    "s_xor_b32 s63, s60, 66432\n"
    "v_mfma_f32_16x16x32_bf16 v[124:127], a[60:63], a[100:103], v[124:127]\n"
    "buffer_load_dwordx4 v196, s[24:27], s63 offen lds\n"
    "v_mfma_f32_16x16x32_bf16 v[128:131], a[96:99], a[64:67], v[128:131]\n"
    "v_mfma_f32_16x16x32_bf16 v[128:131], a[100:103], a[68:71], v[128:131]\n"
    "buffer_load_dwordx4 v197, s[24:27], s63 offen offset:1024 lds\n"
    "v_mfma_f32_16x16x32_bf16 v[132:135], a[96:99], a[72:75], v[132:135]\n"
    "v_mfma_f32_16x16x32_bf16 v[132:135], a[100:103], a[76:79], v[132:135]\n"
    "v_mfma_f32_16x16x32_bf16 v[136:139], a[96:99], a[80:83], v[136:139]\n"
    "v_mfma_f32_16x16x32_bf16 v[136:139], a[100:103], a[84:87], v[136:139]\n"
    "v_mfma_f32_16x16x32_bf16 v[140:143], a[96:99], a[88:91], v[140:143]\n"
    "v_mfma_f32_16x16x32_bf16 v[140:143], a[100:103], a[92:95], v[140:143]\n"
    "s_waitcnt vmcnt(14) lgkmcnt(0)\n"
    "s_barrier\n"
    "v_mfma_f32_16x16x32_bf16 v[144:147], a[48:51], a[104:107], v[144:147]\n"
    "ds_read_b128 a[96:99], v200 offset:0\n"
    "v_mfma_f32_16x16x32_bf16 v[144:147], a[52:55], a[108:111], v[144:147]\n"
    "ds_read_b128 a[100:103], v201 offset:0\n"
    "v_mfma_f32_16x16x32_bf16 v[148:151], a[56:59], a[104:107], v[148:151]\n"
    "s_waitcnt vmcnt(8)\n"
    "ds_read_b64_tr_b16 a[128:129], v212 offset:49152\n"
    "s_add_u32 m0, s61, 98304\n"
    "v_mfma_f32_16x16x32_bf16 v[148:151], a[60:63], a[108:111], v[148:151]\n"
    "s_add_u32 s63, s62, 3145728\n"
    "v_mfma_f32_16x16x32_bf16 v[152:155], a[104:107], a[64:67], v[152:155]\n"
    "ds_read_b64_tr_b16 a[130:131], v212 offset:51200\n"
    "buffer_load_dwordx4 v198, s[20:23], s63 offen lds\n"
    "v_mfma_f32_16x16x32_bf16 v[152:155], a[108:111], a[68:71], v[152:155]\n"
    "v_mfma_f32_16x16x32_bf16 v[156:159], a[104:107], a[72:75], v[156:159]\n"
    "ds_read_b64_tr_b16 a[132:133], v213 offset:49152\n"
    "v_mfma_f32_16x16x32_bf16 v[156:159], a[108:111], a[76:79], v[156:159]\n"
    "buffer_load_dwordx4 v199, s[20:23], s63 offen offset:1024 lds\n"
    "v_mfma_f32_16x16x32_bf16 v[160:163], a[104:107], a[80:83], v[160:163]\n"
    "ds_read_b64_tr_b16 a[134:135], v213 offset:51200\n"
    "v_mfma_f32_16x16x32_bf16 v[160:163], a[108:111], a[84:87], v[160:163]\n"
    "v_mfma_f32_16x16x32_bf16 v[164:167], a[104:107], a[88:91], v[164:167]\n"
    "ds_read_b64_tr_b16 a[136:137], v214 offset:49152\n"
    "v_mfma_f32_16x16x32_bf16 v[164:167], a[108:111], a[92:95], v[164:167]\n"
    "v_mfma_f32_16x16x32_bf16 v[168:171], a[48:51], a[112:115], v[168:171]\n"
    "ds_read_b128 a[104:107], v200 offset:2048\n"
    "ds_read_b64_tr_b16 a[138:139], v214 offset:51200\n"
    "v_mfma_f32_16x16x32_bf16 v[168:171], a[52:55], a[116:119], v[168:171]\n"
    "ds_read_b128 a[108:111], v201 offset:2048\n"
    "s_add_u32 m0, s59, 38912\n"
    "v_mfma_f32_16x16x32_bf16 v[172:175], a[56:59], a[112:115], v[172:175]\n"
    "ds_read_b64_tr_b16 a[140:141], v215 offset:49152\n"
    "s_xor_b32 s63, s60, 99200\n"
    "v_mfma_f32_16x16x32_bf16 v[172:175], a[60:63], a[116:119], v[172:175]\n"
    "buffer_load_dwordx4 v196, s[24:27], s63 offen lds\n"
    "v_mfma_f32_16x16x32_bf16 v[176:179], a[112:115], a[64:67], v[176:179]\n"
    "ds_read_b64_tr_b16 a[142:143], v215 offset:51200\n"
    "v_mfma_f32_16x16x32_bf16 v[176:179], a[116:119], a[68:71], v[176:179]\n"
    "v_mfma_f32_16x16x32_bf16 v[180:183], a[112:115], a[72:75], v[180:183]\n"
    "buffer_load_dwordx4 v197, s[24:27], s63 offen offset:1024 lds\n"
    "v_mfma_f32_16x16x32_bf16 v[180:183], a[116:119], a[76:79], v[180:183]\n"
    "s_cmp_eq_u32 s81, 1\n"
    "s_cbranch_scc0 Lpf5%=\n"
    "v_lshl_add_u64 v[244:245], v[246:247], 0, v[244:245]\n"
    "global_load_dword v249, v[244:245], off\n"
    "Lpf5%=:\n"
    "v_mfma_f32_16x16x32_bf16 v[184:187], a[112:115], a[80:83], v[184:187]\n"
    "v_mfma_f32_16x16x32_bf16 v[184:187], a[116:119], a[84:87], v[184:187]\n"
    "v_mfma_f32_16x16x32_bf16 v[188:191], a[112:115], a[88:91], v[188:191]\n"
    "v_mfma_f32_16x16x32_bf16 v[188:191], a[116:119], a[92:95], v[188:191]\n"
    "s_waitcnt lgkmcnt(10)\n"
    "v_mfma_f32_16x16x32_bf16 v[0:3], a[0:3], a[96:99], v[0:3]\n"
    "ds_read_b128 a[112:115], v200 offset:4096\n"
    "s_waitcnt lgkmcnt(1)\n"
    "s_add_u32 s63, s65, 128\n"
    "v_mfma_f32_16x16x32_bf16 v[0:3], a[4:7], a[100:103], v[0:3]\n"
    "ds_read_b128 a[116:119], v201 offset:4096\n"
    "s_and_b32 s63, s63, 0x7ffff\n"
    "v_mfma_f32_16x16x32_bf16 v[4:7], a[8:11], a[96:99], v[4:7]\n"
    "s_add_u32 m0, s64, 147456\n"
    "v_mfma_f32_16x16x32_bf16 v[4:7], a[12:15], a[100:103], v[4:7]\n"
    "buffer_load_dwordx4 v208, s[32:35], s63 offen offset:0 lds\n"
    "v_mfma_f32_16x16x32_bf16 v[8:11], a[96:99], a[16:19], v[8:11]\n"
    "buffer_load_dwordx4 v209, s[32:35], s63 offen offset:1024 lds\n"
    "v_mfma_f32_16x16x32_bf16 v[8:11], a[100:103], a[20:23], v[8:11]\n"
    "v_mfma_f32_16x16x32_bf16 v[12:15], a[96:99], a[24:27], v[12:15]\n"
    "v_mfma_f32_16x16x32_bf16 v[12:15], a[100:103], a[28:31], v[12:15]\n"
    "v_mfma_f32_16x16x32_bf16 v[16:19], a[96:99], a[32:35], v[16:19]\n"
    "s_add_u32 m0, s61, 106496\n"
    "v_mfma_f32_16x16x32_bf16 v[16:19], a[100:103], a[36:39], v[16:19]\n"
    "s_add_u32 s63, s62, 3147776\n"
    "v_mfma_f32_16x16x32_bf16 v[20:23], a[96:99], a[40:43], v[20:23]\n"
    "buffer_load_dwordx4 v198, s[20:23], s63 offen lds\n"
    "v_mfma_f32_16x16x32_bf16 v[20:23], a[100:103], a[44:47], v[20:23]\n"
    "v_mfma_f32_16x16x32_bf16 v[24:27], a[0:3], a[104:107], v[24:27]\n"
    "ds_read_b128 a[96:99], v200 offset:6144\n"
    "v_mfma_f32_16x16x32_bf16 v[24:27], a[4:7], a[108:111], v[24:27]\n"
    "ds_read_b128 a[100:103], v201 offset:6144\n"
    "buffer_load_dwordx4 v199, s[20:23], s63 offen offset:1024 lds\n"
    "v_mfma_f32_16x16x32_bf16 v[28:31], a[8:11], a[104:107], v[28:31]\n"
    "v_mfma_f32_16x16x32_bf16 v[28:31], a[12:15], a[108:111], v[28:31]\n"
    "v_mfma_f32_16x16x32_bf16 v[32:35], a[104:107], a[16:19], v[32:35]\n"
    "s_add_u32 s63, s65, 128\n"
    "v_mfma_f32_16x16x32_bf16 v[32:35], a[108:111], a[20:23], v[32:35]\n"
    "s_and_b32 s63, s63, 0x7ffff\n"
    "v_mfma_f32_16x16x32_bf16 v[36:39], a[104:107], a[24:27], v[36:39]\n"
    "s_add_u32 m0, s64, 147456\n"
    "v_mfma_f32_16x16x32_bf16 v[36:39], a[108:111], a[28:31], v[36:39]\n"
    "buffer_load_dwordx4 v210, s[32:35], s63 offen offset:2048 lds\n"
    "v_mfma_f32_16x16x32_bf16 v[40:43], a[104:107], a[32:35], v[40:43]\n"
    "buffer_load_dwordx4 v211, s[32:35], s63 offen offset:3072 lds\n"
    "v_mfma_f32_16x16x32_bf16 v[40:43], a[108:111], a[36:39], v[40:43]\n"
    "v_mfma_f32_16x16x32_bf16 v[44:47], a[104:107], a[40:43], v[44:47]\n"
    "v_mfma_f32_16x16x32_bf16 v[44:47], a[108:111], a[44:47], v[44:47]\n"
    "s_waitcnt lgkmcnt(2)\n"
    "s_waitcnt vmcnt(14)\n"
    "s_barrier\n"
    "v_mfma_f32_16x16x32_bf16 v[48:51], a[0:3], a[112:115], v[48:51]\n"
    "ds_read_b128 a[104:107], v200 offset:8192\n"
    "ds_read_b64_tr_b16 a[48:49], v204 offset:24576\n"
    "v_mfma_f32_16x16x32_bf16 v[48:51], a[4:7], a[116:119], v[48:51]\n"
    "ds_read_b128 a[108:111], v201 offset:8192\n"
    "ds_read_b64_tr_b16 a[50:51], v204 offset:25088\n"
    "v_mfma_f32_16x16x32_bf16 v[52:55], a[8:11], a[112:115], v[52:55]\n"
    "s_add_u32 m0, s59, 65536\n"
    "v_mfma_f32_16x16x32_bf16 v[52:55], a[12:15], a[116:119], v[52:55]\n"
    "ds_read_b64_tr_b16 a[52:53], v204 offset:28672\n"
    "s_xor_b32 s63, s60, 1024\n"
    "v_mfma_f32_16x16x32_bf16 v[56:59], a[112:115], a[16:19], v[56:59]\n"
    "buffer_load_dwordx4 v196, s[24:27], s63 offen lds\n"
    "v_mfma_f32_16x16x32_bf16 v[56:59], a[116:119], a[20:23], v[56:59]\n"
    "ds_read_b64_tr_b16 a[54:55], v204 offset:29184\n"
    "v_mfma_f32_16x16x32_bf16 v[60:63], a[112:115], a[24:27], v[60:63]\n"
    "ds_read_b64_tr_b16 a[56:57], v205 offset:24576\n"
    "v_mfma_f32_16x16x32_bf16 v[60:63], a[116:119], a[28:31], v[60:63]\n"
    "buffer_load_dwordx4 v197, s[24:27], s63 offen offset:1024 lds\n"
    "v_mfma_f32_16x16x32_bf16 v[64:67], a[112:115], a[32:35], v[64:67]\n"
    "ds_read_b64_tr_b16 a[58:59], v205 offset:25088\n"
    "v_mfma_f32_16x16x32_bf16 v[64:67], a[116:119], a[36:39], v[64:67]\n"
    "v_mfma_f32_16x16x32_bf16 v[68:71], a[112:115], a[40:43], v[68:71]\n"
    "ds_read_b64_tr_b16 a[60:61], v205 offset:28672\n"
    "v_mfma_f32_16x16x32_bf16 v[68:71], a[116:119], a[44:47], v[68:71]\n"
    "ds_read_b64_tr_b16 a[62:63], v205 offset:29184\n"
    "s_waitcnt lgkmcnt(10)\n"
    "v_mfma_f32_16x16x32_bf16 v[72:75], a[0:3], a[96:99], v[72:75]\n"
    "ds_read_b128 a[112:115], v200 offset:10240\n"
    "v_mfma_f32_16x16x32_bf16 v[72:75], a[4:7], a[100:103], v[72:75]\n"
    "ds_read_b128 a[116:119], v201 offset:10240\n"
    "ds_read_b64_tr_b16 a[64:65], v204 offset:32768\n"
    "v_mfma_f32_16x16x32_bf16 v[76:79], a[8:11], a[96:99], v[76:79]\n"
    "s_add_u32 m0, s61, 114688\n"
    "v_mfma_f32_16x16x32_bf16 v[76:79], a[12:15], a[100:103], v[76:79]\n"
    "ds_read_b64_tr_b16 a[66:67], v204 offset:33280\n"
    "s_add_u32 s63, s62, 3149824\n"
    "v_mfma_f32_16x16x32_bf16 v[80:83], a[96:99], a[16:19], v[80:83]\n"
    "ds_read_b64_tr_b16 a[68:69], v204 offset:36864\n"
    "buffer_load_dwordx4 v198, s[20:23], s63 offen lds\n"
    "v_mfma_f32_16x16x32_bf16 v[80:83], a[100:103], a[20:23], v[80:83]\n"
    "v_mfma_f32_16x16x32_bf16 v[84:87], a[96:99], a[24:27], v[84:87]\n"
    "ds_read_b64_tr_b16 a[70:71], v204 offset:37376\n"
    "v_mfma_f32_16x16x32_bf16 v[84:87], a[100:103], a[28:31], v[84:87]\n"
    "buffer_load_dwordx4 v199, s[20:23], s63 offen offset:1024 lds\n"
    "v_mfma_f32_16x16x32_bf16 v[88:91], a[96:99], a[32:35], v[88:91]\n"
    "ds_read_b64_tr_b16 a[72:73], v205 offset:32768\n"
    "v_mfma_f32_16x16x32_bf16 v[88:91], a[100:103], a[36:39], v[88:91]\n"
    "ds_read_b64_tr_b16 a[74:75], v205 offset:33280\n"
    "buffer_load_dwordx4 v[226:229], v216, s[24:27], s72 offen offset:0\n"
    "v_mfma_f32_16x16x32_bf16 v[92:95], a[96:99], a[40:43], v[92:95]\n"
    "v_mfma_f32_16x16x32_bf16 v[92:95], a[100:103], a[44:47], v[92:95]\n"
    "ds_read_b64_tr_b16 a[76:77], v205 offset:36864\n"
    "s_waitcnt lgkmcnt(14)\n"
    "v_mfma_f32_16x16x32_bf16 v[96:99], a[0:3], a[104:107], v[96:99]\n"
    "ds_read_b128 a[96:99], v200 offset:12288\n"
    "v_mfma_f32_16x16x32_bf16 v[96:99], a[4:7], a[108:111], v[96:99]\n"
    "ds_read_b128 a[100:103], v201 offset:12288\n"
    "ds_read_b64_tr_b16 a[78:79], v205 offset:37376\n"
    "v_mfma_f32_16x16x32_bf16 v[100:103], a[8:11], a[104:107], v[100:103]\n"
    "ds_read_b64_tr_b16 a[80:81], v206 offset:40960\n"
    "s_add_u32 m0, s59, 67584\n"
    "v_mfma_f32_16x16x32_bf16 v[100:103], a[12:15], a[108:111], v[100:103]\n"
    "s_xor_b32 s63, s60, 33792\n"
    "v_mfma_f32_16x16x32_bf16 v[104:107], a[104:107], a[16:19], v[104:107]\n"
    "ds_read_b64_tr_b16 a[82:83], v206 offset:41472\n"
    "buffer_load_dwordx4 v196, s[24:27], s63 offen lds\n"
    "v_mfma_f32_16x16x32_bf16 v[104:107], a[108:111], a[20:23], v[104:107]\n"
    "v_mfma_f32_16x16x32_bf16 v[108:111], a[104:107], a[24:27], v[108:111]\n"
    "ds_read_b64_tr_b16 a[84:85], v206 offset:45056\n"
    "v_mfma_f32_16x16x32_bf16 v[108:111], a[108:111], a[28:31], v[108:111]\n"
    "ds_read_b64_tr_b16 a[86:87], v206 offset:45568\n"
    "buffer_load_dwordx4 v197, s[24:27], s63 offen offset:1024 lds\n"
    "v_mfma_f32_16x16x32_bf16 v[112:115], a[104:107], a[32:35], v[112:115]\n"
    "v_mfma_f32_16x16x32_bf16 v[112:115], a[108:111], a[36:39], v[112:115]\n"
    "ds_read_b64_tr_b16 a[88:89], v207 offset:40960\n"
    "v_mfma_f32_16x16x32_bf16 v[116:119], a[104:107], a[40:43], v[116:119]\n"
    "v_mfma_f32_16x16x32_bf16 v[116:119], a[108:111], a[44:47], v[116:119]\n"
    "ds_read_b64_tr_b16 a[90:91], v207 offset:41472\n"
    "s_waitcnt lgkmcnt(14)\n"
    "v_mfma_f32_16x16x32_bf16 v[120:123], a[0:3], a[112:115], v[120:123]\n"
    "ds_read_b128 a[104:107], v200 offset:14336\n"
    "ds_read_b64_tr_b16 a[92:93], v207 offset:45056\n"
    "v_mfma_f32_16x16x32_bf16 v[120:123], a[4:7], a[116:119], v[120:123]\n"
    "ds_read_b128 a[108:111], v201 offset:14336\n"
    "s_add_u32 m0, s59, 69632\n"
    "v_mfma_f32_16x16x32_bf16 v[124:127], a[8:11], a[112:115], v[124:127]\n"
    "ds_read_b64_tr_b16 a[94:95], v207 offset:45568\n"
    "s_xor_b32 s63, s60, 66560\n"
    "v_mfma_f32_16x16x32_bf16 v[124:127], a[12:15], a[116:119], v[124:127]\n"
    "buffer_load_dwordx4 v196, s[24:27], s63 offen lds\n"
    "v_mfma_f32_16x16x32_bf16 v[128:131], a[112:115], a[16:19], v[128:131]\n"
    "v_mfma_f32_16x16x32_bf16 v[128:131], a[116:119], a[20:23], v[128:131]\n"
    "buffer_load_dwordx4 v197, s[24:27], s63 offen offset:1024 lds\n"
    "v_mfma_f32_16x16x32_bf16 v[132:135], a[112:115], a[24:27], v[132:135]\n"
    "v_mfma_f32_16x16x32_bf16 v[132:135], a[116:119], a[28:31], v[132:135]\n"
    "v_mfma_f32_16x16x32_bf16 v[136:139], a[112:115], a[32:35], v[136:139]\n"
    "v_mfma_f32_16x16x32_bf16 v[136:139], a[116:119], a[36:39], v[136:139]\n"
    "v_mfma_f32_16x16x32_bf16 v[140:143], a[112:115], a[40:43], v[140:143]\n"
    "v_mfma_f32_16x16x32_bf16 v[140:143], a[116:119], a[44:47], v[140:143]\n"
    "s_waitcnt vmcnt(15) lgkmcnt(0)\n"
    "s_barrier\n"
    "v_mfma_f32_16x16x32_bf16 v[144:147], a[0:3], a[96:99], v[144:147]\n"
    "ds_read_b128 a[112:115], v200 offset:32768\n"
    "v_mfma_f32_16x16x32_bf16 v[144:147], a[4:7], a[100:103], v[144:147]\n"
    "ds_read_b128 a[116:119], v201 offset:32768\n"
    "v_mfma_f32_16x16x32_bf16 v[148:151], a[8:11], a[96:99], v[148:151]\n"
    "s_waitcnt vmcnt(9)\n"
    "ds_read_b64_tr_b16 a[144:145], v212 offset:49152\n"
    "s_add_u32 m0, s61, 122880\n"
    "v_mfma_f32_16x16x32_bf16 v[148:151], a[12:15], a[100:103], v[148:151]\n"
    "s_add_u32 s63, s62, 3538944\n"
    "v_mfma_f32_16x16x32_bf16 v[152:155], a[96:99], a[16:19], v[152:155]\n"
    "ds_read_b64_tr_b16 a[146:147], v212 offset:51200\n"
    "buffer_load_dwordx4 v198, s[20:23], s63 offen lds\n"
    "v_mfma_f32_16x16x32_bf16 v[152:155], a[100:103], a[20:23], v[152:155]\n"
    "v_mfma_f32_16x16x32_bf16 v[156:159], a[96:99], a[24:27], v[156:159]\n"
    "ds_read_b64_tr_b16 a[148:149], v213 offset:49152\n"
    "v_mfma_f32_16x16x32_bf16 v[156:159], a[100:103], a[28:31], v[156:159]\n"
    "buffer_load_dwordx4 v199, s[20:23], s63 offen offset:1024 lds\n"
    "v_mfma_f32_16x16x32_bf16 v[160:163], a[96:99], a[32:35], v[160:163]\n"
    "ds_read_b64_tr_b16 a[150:151], v213 offset:51200\n"
    "v_mfma_f32_16x16x32_bf16 v[160:163], a[100:103], a[36:39], v[160:163]\n"
    "v_mfma_f32_16x16x32_bf16 v[164:167], a[96:99], a[40:43], v[164:167]\n"
    "ds_read_b64_tr_b16 a[152:153], v214 offset:49152\n"
    "v_mfma_f32_16x16x32_bf16 v[164:167], a[100:103], a[44:47], v[164:167]\n"
    "v_mfma_f32_16x16x32_bf16 v[168:171], a[0:3], a[104:107], v[168:171]\n"
    "ds_read_b128 a[96:99], v200 offset:34816\n"
    "ds_read_b64_tr_b16 a[154:155], v214 offset:51200\n"
    "v_mfma_f32_16x16x32_bf16 v[168:171], a[4:7], a[108:111], v[168:171]\n"
    "ds_read_b128 a[100:103], v201 offset:34816\n"
    "s_add_u32 m0, s59, 71680\n"
    "v_mfma_f32_16x16x32_bf16 v[172:175], a[8:11], a[104:107], v[172:175]\n"
    "ds_read_b64_tr_b16 a[156:157], v215 offset:49152\n"
    "s_xor_b32 s63, s60, 99328\n"
    "v_mfma_f32_16x16x32_bf16 v[172:175], a[12:15], a[108:111], v[172:175]\n"
    "buffer_load_dwordx4 v196, s[24:27], s63 offen lds\n"
    "v_mfma_f32_16x16x32_bf16 v[176:179], a[104:107], a[16:19], v[176:179]\n"
    "ds_read_b64_tr_b16 a[158:159], v215 offset:51200\n"
    "v_mfma_f32_16x16x32_bf16 v[176:179], a[108:111], a[20:23], v[176:179]\n"
    "v_mfma_f32_16x16x32_bf16 v[180:183], a[104:107], a[24:27], v[180:183]\n"
    "buffer_load_dwordx4 v197, s[24:27], s63 offen offset:1024 lds\n"
    "v_mfma_f32_16x16x32_bf16 v[180:183], a[108:111], a[28:31], v[180:183]\n"
    "s_cmp_eq_u32 s81, 2\n"
    "s_cbranch_scc0 Lpf6%=\n"
    "v_lshl_add_u64 v[244:245], v[246:247], 0, v[244:245]\n"
    "global_load_dword v249, v[244:245], off\n"
    "Lpf6%=:\n"
    "v_mfma_f32_16x16x32_bf16 v[184:187], a[104:107], a[32:35], v[184:187]\n"
    "v_mfma_f32_16x16x32_bf16 v[184:187], a[108:111], a[36:39], v[184:187]\n"
    "v_mfma_f32_16x16x32_bf16 v[188:191], a[104:107], a[40:43], v[188:191]\n"
    "v_mfma_f32_16x16x32_bf16 v[188:191], a[108:111], a[44:47], v[188:191]\n"
    "s_waitcnt lgkmcnt(10)\n"
    "v_mfma_f32_16x16x32_bf16 v[0:3], a[48:51], a[112:115], v[0:3]\n"
    "ds_read_b128 a[104:107], v200 offset:36864\n"
    "s_waitcnt lgkmcnt(1)\n"
    "s_add_u32 s63, s65, 65536\n"
    "v_mfma_f32_16x16x32_bf16 v[0:3], a[52:55], a[116:119], v[0:3]\n"
    "ds_read_b128 a[108:111], v201 offset:36864\n"
    "s_and_b32 s63, s63, 0x7ffff\n"
    "v_mfma_f32_16x16x32_bf16 v[4:7], a[56:59], a[112:115], v[4:7]\n"
    "s_add_u32 m0, s64, 147456\n"
    "v_mfma_f32_16x16x32_bf16 v[4:7], a[60:63], a[116:119], v[4:7]\n"
    "buffer_load_dwordx4 v208, s[32:35], s63 offen offset:0 lds\n"
    "v_mfma_f32_16x16x32_bf16 v[8:11], a[112:115], a[64:67], v[8:11]\n"
    "buffer_load_dwordx4 v209, s[32:35], s63 offen offset:1024 lds\n"
    "v_mfma_f32_16x16x32_bf16 v[8:11], a[116:119], a[68:71], v[8:11]\n"
    "v_mfma_f32_16x16x32_bf16 v[12:15], a[112:115], a[72:75], v[12:15]\n"
    "v_mfma_f32_16x16x32_bf16 v[12:15], a[116:119], a[76:79], v[12:15]\n"
    "v_mfma_f32_16x16x32_bf16 v[16:19], a[112:115], a[80:83], v[16:19]\n"
    "s_add_u32 m0, s61, 131072\n"
    "v_mfma_f32_16x16x32_bf16 v[16:19], a[116:119], a[84:87], v[16:19]\n"
    "s_add_u32 s63, s62, 3540992\n"
    "v_mfma_f32_16x16x32_bf16 v[20:23], a[112:115], a[88:91], v[20:23]\n"
    "buffer_load_dwordx4 v198, s[20:23], s63 offen lds\n"
    "v_mfma_f32_16x16x32_bf16 v[20:23], a[116:119], a[92:95], v[20:23]\n"
    "v_mfma_f32_16x16x32_bf16 v[24:27], a[48:51], a[96:99], v[24:27]\n"
    "ds_read_b128 a[112:115], v200 offset:38912\n"
    "v_mfma_f32_16x16x32_bf16 v[24:27], a[52:55], a[100:103], v[24:27]\n"
    "ds_read_b128 a[116:119], v201 offset:38912\n"
    "buffer_load_dwordx4 v199, s[20:23], s63 offen offset:1024 lds\n"
    "v_mfma_f32_16x16x32_bf16 v[28:31], a[56:59], a[96:99], v[28:31]\n"
    "v_mfma_f32_16x16x32_bf16 v[28:31], a[60:63], a[100:103], v[28:31]\n"
    "v_mfma_f32_16x16x32_bf16 v[32:35], a[96:99], a[64:67], v[32:35]\n"
    "s_add_u32 s63, s65, 65536\n"
    "v_mfma_f32_16x16x32_bf16 v[32:35], a[100:103], a[68:71], v[32:35]\n"
    "s_and_b32 s63, s63, 0x7ffff\n"
    "v_mfma_f32_16x16x32_bf16 v[36:39], a[96:99], a[72:75], v[36:39]\n"
    "s_add_u32 m0, s64, 147456\n"
    "v_mfma_f32_16x16x32_bf16 v[36:39], a[100:103], a[76:79], v[36:39]\n"
    "buffer_load_dwordx4 v210, s[32:35], s63 offen offset:2048 lds\n"
    "v_mfma_f32_16x16x32_bf16 v[40:43], a[96:99], a[80:83], v[40:43]\n"
    "buffer_load_dwordx4 v211, s[32:35], s63 offen offset:3072 lds\n"
    "v_mfma_f32_16x16x32_bf16 v[40:43], a[100:103], a[84:87], v[40:43]\n"
    "v_mfma_f32_16x16x32_bf16 v[44:47], a[96:99], a[88:91], v[44:47]\n"
    "v_mfma_f32_16x16x32_bf16 v[44:47], a[100:103], a[92:95], v[44:47]\n"
    "s_waitcnt lgkmcnt(2)\n"
    "s_waitcnt vmcnt(15)\n"
    "s_barrier\n"
    "v_mfma_f32_16x16x32_bf16 v[48:51], a[48:51], a[104:107], v[48:51]\n"
    "ds_read_b128 a[96:99], v200 offset:40960\n"
    "ds_read_b64_tr_b16 a[0:1], v204 offset:0\n"
    "v_mfma_f32_16x16x32_bf16 v[48:51], a[52:55], a[108:111], v[48:51]\n"
    "ds_read_b128 a[100:103], v201 offset:40960\n"
    "ds_read_b64_tr_b16 a[2:3], v204 offset:512\n"
    "v_mfma_f32_16x16x32_bf16 v[52:55], a[56:59], a[104:107], v[52:55]\n"
    "s_add_u32 m0, s59, 0\n"
    "v_mfma_f32_16x16x32_bf16 v[52:55], a[60:63], a[108:111], v[52:55]\n"
    "ds_read_b64_tr_b16 a[4:5], v204 offset:4096\n"
    "s_xor_b32 s63, s60, 1152\n"
    "v_mfma_f32_16x16x32_bf16 v[56:59], a[104:107], a[64:67], v[56:59]\n"
    "buffer_load_dwordx4 v196, s[24:27], s63 offen lds\n"
    "v_mfma_f32_16x16x32_bf16 v[56:59], a[108:111], a[68:71], v[56:59]\n"
    "ds_read_b64_tr_b16 a[6:7], v204 offset:4608\n"
    "v_mfma_f32_16x16x32_bf16 v[60:63], a[104:107], a[72:75], v[60:63]\n"
    "ds_read_b64_tr_b16 a[8:9], v205 offset:0\n"
    "v_mfma_f32_16x16x32_bf16 v[60:63], a[108:111], a[76:79], v[60:63]\n"
    "buffer_load_dwordx4 v197, s[24:27], s63 offen offset:1024 lds\n"
    "v_mfma_f32_16x16x32_bf16 v[64:67], a[104:107], a[80:83], v[64:67]\n"
    "ds_read_b64_tr_b16 a[10:11], v205 offset:512\n"
    "v_mfma_f32_16x16x32_bf16 v[64:67], a[108:111], a[84:87], v[64:67]\n"
    "v_mfma_f32_16x16x32_bf16 v[68:71], a[104:107], a[88:91], v[68:71]\n"
    "ds_read_b64_tr_b16 a[12:13], v205 offset:4096\n"
    "v_mfma_f32_16x16x32_bf16 v[68:71], a[108:111], a[92:95], v[68:71]\n"
    "ds_read_b64_tr_b16 a[14:15], v205 offset:4608\n"
    "s_waitcnt lgkmcnt(10)\n"
    "v_mfma_f32_16x16x32_bf16 v[72:75], a[48:51], a[112:115], v[72:75]\n"
    "ds_read_b128 a[104:107], v200 offset:43008\n"
    "v_mfma_f32_16x16x32_bf16 v[72:75], a[52:55], a[116:119], v[72:75]\n"
    "ds_read_b128 a[108:111], v201 offset:43008\n"
    "ds_read_b64_tr_b16 a[16:17], v204 offset:8192\n"
    "v_mfma_f32_16x16x32_bf16 v[76:79], a[56:59], a[112:115], v[76:79]\n"
    "s_add_u32 m0, s61, 139264\n"
    "v_mfma_f32_16x16x32_bf16 v[76:79], a[60:63], a[116:119], v[76:79]\n"
    "ds_read_b64_tr_b16 a[18:19], v204 offset:8704\n"
    "s_add_u32 s63, s62, 3543040\n"
    "v_mfma_f32_16x16x32_bf16 v[80:83], a[112:115], a[64:67], v[80:83]\n"
    "ds_read_b64_tr_b16 a[20:21], v204 offset:12288\n"
    "buffer_load_dwordx4 v198, s[20:23], s63 offen lds\n"
    "v_mfma_f32_16x16x32_bf16 v[80:83], a[116:119], a[68:71], v[80:83]\n"
    "v_mfma_f32_16x16x32_bf16 v[84:87], a[112:115], a[72:75], v[84:87]\n"
    "ds_read_b64_tr_b16 a[22:23], v204 offset:12800\n"
    "v_mfma_f32_16x16x32_bf16 v[84:87], a[116:119], a[76:79], v[84:87]\n"
    "buffer_load_dwordx4 v199, s[20:23], s63 offen offset:1024 lds\n"
    "v_mfma_f32_16x16x32_bf16 v[88:91], a[112:115], a[80:83], v[88:91]\n"
    "ds_read_b64_tr_b16 a[24:25], v205 offset:8192\n"
    "v_mfma_f32_16x16x32_bf16 v[88:91], a[116:119], a[84:87], v[88:91]\n"
    "ds_read_b64_tr_b16 a[26:27], v205 offset:8704\n"
    "buffer_load_dwordx4 v[234:237], v216, s[24:27], s72 offen offset:2048\n"
    "v_mfma_f32_16x16x32_bf16 v[92:95], a[112:115], a[88:91], v[92:95]\n"
    "v_mfma_f32_16x16x32_bf16 v[92:95], a[116:119], a[92:95], v[92:95]\n"
    "ds_read_b64_tr_b16 a[28:29], v205 offset:12288\n"
    "s_waitcnt lgkmcnt(14)\n"
    "v_mfma_f32_16x16x32_bf16 v[96:99], a[48:51], a[96:99], v[96:99]\n"
    "ds_read_b128 a[112:115], v200 offset:45056\n"
    "v_mfma_f32_16x16x32_bf16 v[96:99], a[52:55], a[100:103], v[96:99]\n"
    "ds_read_b128 a[116:119], v201 offset:45056\n"
    "ds_read_b64_tr_b16 a[30:31], v205 offset:12800\n"
    "v_mfma_f32_16x16x32_bf16 v[100:103], a[56:59], a[96:99], v[100:103]\n"
    "ds_read_b64_tr_b16 a[32:33], v206 offset:16384\n"
    "s_add_u32 m0, s59, 2048\n"
    "v_mfma_f32_16x16x32_bf16 v[100:103], a[60:63], a[100:103], v[100:103]\n"
    "s_xor_b32 s63, s60, 33920\n"
    "v_mfma_f32_16x16x32_bf16 v[104:107], a[96:99], a[64:67], v[104:107]\n"
    "ds_read_b64_tr_b16 a[34:35], v206 offset:16896\n"
    "buffer_load_dwordx4 v196, s[24:27], s63 offen lds\n"
    "v_mfma_f32_16x16x32_bf16 v[104:107], a[100:103], a[68:71], v[104:107]\n"
    "v_mfma_f32_16x16x32_bf16 v[108:111], a[96:99], a[72:75], v[108:111]\n"
    "ds_read_b64_tr_b16 a[36:37], v206 offset:20480\n"
    "v_mfma_f32_16x16x32_bf16 v[108:111], a[100:103], a[76:79], v[108:111]\n"
    "ds_read_b64_tr_b16 a[38:39], v206 offset:20992\n"
    "buffer_load_dwordx4 v197, s[24:27], s63 offen offset:1024 lds\n"
    "v_mfma_f32_16x16x32_bf16 v[112:115], a[96:99], a[80:83], v[112:115]\n"
    "v_mfma_f32_16x16x32_bf16 v[112:115], a[100:103], a[84:87], v[112:115]\n"
    "ds_read_b64_tr_b16 a[40:41], v207 offset:16384\n"
    "v_mfma_f32_16x16x32_bf16 v[116:119], a[96:99], a[88:91], v[116:119]\n"
    "v_mfma_f32_16x16x32_bf16 v[116:119], a[100:103], a[92:95], v[116:119]\n"
    "ds_read_b64_tr_b16 a[42:43], v207 offset:16896\n"
    "s_waitcnt lgkmcnt(14)\n"
    "v_mfma_f32_16x16x32_bf16 v[120:123], a[48:51], a[104:107], v[120:123]\n"
    "ds_read_b128 a[96:99], v200 offset:47104\n"
    "ds_read_b64_tr_b16 a[44:45], v207 offset:20480\n"
    "v_mfma_f32_16x16x32_bf16 v[120:123], a[52:55], a[108:111], v[120:123]\n"
    "ds_read_b128 a[100:103], v201 offset:47104\n"
    "s_add_u32 m0, s59, 4096\n"
    "v_mfma_f32_16x16x32_bf16 v[124:127], a[56:59], a[104:107], v[124:127]\n"
    "ds_read_b64_tr_b16 a[46:47], v207 offset:20992\n"
    "s_xor_b32 s63, s60, 66688\n"
    "v_mfma_f32_16x16x32_bf16 v[124:127], a[60:63], a[108:111], v[124:127]\n"
    "buffer_load_dwordx4 v196, s[24:27], s63 offen lds\n"
    "v_mfma_f32_16x16x32_bf16 v[128:131], a[104:107], a[64:67], v[128:131]\n"
    "v_mfma_f32_16x16x32_bf16 v[128:131], a[108:111], a[68:71], v[128:131]\n"
    "buffer_load_dwordx4 v197, s[24:27], s63 offen offset:1024 lds\n"
    "v_mfma_f32_16x16x32_bf16 v[132:135], a[104:107], a[72:75], v[132:135]\n"
    "v_mfma_f32_16x16x32_bf16 v[132:135], a[108:111], a[76:79], v[132:135]\n"
    "v_mfma_f32_16x16x32_bf16 v[136:139], a[104:107], a[80:83], v[136:139]\n"
    "v_mfma_f32_16x16x32_bf16 v[136:139], a[108:111], a[84:87], v[136:139]\n"
    "v_mfma_f32_16x16x32_bf16 v[140:143], a[104:107], a[88:91], v[140:143]\n"
    "v_mfma_f32_16x16x32_bf16 v[140:143], a[108:111], a[92:95], v[140:143]\n"
    "s_waitcnt vmcnt(15) lgkmcnt(0)\n"
    "s_barrier\n"
    "v_mfma_f32_16x16x32_bf16 v[144:147], a[48:51], a[112:115], v[144:147]\n"
    "ds_read_b128 a[104:107], v202 offset:0\n"
    "v_mfma_f32_16x16x32_bf16 v[144:147], a[52:55], a[116:119], v[144:147]\n"
    "ds_read_b128 a[108:111], v203 offset:0\n"
    "v_mfma_f32_16x16x32_bf16 v[148:151], a[56:59], a[112:115], v[148:151]\n"
    "s_waitcnt vmcnt(9)\n"
    "ds_read_b64_tr_b16 a[160:161], v212 offset:49152\n"
    "s_add_u32 m0, s61, 98304\n"
    "v_mfma_f32_16x16x32_bf16 v[148:151], a[60:63], a[116:119], v[148:151]\n"
    "s_add_u32 s63, s62, 3932160\n"
    "v_mfma_f32_16x16x32_bf16 v[152:155], a[112:115], a[64:67], v[152:155]\n"
    "ds_read_b64_tr_b16 a[162:163], v212 offset:51200\n"
    "buffer_load_dwordx4 v198, s[20:23], s63 offen lds\n"
    "v_mfma_f32_16x16x32_bf16 v[152:155], a[116:119], a[68:71], v[152:155]\n"
    "v_mfma_f32_16x16x32_bf16 v[156:159], a[112:115], a[72:75], v[156:159]\n"
    "ds_read_b64_tr_b16 a[164:165], v213 offset:49152\n"
    "v_mfma_f32_16x16x32_bf16 v[156:159], a[116:119], a[76:79], v[156:159]\n"
    "buffer_load_dwordx4 v199, s[20:23], s63 offen offset:1024 lds\n"
    "v_mfma_f32_16x16x32_bf16 v[160:163], a[112:115], a[80:83], v[160:163]\n"
    "ds_read_b64_tr_b16 a[166:167], v213 offset:51200\n"
    "v_mfma_f32_16x16x32_bf16 v[160:163], a[116:119], a[84:87], v[160:163]\n"
    "v_mfma_f32_16x16x32_bf16 v[164:167], a[112:115], a[88:91], v[164:167]\n"
    "ds_read_b64_tr_b16 a[168:169], v214 offset:49152\n"
    "v_mfma_f32_16x16x32_bf16 v[164:167], a[116:119], a[92:95], v[164:167]\n"
    "v_mfma_f32_16x16x32_bf16 v[168:171], a[48:51], a[96:99], v[168:171]\n"
    "ds_read_b128 a[112:115], v202 offset:2048\n"
    "ds_read_b64_tr_b16 a[170:171], v214 offset:51200\n"
    "v_mfma_f32_16x16x32_bf16 v[168:171], a[52:55], a[100:103], v[168:171]\n"
    "ds_read_b128 a[116:119], v203 offset:2048\n"
    "s_add_u32 m0, s59, 6144\n"
    "v_mfma_f32_16x16x32_bf16 v[172:175], a[56:59], a[96:99], v[172:175]\n"
    "ds_read_b64_tr_b16 a[172:173], v215 offset:49152\n"
    "s_xor_b32 s63, s60, 99456\n"
    "v_mfma_f32_16x16x32_bf16 v[172:175], a[60:63], a[100:103], v[172:175]\n"
    "buffer_load_dwordx4 v196, s[24:27], s63 offen lds\n"
    "v_mfma_f32_16x16x32_bf16 v[176:179], a[96:99], a[64:67], v[176:179]\n"
    "ds_read_b64_tr_b16 a[174:175], v215 offset:51200\n"
    "v_mfma_f32_16x16x32_bf16 v[176:179], a[100:103], a[68:71], v[176:179]\n"
    "v_mfma_f32_16x16x32_bf16 v[180:183], a[96:99], a[72:75], v[180:183]\n"
    "buffer_load_dwordx4 v197, s[24:27], s63 offen offset:1024 lds\n"
    "v_mfma_f32_16x16x32_bf16 v[180:183], a[100:103], a[76:79], v[180:183]\n"
    "s_cmp_eq_u32 s81, 3\n"
    "s_cbranch_scc0 Lpf7%=\n"
    "v_lshl_add_u64 v[244:245], v[246:247], 0, v[244:245]\n"
    "global_load_dword v249, v[244:245], off\n"
    "Lpf7%=:\n"
    "v_mfma_f32_16x16x32_bf16 v[184:187], a[96:99], a[80:83], v[184:187]\n"
    "v_mfma_f32_16x16x32_bf16 v[184:187], a[100:103], a[84:87], v[184:187]\n"
    "v_mfma_f32_16x16x32_bf16 v[188:191], a[96:99], a[88:91], v[188:191]\n"
    "v_mfma_f32_16x16x32_bf16 v[188:191], a[100:103], a[92:95], v[188:191]\n"
    "s_waitcnt lgkmcnt(10)\n"
    "v_mfma_f32_16x16x32_bf16 v[0:3], a[0:3], a[104:107], v[0:3]\n"
    "ds_read_b128 a[96:99], v202 offset:4096\n"
    "s_waitcnt lgkmcnt(1)\n"
    "s_add_u32 s63, s65, 65664\n"
    "v_mfma_f32_16x16x32_bf16 v[0:3], a[4:7], a[108:111], v[0:3]\n"
    "ds_read_b128 a[100:103], v203 offset:4096\n"
    "s_and_b32 s63, s63, 0x7ffff\n"
    "v_mfma_f32_16x16x32_bf16 v[4:7], a[8:11], a[104:107], v[4:7]\n"
    "s_add_u32 m0, s64, 147456\n"
    "v_cmp_ne_u32 vcc, 0, v246\n"
    "v_mfma_f32_16x16x32_bf16 v[4:7], a[12:15], a[108:111], v[4:7]\n"
    "buffer_load_dwordx4 v208, s[32:35], s63 offen offset:0 lds\n"
    "v_mov_b32 v248, s82\n"
    "v_mfma_f32_16x16x32_bf16 v[8:11], a[104:107], a[16:19], v[8:11]\n"
    "buffer_load_dwordx4 v209, s[32:35], s63 offen offset:1024 lds\n"
    "v_cndmask_b32 v246, v248, v246, vcc\n"
    "v_mfma_f32_16x16x32_bf16 v[8:11], a[108:111], a[20:23], v[8:11]\n"
    "v_ashrrev_i32 v247, 31, v246\n"
    "v_mfma_f32_16x16x32_bf16 v[12:15], a[104:107], a[24:27], v[12:15]\n"
    "v_mfma_f32_16x16x32_bf16 v[12:15], a[108:111], a[28:31], v[12:15]\n"
    "v_mfma_f32_16x16x32_bf16 v[16:19], a[104:107], a[32:35], v[16:19]\n"
    "s_add_u32 m0, s61, 106496\n"
    "v_mfma_f32_16x16x32_bf16 v[16:19], a[108:111], a[36:39], v[16:19]\n"
    "s_add_u32 s63, s62, 3934208\n"
    "v_mfma_f32_16x16x32_bf16 v[20:23], a[104:107], a[40:43], v[20:23]\n"
    "buffer_load_dwordx4 v198, s[20:23], s63 offen lds\n"
    "v_mfma_f32_16x16x32_bf16 v[20:23], a[108:111], a[44:47], v[20:23]\n"
    "v_mfma_f32_16x16x32_bf16 v[24:27], a[0:3], a[112:115], v[24:27]\n"
    "ds_read_b128 a[104:107], v202 offset:6144\n"
    "v_mfma_f32_16x16x32_bf16 v[24:27], a[4:7], a[116:119], v[24:27]\n"
    "ds_read_b128 a[108:111], v203 offset:6144\n"
    "buffer_load_dwordx4 v199, s[20:23], s63 offen offset:1024 lds\n"
    "v_mfma_f32_16x16x32_bf16 v[28:31], a[8:11], a[112:115], v[28:31]\n"
    "v_mfma_f32_16x16x32_bf16 v[28:31], a[12:15], a[116:119], v[28:31]\n"
    "v_mfma_f32_16x16x32_bf16 v[32:35], a[112:115], a[16:19], v[32:35]\n"
    "s_add_u32 s63, s65, 65664\n"
    "v_mfma_f32_16x16x32_bf16 v[32:35], a[116:119], a[20:23], v[32:35]\n"
    "s_and_b32 s63, s63, 0x7ffff\n"
    "v_mfma_f32_16x16x32_bf16 v[36:39], a[112:115], a[24:27], v[36:39]\n"
    "s_add_u32 m0, s64, 147456\n"
    "v_mfma_f32_16x16x32_bf16 v[36:39], a[116:119], a[28:31], v[36:39]\n"
    "buffer_load_dwordx4 v210, s[32:35], s63 offen offset:2048 lds\n"
    "v_mfma_f32_16x16x32_bf16 v[40:43], a[112:115], a[32:35], v[40:43]\n"
    "buffer_load_dwordx4 v211, s[32:35], s63 offen offset:3072 lds\n"
    "v_mfma_f32_16x16x32_bf16 v[40:43], a[116:119], a[36:39], v[40:43]\n"
    "v_mfma_f32_16x16x32_bf16 v[44:47], a[112:115], a[40:43], v[44:47]\n"
    "v_mfma_f32_16x16x32_bf16 v[44:47], a[116:119], a[44:47], v[44:47]\n"
    "s_waitcnt lgkmcnt(2)\n"
    "s_waitcnt vmcnt(15)\n"
    "s_barrier\n"
    "v_mfma_f32_16x16x32_bf16 v[48:51], a[0:3], a[96:99], v[48:51]\n"
    "ds_read_b128 a[112:115], v202 offset:8192\n"
    "ds_read_b64_tr_b16 a[48:49], v204 offset:24576\n"
    "v_mfma_f32_16x16x32_bf16 v[48:51], a[4:7], a[100:103], v[48:51]\n"
    "ds_read_b128 a[116:119], v203 offset:8192\n"
    "ds_read_b64_tr_b16 a[50:51], v204 offset:25088\n"
    "v_mfma_f32_16x16x32_bf16 v[52:55], a[8:11], a[96:99], v[52:55]\n"
    "s_add_u32 m0, s59, 32768\n"
    "v_mfma_f32_16x16x32_bf16 v[52:55], a[12:15], a[100:103], v[52:55]\n"
    "ds_read_b64_tr_b16 a[52:53], v204 offset:28672\n"
    "s_xor_b32 s63, s60, 1280\n"
    "v_mfma_f32_16x16x32_bf16 v[56:59], a[96:99], a[16:19], v[56:59]\n"
    "buffer_load_dwordx4 v196, s[24:27], s63 offen lds\n"
    "v_mfma_f32_16x16x32_bf16 v[56:59], a[100:103], a[20:23], v[56:59]\n"
    "ds_read_b64_tr_b16 a[54:55], v204 offset:29184\n"
    "v_mfma_f32_16x16x32_bf16 v[60:63], a[96:99], a[24:27], v[60:63]\n"
    "ds_read_b64_tr_b16 a[56:57], v205 offset:24576\n"
    "v_mfma_f32_16x16x32_bf16 v[60:63], a[100:103], a[28:31], v[60:63]\n"
    "buffer_load_dwordx4 v197, s[24:27], s63 offen offset:1024 lds\n"
    "v_mfma_f32_16x16x32_bf16 v[64:67], a[96:99], a[32:35], v[64:67]\n"
    "ds_read_b64_tr_b16 a[58:59], v205 offset:25088\n"
    "v_mfma_f32_16x16x32_bf16 v[64:67], a[100:103], a[36:39], v[64:67]\n"
    "v_mfma_f32_16x16x32_bf16 v[68:71], a[96:99], a[40:43], v[68:71]\n"
    "ds_read_b64_tr_b16 a[60:61], v205 offset:28672\n"
    "v_mfma_f32_16x16x32_bf16 v[68:71], a[100:103], a[44:47], v[68:71]\n"
    "ds_read_b64_tr_b16 a[62:63], v205 offset:29184\n"
    "s_waitcnt lgkmcnt(10)\n"
    "v_mfma_f32_16x16x32_bf16 v[72:75], a[0:3], a[104:107], v[72:75]\n"
    "ds_read_b128 a[96:99], v202 offset:10240\n"
    "v_mfma_f32_16x16x32_bf16 v[72:75], a[4:7], a[108:111], v[72:75]\n"
    "ds_read_b128 a[100:103], v203 offset:10240\n"
    "ds_read_b64_tr_b16 a[64:65], v204 offset:32768\n"
    "v_mfma_f32_16x16x32_bf16 v[76:79], a[8:11], a[104:107], v[76:79]\n"
    "s_add_u32 m0, s61, 114688\n"
    "v_mfma_f32_16x16x32_bf16 v[76:79], a[12:15], a[108:111], v[76:79]\n"
    "ds_read_b64_tr_b16 a[66:67], v204 offset:33280\n"
    "s_add_u32 s63, s62, 3936256\n"
    "v_mfma_f32_16x16x32_bf16 v[80:83], a[104:107], a[16:19], v[80:83]\n"
    "ds_read_b64_tr_b16 a[68:69], v204 offset:36864\n"
    "buffer_load_dwordx4 v198, s[20:23], s63 offen lds\n"
    "v_mfma_f32_16x16x32_bf16 v[80:83], a[108:111], a[20:23], v[80:83]\n"
    "v_mfma_f32_16x16x32_bf16 v[84:87], a[104:107], a[24:27], v[84:87]\n"
    "ds_read_b64_tr_b16 a[70:71], v204 offset:37376\n"
    "v_mfma_f32_16x16x32_bf16 v[84:87], a[108:111], a[28:31], v[84:87]\n"
    "buffer_load_dwordx4 v199, s[20:23], s63 offen offset:1024 lds\n"
    "v_mfma_f32_16x16x32_bf16 v[88:91], a[104:107], a[32:35], v[88:91]\n"
    "ds_read_b64_tr_b16 a[72:73], v205 offset:32768\n"
    "v_mfma_f32_16x16x32_bf16 v[88:91], a[108:111], a[36:39], v[88:91]\n"
    "ds_read_b64_tr_b16 a[74:75], v205 offset:33280\n"
    "v_mfma_f32_16x16x32_bf16 v[92:95], a[104:107], a[40:43], v[92:95]\n"
    "v_mfma_f32_16x16x32_bf16 v[92:95], a[108:111], a[44:47], v[92:95]\n"
    "ds_read_b64_tr_b16 a[76:77], v205 offset:36864\n"
    "s_waitcnt lgkmcnt(14)\n"
    "v_mfma_f32_16x16x32_bf16 v[96:99], a[0:3], a[112:115], v[96:99]\n"
    "ds_read_b128 a[104:107], v202 offset:12288\n"
    "v_mfma_f32_16x16x32_bf16 v[96:99], a[4:7], a[116:119], v[96:99]\n"
    "ds_read_b128 a[108:111], v203 offset:12288\n"
    "ds_read_b64_tr_b16 a[78:79], v205 offset:37376\n"
    "v_mfma_f32_16x16x32_bf16 v[100:103], a[8:11], a[112:115], v[100:103]\n"
    "ds_read_b64_tr_b16 a[80:81], v206 offset:40960\n"
    "s_add_u32 m0, s59, 34816\n"
    "v_mfma_f32_16x16x32_bf16 v[100:103], a[12:15], a[116:119], v[100:103]\n"
    "s_xor_b32 s63, s60, 34048\n"
    "v_mfma_f32_16x16x32_bf16 v[104:107], a[112:115], a[16:19], v[104:107]\n"
    "ds_read_b64_tr_b16 a[82:83], v206 offset:41472\n"
    "buffer_load_dwordx4 v196, s[24:27], s63 offen lds\n"
    "v_mfma_f32_16x16x32_bf16 v[104:107], a[116:119], a[20:23], v[104:107]\n"
    "v_mfma_f32_16x16x32_bf16 v[108:111], a[112:115], a[24:27], v[108:111]\n"
    "ds_read_b64_tr_b16 a[84:85], v206 offset:45056\n"
    "v_mfma_f32_16x16x32_bf16 v[108:111], a[116:119], a[28:31], v[108:111]\n"
    "ds_read_b64_tr_b16 a[86:87], v206 offset:45568\n"
    "buffer_load_dwordx4 v197, s[24:27], s63 offen offset:1024 lds\n"
    "v_mfma_f32_16x16x32_bf16 v[112:115], a[112:115], a[32:35], v[112:115]\n"
    "v_mfma_f32_16x16x32_bf16 v[112:115], a[116:119], a[36:39], v[112:115]\n"
    "ds_read_b64_tr_b16 a[88:89], v207 offset:40960\n"
    "v_mfma_f32_16x16x32_bf16 v[116:119], a[112:115], a[40:43], v[116:119]\n"
    "v_mfma_f32_16x16x32_bf16 v[116:119], a[116:119], a[44:47], v[116:119]\n"
    "ds_read_b64_tr_b16 a[90:91], v207 offset:41472\n"
    "s_waitcnt lgkmcnt(14)\n"
    "v_mfma_f32_16x16x32_bf16 v[120:123], a[0:3], a[96:99], v[120:123]\n"
    "ds_read_b128 a[112:115], v202 offset:14336\n"
    "ds_read_b64_tr_b16 a[92:93], v207 offset:45056\n"
    "v_mfma_f32_16x16x32_bf16 v[120:123], a[4:7], a[100:103], v[120:123]\n"
    "ds_read_b128 a[116:119], v203 offset:14336\n"
    "s_add_u32 m0, s59, 36864\n"
    "v_mfma_f32_16x16x32_bf16 v[124:127], a[8:11], a[96:99], v[124:127]\n"
    "ds_read_b64_tr_b16 a[94:95], v207 offset:45568\n"
    "s_xor_b32 s63, s60, 66816\n"
    "v_mfma_f32_16x16x32_bf16 v[124:127], a[12:15], a[100:103], v[124:127]\n"
    "buffer_load_dwordx4 v196, s[24:27], s63 offen lds\n"
    "v_mfma_f32_16x16x32_bf16 v[128:131], a[96:99], a[16:19], v[128:131]\n"
    "v_mfma_f32_16x16x32_bf16 v[128:131], a[100:103], a[20:23], v[128:131]\n"
    "buffer_load_dwordx4 v197, s[24:27], s63 offen offset:1024 lds\n"
    "v_mfma_f32_16x16x32_bf16 v[132:135], a[96:99], a[24:27], v[132:135]\n"
    "v_mfma_f32_16x16x32_bf16 v[132:135], a[100:103], a[28:31], v[132:135]\n"
    "v_mfma_f32_16x16x32_bf16 v[136:139], a[96:99], a[32:35], v[136:139]\n"
    "v_mfma_f32_16x16x32_bf16 v[136:139], a[100:103], a[36:39], v[136:139]\n"
    "v_mfma_f32_16x16x32_bf16 v[140:143], a[96:99], a[40:43], v[140:143]\n"
    "v_mfma_f32_16x16x32_bf16 v[140:143], a[100:103], a[44:47], v[140:143]\n"
    "s_waitcnt vmcnt(14) lgkmcnt(0)\n"
    "s_barrier\n"
    "v_mfma_f32_16x16x32_bf16 v[144:147], a[0:3], a[104:107], v[144:147]\n"
    "ds_read_b128 a[96:99], v200 offset:0\n"
    "v_mfma_f32_16x16x32_bf16 v[144:147], a[4:7], a[108:111], v[144:147]\n"
    "ds_read_b128 a[100:103], v201 offset:0\n"
    "v_mfma_f32_16x16x32_bf16 v[148:151], a[8:11], a[104:107], v[148:151]\n"
    "s_waitcnt vmcnt(8)\n"
    "ds_read_b64_tr_b16 a[176:177], v212 offset:49152\n"
    "s_add_u32 m0, s61, 122880\n"
    "v_mfma_f32_16x16x32_bf16 v[148:151], a[12:15], a[108:111], v[148:151]\n"
    "s_add_u32 s63, s62, 4325376\n"
    "v_mfma_f32_16x16x32_bf16 v[152:155], a[104:107], a[16:19], v[152:155]\n"
    "ds_read_b64_tr_b16 a[178:179], v212 offset:51200\n"
    "buffer_load_dwordx4 v198, s[20:23], s63 offen lds\n"
    "v_mfma_f32_16x16x32_bf16 v[152:155], a[108:111], a[20:23], v[152:155]\n"
    "v_mfma_f32_16x16x32_bf16 v[156:159], a[104:107], a[24:27], v[156:159]\n"
    "ds_read_b64_tr_b16 a[180:181], v213 offset:49152\n"
    "v_mfma_f32_16x16x32_bf16 v[156:159], a[108:111], a[28:31], v[156:159]\n"
    "buffer_load_dwordx4 v199, s[20:23], s63 offen offset:1024 lds\n"
    "v_mfma_f32_16x16x32_bf16 v[160:163], a[104:107], a[32:35], v[160:163]\n"
    "ds_read_b64_tr_b16 a[182:183], v213 offset:51200\n"
    "v_mfma_f32_16x16x32_bf16 v[160:163], a[108:111], a[36:39], v[160:163]\n"
    "v_mfma_f32_16x16x32_bf16 v[164:167], a[104:107], a[40:43], v[164:167]\n"
    "ds_read_b64_tr_b16 a[184:185], v214 offset:49152\n"
    "v_mfma_f32_16x16x32_bf16 v[164:167], a[108:111], a[44:47], v[164:167]\n"
    "v_mfma_f32_16x16x32_bf16 v[168:171], a[0:3], a[112:115], v[168:171]\n"
    "ds_read_b128 a[104:107], v200 offset:2048\n"
    "ds_read_b64_tr_b16 a[186:187], v214 offset:51200\n"
    "v_mfma_f32_16x16x32_bf16 v[168:171], a[4:7], a[116:119], v[168:171]\n"
    "ds_read_b128 a[108:111], v201 offset:2048\n"
    "s_add_u32 m0, s59, 38912\n"
    "v_mfma_f32_16x16x32_bf16 v[172:175], a[8:11], a[112:115], v[172:175]\n"
    "ds_read_b64_tr_b16 a[188:189], v215 offset:49152\n"
    "s_xor_b32 s63, s60, 99584\n"
    "v_mfma_f32_16x16x32_bf16 v[172:175], a[12:15], a[116:119], v[172:175]\n"
    "buffer_load_dwordx4 v196, s[24:27], s63 offen lds\n"
    "v_mfma_f32_16x16x32_bf16 v[176:179], a[112:115], a[16:19], v[176:179]\n"
    "ds_read_b64_tr_b16 a[190:191], v215 offset:51200\n"
    "v_mfma_f32_16x16x32_bf16 v[176:179], a[116:119], a[20:23], v[176:179]\n"
    "v_mfma_f32_16x16x32_bf16 v[180:183], a[112:115], a[24:27], v[180:183]\n"
    "buffer_load_dwordx4 v197, s[24:27], s63 offen offset:1024 lds\n"
    "v_mfma_f32_16x16x32_bf16 v[180:183], a[116:119], a[28:31], v[180:183]\n"
    "s_cmp_eq_u32 s81, 0\n"
    "s_cbranch_scc0 Lpf8%=\n"
    "v_lshl_add_u64 v[244:245], v[246:247], 0, v[244:245]\n"
    "global_load_dword v249, v[244:245], off\n"
    "Lpf8%=:\n"
    "v_mfma_f32_16x16x32_bf16 v[184:187], a[112:115], a[32:35], v[184:187]\n"
    "v_mfma_f32_16x16x32_bf16 v[184:187], a[116:119], a[36:39], v[184:187]\n"
    "v_mfma_f32_16x16x32_bf16 v[188:191], a[112:115], a[40:43], v[188:191]\n"
    "v_mfma_f32_16x16x32_bf16 v[188:191], a[116:119], a[44:47], v[188:191]\n"
    "s_waitcnt lgkmcnt(10)\n"
    "v_mfma_f32_16x16x32_bf16 v[0:3], a[48:51], a[96:99], v[0:3]\n"
    "ds_read_b128 a[112:115], v200 offset:4096\n"
    "s_waitcnt lgkmcnt(1)\n"
    "s_add_u32 s63, s65, 131072\n"
    "v_mfma_f32_16x16x32_bf16 v[0:3], a[52:55], a[100:103], v[0:3]\n"
    "ds_read_b128 a[116:119], v201 offset:4096\n"
    "s_and_b32 s63, s63, 0x7ffff\n"
    "v_mfma_f32_16x16x32_bf16 v[4:7], a[56:59], a[96:99], v[4:7]\n"
    "s_add_u32 m0, s64, 147456\n"
    "v_mfma_f32_16x16x32_bf16 v[4:7], a[60:63], a[100:103], v[4:7]\n"
    "buffer_load_dwordx4 v208, s[32:35], s63 offen offset:0 lds\n"
    "v_mfma_f32_16x16x32_bf16 v[8:11], a[96:99], a[64:67], v[8:11]\n"
    "buffer_load_dwordx4 v209, s[32:35], s63 offen offset:1024 lds\n"
    "v_mfma_f32_16x16x32_bf16 v[8:11], a[100:103], a[68:71], v[8:11]\n"
    "v_mfma_f32_16x16x32_bf16 v[12:15], a[96:99], a[72:75], v[12:15]\n"
    "v_mfma_f32_16x16x32_bf16 v[12:15], a[100:103], a[76:79], v[12:15]\n"
    "v_mfma_f32_16x16x32_bf16 v[16:19], a[96:99], a[80:83], v[16:19]\n"
    "s_add_u32 m0, s61, 131072\n"
    "v_mfma_f32_16x16x32_bf16 v[16:19], a[100:103], a[84:87], v[16:19]\n"
    "s_add_u32 s63, s62, 4327424\n"
    "v_mfma_f32_16x16x32_bf16 v[20:23], a[96:99], a[88:91], v[20:23]\n"
    "buffer_load_dwordx4 v198, s[20:23], s63 offen lds\n"
    "v_mfma_f32_16x16x32_bf16 v[20:23], a[100:103], a[92:95], v[20:23]\n"
    "v_mfma_f32_16x16x32_bf16 v[24:27], a[48:51], a[104:107], v[24:27]\n"
    "ds_read_b128 a[96:99], v200 offset:6144\n"
    "v_mfma_f32_16x16x32_bf16 v[24:27], a[52:55], a[108:111], v[24:27]\n"
    "ds_read_b128 a[100:103], v201 offset:6144\n"
    "buffer_load_dwordx4 v199, s[20:23], s63 offen offset:1024 lds\n"
    "v_mfma_f32_16x16x32_bf16 v[28:31], a[56:59], a[104:107], v[28:31]\n"
    "v_mfma_f32_16x16x32_bf16 v[28:31], a[60:63], a[108:111], v[28:31]\n"
    "v_mfma_f32_16x16x32_bf16 v[32:35], a[104:107], a[64:67], v[32:35]\n"
    "s_add_u32 s63, s65, 131072\n"
    "v_mfma_f32_16x16x32_bf16 v[32:35], a[108:111], a[68:71], v[32:35]\n"
    "s_and_b32 s63, s63, 0x7ffff\n"
    "v_mfma_f32_16x16x32_bf16 v[36:39], a[104:107], a[72:75], v[36:39]\n"
    "s_add_u32 m0, s64, 147456\n"
    "v_mfma_f32_16x16x32_bf16 v[36:39], a[108:111], a[76:79], v[36:39]\n"
    "buffer_load_dwordx4 v210, s[32:35], s63 offen offset:2048 lds\n"
    "v_mfma_f32_16x16x32_bf16 v[40:43], a[104:107], a[80:83], v[40:43]\n"
    "buffer_load_dwordx4 v211, s[32:35], s63 offen offset:3072 lds\n"
    "v_mfma_f32_16x16x32_bf16 v[40:43], a[108:111], a[84:87], v[40:43]\n"
    "v_mfma_f32_16x16x32_bf16 v[44:47], a[104:107], a[88:91], v[44:47]\n"
    "v_mfma_f32_16x16x32_bf16 v[44:47], a[108:111], a[92:95], v[44:47]\n"
    "s_waitcnt lgkmcnt(2)\n"
    "s_waitcnt vmcnt(14)\n"
    "s_barrier\n"
    "v_mfma_f32_16x16x32_bf16 v[48:51], a[48:51], a[112:115], v[48:51]\n"
    "ds_read_b128 a[104:107], v200 offset:8192\n"
    "ds_read_b64_tr_b16 a[0:1], v204 offset:0\n"
    "v_mfma_f32_16x16x32_bf16 v[48:51], a[52:55], a[116:119], v[48:51]\n"
    "ds_read_b128 a[108:111], v201 offset:8192\n"
    "ds_read_b64_tr_b16 a[2:3], v204 offset:512\n"
    "v_mfma_f32_16x16x32_bf16 v[52:55], a[56:59], a[112:115], v[52:55]\n"
    "s_add_u32 m0, s59, 65536\n"
    "v_mfma_f32_16x16x32_bf16 v[52:55], a[60:63], a[116:119], v[52:55]\n"
    "ds_read_b64_tr_b16 a[4:5], v204 offset:4096\n"
    "s_xor_b32 s63, s60, 1408\n"
    "v_mfma_f32_16x16x32_bf16 v[56:59], a[112:115], a[64:67], v[56:59]\n"
    "buffer_load_dwordx4 v196, s[24:27], s63 offen lds\n"
    "v_mfma_f32_16x16x32_bf16 v[56:59], a[116:119], a[68:71], v[56:59]\n"
    "ds_read_b64_tr_b16 a[6:7], v204 offset:4608\n"
    "v_mfma_f32_16x16x32_bf16 v[60:63], a[112:115], a[72:75], v[60:63]\n"
    "ds_read_b64_tr_b16 a[8:9], v205 offset:0\n"
    "v_mfma_f32_16x16x32_bf16 v[60:63], a[116:119], a[76:79], v[60:63]\n"
    "buffer_load_dwordx4 v197, s[24:27], s63 offen offset:1024 lds\n"
    "v_mfma_f32_16x16x32_bf16 v[64:67], a[112:115], a[80:83], v[64:67]\n"
    "ds_read_b64_tr_b16 a[10:11], v205 offset:512\n"
    "v_mfma_f32_16x16x32_bf16 v[64:67], a[116:119], a[84:87], v[64:67]\n"
    "v_mfma_f32_16x16x32_bf16 v[68:71], a[112:115], a[88:91], v[68:71]\n"
    "ds_read_b64_tr_b16 a[12:13], v205 offset:4096\n"
    "v_mfma_f32_16x16x32_bf16 v[68:71], a[116:119], a[92:95], v[68:71]\n"
    "ds_read_b64_tr_b16 a[14:15], v205 offset:4608\n"
    "s_waitcnt lgkmcnt(10)\n"
    "v_mfma_f32_16x16x32_bf16 v[72:75], a[48:51], a[96:99], v[72:75]\n"
    "ds_read_b128 a[112:115], v200 offset:10240\n"
    "v_mfma_f32_16x16x32_bf16 v[72:75], a[52:55], a[100:103], v[72:75]\n"
    "ds_read_b128 a[116:119], v201 offset:10240\n"
    "ds_read_b64_tr_b16 a[16:17], v204 offset:8192\n"
    "v_mfma_f32_16x16x32_bf16 v[76:79], a[56:59], a[96:99], v[76:79]\n"
    "s_add_u32 m0, s61, 139264\n"
    "v_mfma_f32_16x16x32_bf16 v[76:79], a[60:63], a[100:103], v[76:79]\n"
    "ds_read_b64_tr_b16 a[18:19], v204 offset:8704\n"
    "s_add_u32 s63, s62, 4329472\n"
    "v_mfma_f32_16x16x32_bf16 v[80:83], a[96:99], a[64:67], v[80:83]\n"
    "ds_read_b64_tr_b16 a[20:21], v204 offset:12288\n"
    "buffer_load_dwordx4 v198, s[20:23], s63 offen lds\n"
    "v_mfma_f32_16x16x32_bf16 v[80:83], a[100:103], a[68:71], v[80:83]\n"
    "v_mfma_f32_16x16x32_bf16 v[84:87], a[96:99], a[72:75], v[84:87]\n"
    "ds_read_b64_tr_b16 a[22:23], v204 offset:12800\n"
    "v_mfma_f32_16x16x32_bf16 v[84:87], a[100:103], a[76:79], v[84:87]\n"
    "buffer_load_dwordx4 v199, s[20:23], s63 offen offset:1024 lds\n"
    "v_mfma_f32_16x16x32_bf16 v[88:91], a[96:99], a[80:83], v[88:91]\n"
    "ds_read_b64_tr_b16 a[24:25], v205 offset:8192\n"
    "v_mfma_f32_16x16x32_bf16 v[88:91], a[100:103], a[84:87], v[88:91]\n"
    "ds_read_b64_tr_b16 a[26:27], v205 offset:8704\n"
    "v_mfma_f32_16x16x32_bf16 v[92:95], a[96:99], a[88:91], v[92:95]\n"
    "v_mfma_f32_16x16x32_bf16 v[92:95], a[100:103], a[92:95], v[92:95]\n"
    "ds_read_b64_tr_b16 a[28:29], v205 offset:12288\n"
    "s_waitcnt lgkmcnt(14)\n"
    "v_mfma_f32_16x16x32_bf16 v[96:99], a[48:51], a[104:107], v[96:99]\n"
    "ds_read_b128 a[96:99], v200 offset:12288\n"
    "v_mfma_f32_16x16x32_bf16 v[96:99], a[52:55], a[108:111], v[96:99]\n"
    "ds_read_b128 a[100:103], v201 offset:12288\n"
    "ds_read_b64_tr_b16 a[30:31], v205 offset:12800\n"
    "v_mfma_f32_16x16x32_bf16 v[100:103], a[56:59], a[104:107], v[100:103]\n"
    "ds_read_b64_tr_b16 a[32:33], v206 offset:16384\n"
    "s_add_u32 m0, s59, 67584\n"
    "v_mfma_f32_16x16x32_bf16 v[100:103], a[60:63], a[108:111], v[100:103]\n"
    "s_xor_b32 s63, s60, 34176\n"
    "v_mfma_f32_16x16x32_bf16 v[104:107], a[104:107], a[64:67], v[104:107]\n"
    "ds_read_b64_tr_b16 a[34:35], v206 offset:16896\n"
    "buffer_load_dwordx4 v196, s[24:27], s63 offen lds\n"
    "v_mfma_f32_16x16x32_bf16 v[104:107], a[108:111], a[68:71], v[104:107]\n"
    "v_mfma_f32_16x16x32_bf16 v[108:111], a[104:107], a[72:75], v[108:111]\n"
    "ds_read_b64_tr_b16 a[36:37], v206 offset:20480\n"
    "v_mfma_f32_16x16x32_bf16 v[108:111], a[108:111], a[76:79], v[108:111]\n"
    "ds_read_b64_tr_b16 a[38:39], v206 offset:20992\n"
    "buffer_load_dwordx4 v197, s[24:27], s63 offen offset:1024 lds\n"
    "v_mfma_f32_16x16x32_bf16 v[112:115], a[104:107], a[80:83], v[112:115]\n"
    "v_mfma_f32_16x16x32_bf16 v[112:115], a[108:111], a[84:87], v[112:115]\n"
    "ds_read_b64_tr_b16 a[40:41], v207 offset:16384\n"
    "v_mfma_f32_16x16x32_bf16 v[116:119], a[104:107], a[88:91], v[116:119]\n"
    "v_mfma_f32_16x16x32_bf16 v[116:119], a[108:111], a[92:95], v[116:119]\n"
    "ds_read_b64_tr_b16 a[42:43], v207 offset:16896\n"
    "s_waitcnt lgkmcnt(14)\n"
    "v_mfma_f32_16x16x32_bf16 v[120:123], a[48:51], a[112:115], v[120:123]\n"
    "ds_read_b128 a[104:107], v200 offset:14336\n"
    "ds_read_b64_tr_b16 a[44:45], v207 offset:20480\n"
    "v_mfma_f32_16x16x32_bf16 v[120:123], a[52:55], a[116:119], v[120:123]\n"
    "ds_read_b128 a[108:111], v201 offset:14336\n"
    "s_add_u32 m0, s59, 69632\n"
    "v_mfma_f32_16x16x32_bf16 v[124:127], a[56:59], a[112:115], v[124:127]\n"
    "ds_read_b64_tr_b16 a[46:47], v207 offset:20992\n"
    "s_xor_b32 s63, s60, 66944\n"
    "v_mfma_f32_16x16x32_bf16 v[124:127], a[60:63], a[116:119], v[124:127]\n"
    "buffer_load_dwordx4 v196, s[24:27], s63 offen lds\n"
    "v_mfma_f32_16x16x32_bf16 v[128:131], a[112:115], a[64:67], v[128:131]\n"
    "v_mfma_f32_16x16x32_bf16 v[128:131], a[116:119], a[68:71], v[128:131]\n"
    "buffer_load_dwordx4 v197, s[24:27], s63 offen offset:1024 lds\n"
    "v_mfma_f32_16x16x32_bf16 v[132:135], a[112:115], a[72:75], v[132:135]\n"
    "v_mfma_f32_16x16x32_bf16 v[132:135], a[116:119], a[76:79], v[132:135]\n"
    "v_mfma_f32_16x16x32_bf16 v[136:139], a[112:115], a[80:83], v[136:139]\n"
    "v_mfma_f32_16x16x32_bf16 v[136:139], a[116:119], a[84:87], v[136:139]\n"
    "v_mfma_f32_16x16x32_bf16 v[140:143], a[112:115], a[88:91], v[140:143]\n"
    "v_mfma_f32_16x16x32_bf16 v[140:143], a[116:119], a[92:95], v[140:143]\n"
    "s_waitcnt vmcnt(14) lgkmcnt(0)\n"
    "s_barrier\n"
    "v_mfma_f32_16x16x32_bf16 v[144:147], a[48:51], a[96:99], v[144:147]\n"
    "ds_read_b128 a[112:115], v200 offset:32768\n"
    "v_mfma_f32_16x16x32_bf16 v[144:147], a[52:55], a[100:103], v[144:147]\n"
    "ds_read_b128 a[116:119], v201 offset:32768\n"
    "v_mfma_f32_16x16x32_bf16 v[148:151], a[56:59], a[96:99], v[148:151]\n"
    "s_waitcnt vmcnt(8)\n"
    "ds_read_b64_tr_b16 a[192:193], v212 offset:49152\n"
    "s_add_u32 m0, s61, 98304\n"
    "v_mfma_f32_16x16x32_bf16 v[148:151], a[60:63], a[100:103], v[148:151]\n"
    "s_add_u32 s63, s62, 4718592\n"
    "v_mfma_f32_16x16x32_bf16 v[152:155], a[96:99], a[64:67], v[152:155]\n"
    "ds_read_b64_tr_b16 a[194:195], v212 offset:51200\n"
    "buffer_load_dwordx4 v198, s[20:23], s63 offen lds\n"
    "v_mfma_f32_16x16x32_bf16 v[152:155], a[100:103], a[68:71], v[152:155]\n"
    "v_mfma_f32_16x16x32_bf16 v[156:159], a[96:99], a[72:75], v[156:159]\n"
    "ds_read_b64_tr_b16 a[196:197], v213 offset:49152\n"
    "v_mfma_f32_16x16x32_bf16 v[156:159], a[100:103], a[76:79], v[156:159]\n"
    "buffer_load_dwordx4 v199, s[20:23], s63 offen offset:1024 lds\n"
    "v_mfma_f32_16x16x32_bf16 v[160:163], a[96:99], a[80:83], v[160:163]\n"
    "ds_read_b64_tr_b16 a[198:199], v213 offset:51200\n"
    "v_mfma_f32_16x16x32_bf16 v[160:163], a[100:103], a[84:87], v[160:163]\n"
    "v_mfma_f32_16x16x32_bf16 v[164:167], a[96:99], a[88:91], v[164:167]\n"
    "ds_read_b64_tr_b16 a[200:201], v214 offset:49152\n"
    "v_mfma_f32_16x16x32_bf16 v[164:167], a[100:103], a[92:95], v[164:167]\n"
    "v_mfma_f32_16x16x32_bf16 v[168:171], a[48:51], a[104:107], v[168:171]\n"
    "ds_read_b128 a[96:99], v200 offset:34816\n"
    "ds_read_b64_tr_b16 a[202:203], v214 offset:51200\n"
    "v_mfma_f32_16x16x32_bf16 v[168:171], a[52:55], a[108:111], v[168:171]\n"
    "ds_read_b128 a[100:103], v201 offset:34816\n"
    "s_add_u32 m0, s59, 71680\n"
    "v_mfma_f32_16x16x32_bf16 v[172:175], a[56:59], a[104:107], v[172:175]\n"
    "ds_read_b64_tr_b16 a[204:205], v215 offset:49152\n"
    "s_xor_b32 s63, s60, 99712\n"
    "v_mfma_f32_16x16x32_bf16 v[172:175], a[60:63], a[108:111], v[172:175]\n"
    "buffer_load_dwordx4 v196, s[24:27], s63 offen lds\n"
    "v_mfma_f32_16x16x32_bf16 v[176:179], a[104:107], a[64:67], v[176:179]\n"
    "ds_read_b64_tr_b16 a[206:207], v215 offset:51200\n"
    "v_mfma_f32_16x16x32_bf16 v[176:179], a[108:111], a[68:71], v[176:179]\n"
    "v_mfma_f32_16x16x32_bf16 v[180:183], a[104:107], a[72:75], v[180:183]\n"
    "buffer_load_dwordx4 v197, s[24:27], s63 offen offset:1024 lds\n"
    "v_mfma_f32_16x16x32_bf16 v[180:183], a[108:111], a[76:79], v[180:183]\n"
    "s_cmp_eq_u32 s81, 1\n"
    "s_cbranch_scc0 Lpf9%=\n"
    "v_lshl_add_u64 v[244:245], v[246:247], 0, v[244:245]\n"
    "global_load_dword v249, v[244:245], off\n"
    "Lpf9%=:\n"
    "v_mfma_f32_16x16x32_bf16 v[184:187], a[104:107], a[80:83], v[184:187]\n"
    "v_mfma_f32_16x16x32_bf16 v[184:187], a[108:111], a[84:87], v[184:187]\n"
    "v_mfma_f32_16x16x32_bf16 v[188:191], a[104:107], a[88:91], v[188:191]\n"
    "v_mfma_f32_16x16x32_bf16 v[188:191], a[108:111], a[92:95], v[188:191]\n"
    "s_waitcnt lgkmcnt(10)\n"
    "v_mfma_f32_16x16x32_bf16 v[0:3], a[0:3], a[112:115], v[0:3]\n"
    "ds_read_b128 a[104:107], v200 offset:36864\n"
    "s_waitcnt lgkmcnt(1)\n"
    "s_add_u32 s63, s65, 131200\n"
    "v_mfma_f32_16x16x32_bf16 v[0:3], a[4:7], a[116:119], v[0:3]\n"
    "ds_read_b128 a[108:111], v201 offset:36864\n"
    "s_and_b32 s63, s63, 0x7ffff\n"
    "v_mfma_f32_16x16x32_bf16 v[4:7], a[8:11], a[112:115], v[4:7]\n"
    "s_add_u32 m0, s64, 147456\n"
    "v_lshlrev_b32 v193, 16, v218\n"
    "v_mfma_f32_16x16x32_bf16 v[4:7], a[12:15], a[116:119], v[4:7]\n"
    "buffer_load_dwordx4 v208, s[32:35], s63 offen offset:0 lds\n"
    "v_mfma_f32_16x16x32_bf16 v[8:11], a[112:115], a[16:19], v[8:11]\n"
    "buffer_load_dwordx4 v209, s[32:35], s63 offen offset:1024 lds\n"
    "v_and_b32 v218, 0xffff0000, v218\n"
    "v_mfma_f32_16x16x32_bf16 v[8:11], a[116:119], a[20:23], v[8:11]\n"
    "v_mfma_f32_16x16x32_bf16 v[12:15], a[112:115], a[24:27], v[12:15]\n"
    "v_lshlrev_b32 v194, 16, v226\n"
    "v_mfma_f32_16x16x32_bf16 v[12:15], a[116:119], a[28:31], v[12:15]\n"
    "v_mfma_f32_16x16x32_bf16 v[16:19], a[112:115], a[32:35], v[16:19]\n"
    "s_add_u32 m0, s61, 106496\n"
    "v_and_b32 v226, 0xffff0000, v226\n"
    "v_mfma_f32_16x16x32_bf16 v[16:19], a[116:119], a[36:39], v[16:19]\n"
    "s_add_u32 s63, s62, 4720640\n"
    "v_mfma_f32_16x16x32_bf16 v[20:23], a[112:115], a[40:43], v[20:23]\n"
    "buffer_load_dwordx4 v198, s[20:23], s63 offen lds\n"
    "v_fmac_f32 v242, v193, v194\n"
    "v_mfma_f32_16x16x32_bf16 v[20:23], a[116:119], a[44:47], v[20:23]\n"
    "v_mfma_f32_16x16x32_bf16 v[24:27], a[0:3], a[96:99], v[24:27]\n"
    "ds_read_b128 a[112:115], v200 offset:38912\n"
    "v_fmac_f32 v242, v218, v226\n"
    "v_mfma_f32_16x16x32_bf16 v[24:27], a[4:7], a[100:103], v[24:27]\n"
    "ds_read_b128 a[116:119], v201 offset:38912\n"
    "buffer_load_dwordx4 v199, s[20:23], s63 offen offset:1024 lds\n"
    "v_mfma_f32_16x16x32_bf16 v[28:31], a[8:11], a[96:99], v[28:31]\n"
    "v_lshlrev_b32 v194, 16, v234\n"
    "v_mfma_f32_16x16x32_bf16 v[28:31], a[12:15], a[100:103], v[28:31]\n"
    "v_mfma_f32_16x16x32_bf16 v[32:35], a[96:99], a[16:19], v[32:35]\n"
    "s_add_u32 s63, s65, 131200\n"
    "v_and_b32 v234, 0xffff0000, v234\n"
    "v_mfma_f32_16x16x32_bf16 v[32:35], a[100:103], a[20:23], v[32:35]\n"
    "s_and_b32 s63, s63, 0x7ffff\n"
    "v_mfma_f32_16x16x32_bf16 v[36:39], a[96:99], a[24:27], v[36:39]\n"
    "s_add_u32 m0, s64, 147456\n"
    "v_fmac_f32 v243, v193, v194\n"
    "v_mfma_f32_16x16x32_bf16 v[36:39], a[100:103], a[28:31], v[36:39]\n"
    "buffer_load_dwordx4 v210, s[32:35], s63 offen offset:2048 lds\n"
    "v_mfma_f32_16x16x32_bf16 v[40:43], a[96:99], a[32:35], v[40:43]\n"
    "buffer_load_dwordx4 v211, s[32:35], s63 offen offset:3072 lds\n"
    "v_fmac_f32 v243, v218, v234\n"
    "v_mfma_f32_16x16x32_bf16 v[40:43], a[100:103], a[36:39], v[40:43]\n"
    "v_mfma_f32_16x16x32_bf16 v[44:47], a[96:99], a[40:43], v[44:47]\n"
    "v_lshlrev_b32 v193, 16, v219\n"
    "v_mfma_f32_16x16x32_bf16 v[44:47], a[100:103], a[44:47], v[44:47]\n"
    "s_waitcnt lgkmcnt(2)\n"
    "s_waitcnt vmcnt(14)\n"
    "s_barrier\n"
    "v_mfma_f32_16x16x32_bf16 v[48:51], a[0:3], a[104:107], v[48:51]\n"
    "ds_read_b128 a[96:99], v200 offset:40960\n"
    "ds_read_b64_tr_b16 a[48:49], v204 offset:24576\n"
    "v_mfma_f32_16x16x32_bf16 v[48:51], a[4:7], a[108:111], v[48:51]\n"
    "ds_read_b128 a[100:103], v201 offset:40960\n"
    "ds_read_b64_tr_b16 a[50:51], v204 offset:25088\n"
    "v_mfma_f32_16x16x32_bf16 v[52:55], a[8:11], a[104:107], v[52:55]\n"
    "s_add_u32 m0, s59, 0\n"
    "v_and_b32 v219, 0xffff0000, v219\n"
    "v_mfma_f32_16x16x32_bf16 v[52:55], a[12:15], a[108:111], v[52:55]\n"
    "ds_read_b64_tr_b16 a[52:53], v204 offset:28672\n"
    "s_xor_b32 s63, s60, 1536\n"
    "v_mfma_f32_16x16x32_bf16 v[56:59], a[104:107], a[16:19], v[56:59]\n"
    "buffer_load_dwordx4 v196, s[24:27], s63 offen lds\n"
    "v_lshlrev_b32 v194, 16, v227\n"
    "v_mfma_f32_16x16x32_bf16 v[56:59], a[108:111], a[20:23], v[56:59]\n"
    "ds_read_b64_tr_b16 a[54:55], v204 offset:29184\n"
    "v_mfma_f32_16x16x32_bf16 v[60:63], a[104:107], a[24:27], v[60:63]\n"
    "ds_read_b64_tr_b16 a[56:57], v205 offset:24576\n"
    "v_and_b32 v227, 0xffff0000, v227\n"
    "v_mfma_f32_16x16x32_bf16 v[60:63], a[108:111], a[28:31], v[60:63]\n"
    "buffer_load_dwordx4 v197, s[24:27], s63 offen offset:1024 lds\n"
    "v_mfma_f32_16x16x32_bf16 v[64:67], a[104:107], a[32:35], v[64:67]\n"
    "ds_read_b64_tr_b16 a[58:59], v205 offset:25088\n"
    "v_fmac_f32 v242, v193, v194\n"
    "v_mfma_f32_16x16x32_bf16 v[64:67], a[108:111], a[36:39], v[64:67]\n"
    "v_mfma_f32_16x16x32_bf16 v[68:71], a[104:107], a[40:43], v[68:71]\n"
    "ds_read_b64_tr_b16 a[60:61], v205 offset:28672\n"
    "v_fmac_f32 v242, v219, v227\n"
    "v_mfma_f32_16x16x32_bf16 v[68:71], a[108:111], a[44:47], v[68:71]\n"
    "ds_read_b64_tr_b16 a[62:63], v205 offset:29184\n"
    "s_waitcnt lgkmcnt(10)\n"
    "v_mfma_f32_16x16x32_bf16 v[72:75], a[0:3], a[112:115], v[72:75]\n"
    "ds_read_b128 a[104:107], v200 offset:43008\n"
    "v_lshlrev_b32 v194, 16, v235\n"
    "v_mfma_f32_16x16x32_bf16 v[72:75], a[4:7], a[116:119], v[72:75]\n"
    "ds_read_b128 a[108:111], v201 offset:43008\n"
    "ds_read_b64_tr_b16 a[64:65], v204 offset:32768\n"
    "v_mfma_f32_16x16x32_bf16 v[76:79], a[8:11], a[112:115], v[76:79]\n"
    "s_add_u32 m0, s61, 114688\n"
    "v_and_b32 v235, 0xffff0000, v235\n"
    "v_mfma_f32_16x16x32_bf16 v[76:79], a[12:15], a[116:119], v[76:79]\n"
    "ds_read_b64_tr_b16 a[66:67], v204 offset:33280\n"
    "s_add_u32 s63, s62, 4722688\n"
    "v_mfma_f32_16x16x32_bf16 v[80:83], a[112:115], a[16:19], v[80:83]\n"
    "ds_read_b64_tr_b16 a[68:69], v204 offset:36864\n"
    "buffer_load_dwordx4 v198, s[20:23], s63 offen lds\n"
    "v_mfma_f32_16x16x32_bf16 v[80:83], a[116:119], a[20:23], v[80:83]\n"
    "v_fmac_f32 v243, v193, v194\n"
    "v_mfma_f32_16x16x32_bf16 v[84:87], a[112:115], a[24:27], v[84:87]\n"
    "ds_read_b64_tr_b16 a[70:71], v204 offset:37376\n"
    "v_mfma_f32_16x16x32_bf16 v[84:87], a[116:119], a[28:31], v[84:87]\n"
    "buffer_load_dwordx4 v199, s[20:23], s63 offen offset:1024 lds\n"
    "v_fmac_f32 v243, v219, v235\n"
    "v_mfma_f32_16x16x32_bf16 v[88:91], a[112:115], a[32:35], v[88:91]\n"
    "ds_read_b64_tr_b16 a[72:73], v205 offset:32768\n"
    "v_mfma_f32_16x16x32_bf16 v[88:91], a[116:119], a[36:39], v[88:91]\n"
    "ds_read_b64_tr_b16 a[74:75], v205 offset:33280\n"
    "v_lshlrev_b32 v193, 16, v220\n"
    "v_mfma_f32_16x16x32_bf16 v[92:95], a[112:115], a[40:43], v[92:95]\n"
    "v_mfma_f32_16x16x32_bf16 v[92:95], a[116:119], a[44:47], v[92:95]\n"
    "ds_read_b64_tr_b16 a[76:77], v205 offset:36864\n"
    "s_waitcnt lgkmcnt(14)\n"
    "v_mfma_f32_16x16x32_bf16 v[96:99], a[0:3], a[96:99], v[96:99]\n"
    "ds_read_b128 a[112:115], v200 offset:45056\n"
    "v_and_b32 v220, 0xffff0000, v220\n"
    "v_mfma_f32_16x16x32_bf16 v[96:99], a[4:7], a[100:103], v[96:99]\n"
    "ds_read_b128 a[116:119], v201 offset:45056\n"
    "ds_read_b64_tr_b16 a[78:79], v205 offset:37376\n"
    "v_mfma_f32_16x16x32_bf16 v[100:103], a[8:11], a[96:99], v[100:103]\n"
    "ds_read_b64_tr_b16 a[80:81], v206 offset:40960\n"
    "s_add_u32 m0, s59, 2048\n"
    "v_mfma_f32_16x16x32_bf16 v[100:103], a[12:15], a[100:103], v[100:103]\n"
    "s_xor_b32 s63, s60, 34304\n"
    "v_lshlrev_b32 v194, 16, v228\n"
    "v_mfma_f32_16x16x32_bf16 v[104:107], a[96:99], a[16:19], v[104:107]\n"
    "ds_read_b64_tr_b16 a[82:83], v206 offset:41472\n"
    "buffer_load_dwordx4 v196, s[24:27], s63 offen lds\n"
    "v_mfma_f32_16x16x32_bf16 v[104:107], a[100:103], a[20:23], v[104:107]\n"
    "v_and_b32 v228, 0xffff0000, v228\n"
    "v_mfma_f32_16x16x32_bf16 v[108:111], a[96:99], a[24:27], v[108:111]\n"
    "ds_read_b64_tr_b16 a[84:85], v206 offset:45056\n"
    "v_mfma_f32_16x16x32_bf16 v[108:111], a[100:103], a[28:31], v[108:111]\n"
    "ds_read_b64_tr_b16 a[86:87], v206 offset:45568\n"
    "buffer_load_dwordx4 v197, s[24:27], s63 offen offset:1024 lds\n"
    "v_mfma_f32_16x16x32_bf16 v[112:115], a[96:99], a[32:35], v[112:115]\n"
    "v_fmac_f32 v242, v193, v194\n"
    "v_mfma_f32_16x16x32_bf16 v[112:115], a[100:103], a[36:39], v[112:115]\n"
    "ds_read_b64_tr_b16 a[88:89], v207 offset:40960\n"
    "v_mfma_f32_16x16x32_bf16 v[116:119], a[96:99], a[40:43], v[116:119]\n"
    "v_fmac_f32 v242, v220, v228\n"
    "v_mfma_f32_16x16x32_bf16 v[116:119], a[100:103], a[44:47], v[116:119]\n"
    "ds_read_b64_tr_b16 a[90:91], v207 offset:41472\n"
    "s_waitcnt lgkmcnt(14)\n"
    "v_mfma_f32_16x16x32_bf16 v[120:123], a[0:3], a[104:107], v[120:123]\n"
    "ds_read_b128 a[96:99], v200 offset:47104\n"
    "ds_read_b64_tr_b16 a[92:93], v207 offset:45056\n"
    "v_mfma_f32_16x16x32_bf16 v[120:123], a[4:7], a[108:111], v[120:123]\n"
    "ds_read_b128 a[100:103], v201 offset:47104\n"
    "s_add_u32 m0, s59, 4096\n"
    "v_mfma_f32_16x16x32_bf16 v[124:127], a[8:11], a[104:107], v[124:127]\n"
    "ds_read_b64_tr_b16 a[94:95], v207 offset:45568\n"
    "s_xor_b32 s63, s60, 67072\n"
    "v_mfma_f32_16x16x32_bf16 v[124:127], a[12:15], a[108:111], v[124:127]\n"
    "buffer_load_dwordx4 v196, s[24:27], s63 offen lds\n"
    "v_lshlrev_b32 v194, 16, v236\n"
    "v_mfma_f32_16x16x32_bf16 v[128:131], a[104:107], a[16:19], v[128:131]\n"
    "v_mfma_f32_16x16x32_bf16 v[128:131], a[108:111], a[20:23], v[128:131]\n"
    "buffer_load_dwordx4 v197, s[24:27], s63 offen offset:1024 lds\n"
    "v_and_b32 v236, 0xffff0000, v236\n"
    "v_mfma_f32_16x16x32_bf16 v[132:135], a[104:107], a[24:27], v[132:135]\n"
    "v_mfma_f32_16x16x32_bf16 v[132:135], a[108:111], a[28:31], v[132:135]\n"
    "v_fmac_f32 v243, v193, v194\n"
    "v_mfma_f32_16x16x32_bf16 v[136:139], a[104:107], a[32:35], v[136:139]\n"
    "v_mfma_f32_16x16x32_bf16 v[136:139], a[108:111], a[36:39], v[136:139]\n"
    "v_fmac_f32 v243, v220, v236\n"
    "v_mfma_f32_16x16x32_bf16 v[140:143], a[104:107], a[40:43], v[140:143]\n"
    "v_mfma_f32_16x16x32_bf16 v[140:143], a[108:111], a[44:47], v[140:143]\n"
    "s_waitcnt vmcnt(14) lgkmcnt(0)\n"
    "s_barrier\n"
    "v_mfma_f32_16x16x32_bf16 v[144:147], a[0:3], a[112:115], v[144:147]\n"
    "ds_read_b128 a[104:107], v202 offset:0\n"
    "v_lshlrev_b32 v193, 16, v221\n"
    "v_mfma_f32_16x16x32_bf16 v[144:147], a[4:7], a[116:119], v[144:147]\n"
    "ds_read_b128 a[108:111], v203 offset:0\n"
    "v_mfma_f32_16x16x32_bf16 v[148:151], a[8:11], a[112:115], v[148:151]\n"
    "s_waitcnt vmcnt(8)\n"
    "ds_read_b64_tr_b16 a[208:209], v212 offset:49152\n"
    "s_add_u32 m0, s61, 122880\n"
    "v_mfma_f32_16x16x32_bf16 v[148:151], a[12:15], a[116:119], v[148:151]\n"
    "s_add_u32 s63, s62, 5111808\n"
    "v_and_b32 v221, 0xffff0000, v221\n"
    "v_mfma_f32_16x16x32_bf16 v[152:155], a[112:115], a[16:19], v[152:155]\n"
    "ds_read_b64_tr_b16 a[210:211], v212 offset:51200\n"
    "buffer_load_dwordx4 v198, s[20:23], s63 offen lds\n"
    "v_mfma_f32_16x16x32_bf16 v[152:155], a[116:119], a[20:23], v[152:155]\n"
    "v_lshlrev_b32 v194, 16, v229\n"
    "v_mfma_f32_16x16x32_bf16 v[156:159], a[112:115], a[24:27], v[156:159]\n"
    "ds_read_b64_tr_b16 a[212:213], v213 offset:49152\n"
    "v_mfma_f32_16x16x32_bf16 v[156:159], a[116:119], a[28:31], v[156:159]\n"
    "buffer_load_dwordx4 v199, s[20:23], s63 offen offset:1024 lds\n"
    "v_and_b32 v229, 0xffff0000, v229\n"
    "v_mfma_f32_16x16x32_bf16 v[160:163], a[112:115], a[32:35], v[160:163]\n"
    "ds_read_b64_tr_b16 a[214:215], v213 offset:51200\n"
    "v_mfma_f32_16x16x32_bf16 v[160:163], a[116:119], a[36:39], v[160:163]\n"
    "v_fmac_f32 v242, v193, v194\n"
    "v_mfma_f32_16x16x32_bf16 v[164:167], a[112:115], a[40:43], v[164:167]\n"
    "ds_read_b64_tr_b16 a[216:217], v214 offset:49152\n"
    "v_mfma_f32_16x16x32_bf16 v[164:167], a[116:119], a[44:47], v[164:167]\n"
    "v_fmac_f32 v242, v221, v229\n"
    "v_mfma_f32_16x16x32_bf16 v[168:171], a[0:3], a[96:99], v[168:171]\n"
    "ds_read_b128 a[112:115], v202 offset:2048\n"
    "ds_read_b64_tr_b16 a[218:219], v214 offset:51200\n"
    "v_mfma_f32_16x16x32_bf16 v[168:171], a[4:7], a[100:103], v[168:171]\n"
    "ds_read_b128 a[116:119], v203 offset:2048\n"
    "s_add_u32 m0, s59, 6144\n"
    "v_mfma_f32_16x16x32_bf16 v[172:175], a[8:11], a[96:99], v[172:175]\n"
    "ds_read_b64_tr_b16 a[220:221], v215 offset:49152\n"
    "s_xor_b32 s63, s60, 99840\n"
    "v_mfma_f32_16x16x32_bf16 v[172:175], a[12:15], a[100:103], v[172:175]\n"
    "buffer_load_dwordx4 v196, s[24:27], s63 offen lds\n"
    "v_lshlrev_b32 v194, 16, v237\n"
    "v_mfma_f32_16x16x32_bf16 v[176:179], a[96:99], a[16:19], v[176:179]\n"
    "ds_read_b64_tr_b16 a[222:223], v215 offset:51200\n"
    "v_mfma_f32_16x16x32_bf16 v[176:179], a[100:103], a[20:23], v[176:179]\n"
    "v_and_b32 v237, 0xffff0000, v237\n"
    "v_mfma_f32_16x16x32_bf16 v[180:183], a[96:99], a[24:27], v[180:183]\n"
    "buffer_load_dwordx4 v197, s[24:27], s63 offen offset:1024 lds\n"
    "v_mfma_f32_16x16x32_bf16 v[180:183], a[100:103], a[28:31], v[180:183]\n"
    "s_cmp_eq_u32 s81, 2\n"
    "s_cbranch_scc0 Lpf10%=\n"
    "v_lshl_add_u64 v[244:245], v[246:247], 0, v[244:245]\n"
    "global_load_dword v249, v[244:245], off\n"
    "Lpf10%=:\n"
    "v_mfma_f32_16x16x32_bf16 v[184:187], a[96:99], a[32:35], v[184:187]\n"
    "v_fmac_f32 v243, v193, v194\n"
    "v_mfma_f32_16x16x32_bf16 v[184:187], a[100:103], a[36:39], v[184:187]\n"
    "v_mfma_f32_16x16x32_bf16 v[188:191], a[96:99], a[40:43], v[188:191]\n"
    "v_fmac_f32 v243, v221, v237\n"
    "v_mfma_f32_16x16x32_bf16 v[188:191], a[100:103], a[44:47], v[188:191]\n"
    "s_waitcnt lgkmcnt(10)\n"
    "v_mfma_f32_16x16x32_bf16 v[0:3], a[48:51], a[104:107], v[0:3]\n"
    "ds_read_b128 a[96:99], v202 offset:4096\n"
    "s_waitcnt lgkmcnt(1)\n"
    "s_add_u32 s63, s65, 196608\n"
    "v_mfma_f32_16x16x32_bf16 v[0:3], a[52:55], a[108:111], v[0:3]\n"
    "ds_read_b128 a[100:103], v203 offset:4096\n"
    "s_and_b32 s63, s63, 0x7ffff\n"
    "v_mfma_f32_16x16x32_bf16 v[4:7], a[56:59], a[104:107], v[4:7]\n"
    "s_add_u32 m0, s64, 147456\n"
    "v_mfma_f32_16x16x32_bf16 v[4:7], a[60:63], a[108:111], v[4:7]\n"
    "buffer_load_dwordx4 v208, s[32:35], s63 offen offset:0 lds\n"
    "v_mfma_f32_16x16x32_bf16 v[8:11], a[104:107], a[64:67], v[8:11]\n"
    "buffer_load_dwordx4 v209, s[32:35], s63 offen offset:1024 lds\n"
    "v_mfma_f32_16x16x32_bf16 v[8:11], a[108:111], a[68:71], v[8:11]\n"
    "v_mfma_f32_16x16x32_bf16 v[12:15], a[104:107], a[72:75], v[12:15]\n"
    "v_mfma_f32_16x16x32_bf16 v[12:15], a[108:111], a[76:79], v[12:15]\n"
    "v_mfma_f32_16x16x32_bf16 v[16:19], a[104:107], a[80:83], v[16:19]\n"
    "s_add_u32 m0, s61, 131072\n"
    "v_mfma_f32_16x16x32_bf16 v[16:19], a[108:111], a[84:87], v[16:19]\n"
    "s_add_u32 s63, s62, 5113856\n"
    "v_mfma_f32_16x16x32_bf16 v[20:23], a[104:107], a[88:91], v[20:23]\n"
    "buffer_load_dwordx4 v198, s[20:23], s63 offen lds\n"
    "v_mfma_f32_16x16x32_bf16 v[20:23], a[108:111], a[92:95], v[20:23]\n"
    "v_mfma_f32_16x16x32_bf16 v[24:27], a[48:51], a[112:115], v[24:27]\n"
    "ds_read_b128 a[104:107], v202 offset:6144\n"
    "v_mfma_f32_16x16x32_bf16 v[24:27], a[52:55], a[116:119], v[24:27]\n"
    "ds_read_b128 a[108:111], v203 offset:6144\n"
    "buffer_load_dwordx4 v199, s[20:23], s63 offen offset:1024 lds\n"
    "v_mfma_f32_16x16x32_bf16 v[28:31], a[56:59], a[112:115], v[28:31]\n"
    "v_mfma_f32_16x16x32_bf16 v[28:31], a[60:63], a[116:119], v[28:31]\n"
    "v_mfma_f32_16x16x32_bf16 v[32:35], a[112:115], a[64:67], v[32:35]\n"
    "s_add_u32 s63, s65, 196608\n"
    "v_mfma_f32_16x16x32_bf16 v[32:35], a[116:119], a[68:71], v[32:35]\n"
    "s_and_b32 s63, s63, 0x7ffff\n"
    "v_mfma_f32_16x16x32_bf16 v[36:39], a[112:115], a[72:75], v[36:39]\n"
    "s_add_u32 m0, s64, 147456\n"
    "v_mfma_f32_16x16x32_bf16 v[36:39], a[116:119], a[76:79], v[36:39]\n"
    "buffer_load_dwordx4 v210, s[32:35], s63 offen offset:2048 lds\n"
    "v_mfma_f32_16x16x32_bf16 v[40:43], a[112:115], a[80:83], v[40:43]\n"
    "buffer_load_dwordx4 v211, s[32:35], s63 offen offset:3072 lds\n"
    "v_mfma_f32_16x16x32_bf16 v[40:43], a[116:119], a[84:87], v[40:43]\n"
    "v_mfma_f32_16x16x32_bf16 v[44:47], a[112:115], a[88:91], v[44:47]\n"
    "v_mfma_f32_16x16x32_bf16 v[44:47], a[116:119], a[92:95], v[44:47]\n"
    "s_waitcnt lgkmcnt(2)\n"
    "s_waitcnt vmcnt(14)\n"
    "s_barrier\n"
    "v_mfma_f32_16x16x32_bf16 v[48:51], a[48:51], a[96:99], v[48:51]\n"
    "ds_read_b128 a[112:115], v202 offset:8192\n"
    "ds_read_b64_tr_b16 a[0:1], v204 offset:0\n"
    "v_mfma_f32_16x16x32_bf16 v[48:51], a[52:55], a[100:103], v[48:51]\n"
    "ds_read_b128 a[116:119], v203 offset:8192\n"
    "ds_read_b64_tr_b16 a[2:3], v204 offset:512\n"
    "v_mfma_f32_16x16x32_bf16 v[52:55], a[56:59], a[96:99], v[52:55]\n"
    "s_add_u32 m0, s59, 32768\n"
    "v_mfma_f32_16x16x32_bf16 v[52:55], a[60:63], a[100:103], v[52:55]\n"
    "ds_read_b64_tr_b16 a[4:5], v204 offset:4096\n"
    "s_xor_b32 s63, s60, 1664\n"
    "v_mfma_f32_16x16x32_bf16 v[56:59], a[96:99], a[64:67], v[56:59]\n"
    "buffer_load_dwordx4 v196, s[24:27], s63 offen lds\n"
    "v_mfma_f32_16x16x32_bf16 v[56:59], a[100:103], a[68:71], v[56:59]\n"
    "ds_read_b64_tr_b16 a[6:7], v204 offset:4608\n"
    "v_mfma_f32_16x16x32_bf16 v[60:63], a[96:99], a[72:75], v[60:63]\n"
    "ds_read_b64_tr_b16 a[8:9], v205 offset:0\n"
    "v_mfma_f32_16x16x32_bf16 v[60:63], a[100:103], a[76:79], v[60:63]\n"
    "buffer_load_dwordx4 v197, s[24:27], s63 offen offset:1024 lds\n"
    "v_mfma_f32_16x16x32_bf16 v[64:67], a[96:99], a[80:83], v[64:67]\n"
    "ds_read_b64_tr_b16 a[10:11], v205 offset:512\n"
    "v_mfma_f32_16x16x32_bf16 v[64:67], a[100:103], a[84:87], v[64:67]\n"
    "v_mfma_f32_16x16x32_bf16 v[68:71], a[96:99], a[88:91], v[68:71]\n"
    "ds_read_b64_tr_b16 a[12:13], v205 offset:4096\n"
    "v_mfma_f32_16x16x32_bf16 v[68:71], a[100:103], a[92:95], v[68:71]\n"
    "ds_read_b64_tr_b16 a[14:15], v205 offset:4608\n"
    "s_waitcnt lgkmcnt(10)\n"
    "v_mfma_f32_16x16x32_bf16 v[72:75], a[48:51], a[104:107], v[72:75]\n"
    "ds_read_b128 a[96:99], v202 offset:10240\n"
    "v_mfma_f32_16x16x32_bf16 v[72:75], a[52:55], a[108:111], v[72:75]\n"
    "ds_read_b128 a[100:103], v203 offset:10240\n"
    "ds_read_b64_tr_b16 a[16:17], v204 offset:8192\n"
    "v_mfma_f32_16x16x32_bf16 v[76:79], a[56:59], a[104:107], v[76:79]\n"
    "s_add_u32 m0, s61, 139264\n"
    "v_mfma_f32_16x16x32_bf16 v[76:79], a[60:63], a[108:111], v[76:79]\n"
    "ds_read_b64_tr_b16 a[18:19], v204 offset:8704\n"
    "s_add_u32 s63, s62, 5115904\n"
    "v_mfma_f32_16x16x32_bf16 v[80:83], a[104:107], a[64:67], v[80:83]\n"
    "ds_read_b64_tr_b16 a[20:21], v204 offset:12288\n"
    "buffer_load_dwordx4 v198, s[20:23], s63 offen lds\n"
    "v_mfma_f32_16x16x32_bf16 v[80:83], a[108:111], a[68:71], v[80:83]\n"
    "v_mfma_f32_16x16x32_bf16 v[84:87], a[104:107], a[72:75], v[84:87]\n"
    "ds_read_b64_tr_b16 a[22:23], v204 offset:12800\n"
    "v_mfma_f32_16x16x32_bf16 v[84:87], a[108:111], a[76:79], v[84:87]\n"
    "buffer_load_dwordx4 v199, s[20:23], s63 offen offset:1024 lds\n"
    "v_mfma_f32_16x16x32_bf16 v[88:91], a[104:107], a[80:83], v[88:91]\n"
    "ds_read_b64_tr_b16 a[24:25], v205 offset:8192\n"
    "v_mfma_f32_16x16x32_bf16 v[88:91], a[108:111], a[84:87], v[88:91]\n"
    "ds_read_b64_tr_b16 a[26:27], v205 offset:8704\n"
    "v_mfma_f32_16x16x32_bf16 v[92:95], a[104:107], a[88:91], v[92:95]\n"
    "v_mfma_f32_16x16x32_bf16 v[92:95], a[108:111], a[92:95], v[92:95]\n"
    "ds_read_b64_tr_b16 a[28:29], v205 offset:12288\n"
    "s_waitcnt lgkmcnt(14)\n"
    "v_mfma_f32_16x16x32_bf16 v[96:99], a[48:51], a[112:115], v[96:99]\n"
    "ds_read_b128 a[104:107], v202 offset:12288\n"
    "v_mfma_f32_16x16x32_bf16 v[96:99], a[52:55], a[116:119], v[96:99]\n"
    "ds_read_b128 a[108:111], v203 offset:12288\n"
    "ds_read_b64_tr_b16 a[30:31], v205 offset:12800\n"
    "v_mfma_f32_16x16x32_bf16 v[100:103], a[56:59], a[112:115], v[100:103]\n"
    "ds_read_b64_tr_b16 a[32:33], v206 offset:16384\n"
    "s_add_u32 m0, s59, 34816\n"
    "v_mfma_f32_16x16x32_bf16 v[100:103], a[60:63], a[116:119], v[100:103]\n"
    "s_xor_b32 s63, s60, 34432\n"
    "v_mfma_f32_16x16x32_bf16 v[104:107], a[112:115], a[64:67], v[104:107]\n"
    "ds_read_b64_tr_b16 a[34:35], v206 offset:16896\n"
    "buffer_load_dwordx4 v196, s[24:27], s63 offen lds\n"
    "v_mfma_f32_16x16x32_bf16 v[104:107], a[116:119], a[68:71], v[104:107]\n"
    "v_mfma_f32_16x16x32_bf16 v[108:111], a[112:115], a[72:75], v[108:111]\n"
    "ds_read_b64_tr_b16 a[36:37], v206 offset:20480\n"
    "v_mfma_f32_16x16x32_bf16 v[108:111], a[116:119], a[76:79], v[108:111]\n"
    "ds_read_b64_tr_b16 a[38:39], v206 offset:20992\n"
    "buffer_load_dwordx4 v197, s[24:27], s63 offen offset:1024 lds\n"
    "v_mfma_f32_16x16x32_bf16 v[112:115], a[112:115], a[80:83], v[112:115]\n"
    "v_mfma_f32_16x16x32_bf16 v[112:115], a[116:119], a[84:87], v[112:115]\n"
    "ds_read_b64_tr_b16 a[40:41], v207 offset:16384\n"
    "v_mfma_f32_16x16x32_bf16 v[116:119], a[112:115], a[88:91], v[116:119]\n"
    "v_mfma_f32_16x16x32_bf16 v[116:119], a[116:119], a[92:95], v[116:119]\n"
    "ds_read_b64_tr_b16 a[42:43], v207 offset:16896\n"
    "s_waitcnt lgkmcnt(14)\n"
    "v_mfma_f32_16x16x32_bf16 v[120:123], a[48:51], a[96:99], v[120:123]\n"
    "ds_read_b128 a[112:115], v202 offset:14336\n"
    "ds_read_b64_tr_b16 a[44:45], v207 offset:20480\n"
    "v_mfma_f32_16x16x32_bf16 v[120:123], a[52:55], a[100:103], v[120:123]\n"
    "ds_read_b128 a[116:119], v203 offset:14336\n"
    "s_add_u32 m0, s59, 36864\n"
    "v_mfma_f32_16x16x32_bf16 v[124:127], a[56:59], a[96:99], v[124:127]\n"
    "ds_read_b64_tr_b16 a[46:47], v207 offset:20992\n"
    "s_xor_b32 s63, s60, 67200\n"
    "v_mfma_f32_16x16x32_bf16 v[124:127], a[60:63], a[100:103], v[124:127]\n"
    "buffer_load_dwordx4 v196, s[24:27], s63 offen lds\n"
    "v_mfma_f32_16x16x32_bf16 v[128:131], a[96:99], a[64:67], v[128:131]\n"
    "v_mfma_f32_16x16x32_bf16 v[128:131], a[100:103], a[68:71], v[128:131]\n"
    "buffer_load_dwordx4 v197, s[24:27], s63 offen offset:1024 lds\n"
    "v_mfma_f32_16x16x32_bf16 v[132:135], a[96:99], a[72:75], v[132:135]\n"
    "v_mfma_f32_16x16x32_bf16 v[132:135], a[100:103], a[76:79], v[132:135]\n"
    "v_mfma_f32_16x16x32_bf16 v[136:139], a[96:99], a[80:83], v[136:139]\n"
    "v_mfma_f32_16x16x32_bf16 v[136:139], a[100:103], a[84:87], v[136:139]\n"
    "v_mfma_f32_16x16x32_bf16 v[140:143], a[96:99], a[88:91], v[140:143]\n"
    "v_mfma_f32_16x16x32_bf16 v[140:143], a[100:103], a[92:95], v[140:143]\n"
    "s_waitcnt vmcnt(14) lgkmcnt(0)\n"
    "s_barrier\n"
    "v_mfma_f32_16x16x32_bf16 v[144:147], a[48:51], a[104:107], v[144:147]\n"
    "ds_read_b128 a[96:99], v200 offset:0\n"
    "v_mfma_f32_16x16x32_bf16 v[144:147], a[52:55], a[108:111], v[144:147]\n"
    "ds_read_b128 a[100:103], v201 offset:0\n"
    "v_mfma_f32_16x16x32_bf16 v[148:151], a[56:59], a[104:107], v[148:151]\n"
    "s_waitcnt vmcnt(8)\n"
    "ds_read_b64_tr_b16 a[224:225], v212 offset:49152\n"
    "s_add_u32 m0, s61, 98304\n"
    "v_mfma_f32_16x16x32_bf16 v[148:151], a[60:63], a[108:111], v[148:151]\n"
    "s_add_u32 s63, s62, 5505024\n"
    "v_mfma_f32_16x16x32_bf16 v[152:155], a[104:107], a[64:67], v[152:155]\n"
    "ds_read_b64_tr_b16 a[226:227], v212 offset:51200\n"
    "buffer_load_dwordx4 v198, s[20:23], s63 offen lds\n"
    "v_mfma_f32_16x16x32_bf16 v[152:155], a[108:111], a[68:71], v[152:155]\n"
    "v_mfma_f32_16x16x32_bf16 v[156:159], a[104:107], a[72:75], v[156:159]\n"
    "ds_read_b64_tr_b16 a[228:229], v213 offset:49152\n"
    "v_mfma_f32_16x16x32_bf16 v[156:159], a[108:111], a[76:79], v[156:159]\n"
    "buffer_load_dwordx4 v199, s[20:23], s63 offen offset:1024 lds\n"
    "v_mfma_f32_16x16x32_bf16 v[160:163], a[104:107], a[80:83], v[160:163]\n"
    "ds_read_b64_tr_b16 a[230:231], v213 offset:51200\n"
    "v_mfma_f32_16x16x32_bf16 v[160:163], a[108:111], a[84:87], v[160:163]\n"
    "v_mfma_f32_16x16x32_bf16 v[164:167], a[104:107], a[88:91], v[164:167]\n"
    "ds_read_b64_tr_b16 a[232:233], v214 offset:49152\n"
    "v_mfma_f32_16x16x32_bf16 v[164:167], a[108:111], a[92:95], v[164:167]\n"
    "v_mfma_f32_16x16x32_bf16 v[168:171], a[48:51], a[112:115], v[168:171]\n"
    "ds_read_b128 a[104:107], v200 offset:2048\n"
    "ds_read_b64_tr_b16 a[234:235], v214 offset:51200\n"
    "v_mfma_f32_16x16x32_bf16 v[168:171], a[52:55], a[116:119], v[168:171]\n"
    "ds_read_b128 a[108:111], v201 offset:2048\n"
    "s_add_u32 m0, s59, 38912\n"
    "v_mfma_f32_16x16x32_bf16 v[172:175], a[56:59], a[112:115], v[172:175]\n"
    "ds_read_b64_tr_b16 a[236:237], v215 offset:49152\n"
    "s_xor_b32 s63, s60, 99968\n"
    "v_mfma_f32_16x16x32_bf16 v[172:175], a[60:63], a[116:119], v[172:175]\n"
    "buffer_load_dwordx4 v196, s[24:27], s63 offen lds\n"
    "v_mfma_f32_16x16x32_bf16 v[176:179], a[112:115], a[64:67], v[176:179]\n"
    "ds_read_b64_tr_b16 a[238:239], v215 offset:51200\n"
    "v_mfma_f32_16x16x32_bf16 v[176:179], a[116:119], a[68:71], v[176:179]\n"
    "v_mfma_f32_16x16x32_bf16 v[180:183], a[112:115], a[72:75], v[180:183]\n"
    "buffer_load_dwordx4 v197, s[24:27], s63 offen offset:1024 lds\n"
    "v_mfma_f32_16x16x32_bf16 v[180:183], a[116:119], a[76:79], v[180:183]\n"
    "s_cmp_eq_u32 s81, 3\n"
    "s_cbranch_scc0 Lpf11%=\n"
    "v_lshl_add_u64 v[244:245], v[246:247], 0, v[244:245]\n"
    "global_load_dword v249, v[244:245], off\n"
    "Lpf11%=:\n"
    "v_mfma_f32_16x16x32_bf16 v[184:187], a[112:115], a[80:83], v[184:187]\n"
    "v_mfma_f32_16x16x32_bf16 v[184:187], a[116:119], a[84:87], v[184:187]\n"
    "v_mfma_f32_16x16x32_bf16 v[188:191], a[112:115], a[88:91], v[188:191]\n"
    "v_mfma_f32_16x16x32_bf16 v[188:191], a[116:119], a[92:95], v[188:191]\n"
    "s_waitcnt lgkmcnt(10)\n"
    "v_mfma_f32_16x16x32_bf16 v[0:3], a[0:3], a[96:99], v[0:3]\n"
    "ds_read_b128 a[112:115], v200 offset:4096\n"
    "s_waitcnt lgkmcnt(1)\n"
    "s_add_u32 s63, s65, 196736\n"
    "v_mfma_f32_16x16x32_bf16 v[0:3], a[4:7], a[100:103], v[0:3]\n"
    "ds_read_b128 a[116:119], v201 offset:4096\n"
    "s_and_b32 s63, s63, 0x7ffff\n"
    "v_mfma_f32_16x16x32_bf16 v[4:7], a[8:11], a[96:99], v[4:7]\n"
    "s_add_u32 m0, s64, 147456\n"
    "v_mfma_f32_16x16x32_bf16 v[4:7], a[12:15], a[100:103], v[4:7]\n"
    "buffer_load_dwordx4 v208, s[32:35], s63 offen offset:0 lds\n"
    "v_mfma_f32_16x16x32_bf16 v[8:11], a[96:99], a[16:19], v[8:11]\n"
    "buffer_load_dwordx4 v209, s[32:35], s63 offen offset:1024 lds\n"
    "v_mfma_f32_16x16x32_bf16 v[8:11], a[100:103], a[20:23], v[8:11]\n"
    "v_mfma_f32_16x16x32_bf16 v[12:15], a[96:99], a[24:27], v[12:15]\n"
    "v_mfma_f32_16x16x32_bf16 v[12:15], a[100:103], a[28:31], v[12:15]\n"
    "v_mfma_f32_16x16x32_bf16 v[16:19], a[96:99], a[32:35], v[16:19]\n"
    "s_add_u32 m0, s61, 106496\n"
    "v_mfma_f32_16x16x32_bf16 v[16:19], a[100:103], a[36:39], v[16:19]\n"
    "s_add_u32 s63, s62, 5507072\n"
    "v_mfma_f32_16x16x32_bf16 v[20:23], a[96:99], a[40:43], v[20:23]\n"
    "buffer_load_dwordx4 v198, s[20:23], s63 offen lds\n"
    "v_mfma_f32_16x16x32_bf16 v[20:23], a[100:103], a[44:47], v[20:23]\n"
    "v_mfma_f32_16x16x32_bf16 v[24:27], a[0:3], a[104:107], v[24:27]\n"
    "ds_read_b128 a[96:99], v200 offset:6144\n"
    "v_mfma_f32_16x16x32_bf16 v[24:27], a[4:7], a[108:111], v[24:27]\n"
    "ds_read_b128 a[100:103], v201 offset:6144\n"
    "buffer_load_dwordx4 v199, s[20:23], s63 offen offset:1024 lds\n"
    "v_mfma_f32_16x16x32_bf16 v[28:31], a[8:11], a[104:107], v[28:31]\n"
    "v_mfma_f32_16x16x32_bf16 v[28:31], a[12:15], a[108:111], v[28:31]\n"
    "v_mfma_f32_16x16x32_bf16 v[32:35], a[104:107], a[16:19], v[32:35]\n"
    "s_add_u32 s63, s65, 196736\n"
    "v_mfma_f32_16x16x32_bf16 v[32:35], a[108:111], a[20:23], v[32:35]\n"
    "s_and_b32 s63, s63, 0x7ffff\n"
    "v_mfma_f32_16x16x32_bf16 v[36:39], a[104:107], a[24:27], v[36:39]\n"
    "s_add_u32 m0, s64, 147456\n"
    "v_mfma_f32_16x16x32_bf16 v[36:39], a[108:111], a[28:31], v[36:39]\n"
    "buffer_load_dwordx4 v210, s[32:35], s63 offen offset:2048 lds\n"
    "v_mfma_f32_16x16x32_bf16 v[40:43], a[104:107], a[32:35], v[40:43]\n"
    "buffer_load_dwordx4 v211, s[32:35], s63 offen offset:3072 lds\n"
    "v_mfma_f32_16x16x32_bf16 v[40:43], a[108:111], a[36:39], v[40:43]\n"
    "v_mfma_f32_16x16x32_bf16 v[44:47], a[104:107], a[40:43], v[44:47]\n"
    "v_mfma_f32_16x16x32_bf16 v[44:47], a[108:111], a[44:47], v[44:47]\n"
    "s_waitcnt lgkmcnt(2)\n"
    "s_waitcnt vmcnt(14)\n"
    "s_barrier\n"
    "v_mfma_f32_16x16x32_bf16 v[48:51], a[0:3], a[112:115], v[48:51]\n"
    "ds_read_b128 a[104:107], v200 offset:8192\n"
    "ds_read_b64_tr_b16 a[48:49], v204 offset:24576\n"
    "v_mfma_f32_16x16x32_bf16 v[48:51], a[4:7], a[116:119], v[48:51]\n"
    "ds_read_b128 a[108:111], v201 offset:8192\n"
    "ds_read_b64_tr_b16 a[50:51], v204 offset:25088\n"
    "v_mfma_f32_16x16x32_bf16 v[52:55], a[8:11], a[112:115], v[52:55]\n"
    "s_add_u32 m0, s59, 65536\n"
    "v_mfma_f32_16x16x32_bf16 v[52:55], a[12:15], a[116:119], v[52:55]\n"
    "ds_read_b64_tr_b16 a[52:53], v204 offset:28672\n"
    "s_xor_b32 s63, s60, 1792\n"
    "v_mfma_f32_16x16x32_bf16 v[56:59], a[112:115], a[16:19], v[56:59]\n"
    "buffer_load_dwordx4 v196, s[24:27], s63 offen lds\n"
    "v_mfma_f32_16x16x32_bf16 v[56:59], a[116:119], a[20:23], v[56:59]\n"
    "ds_read_b64_tr_b16 a[54:55], v204 offset:29184\n"
    "v_mfma_f32_16x16x32_bf16 v[60:63], a[112:115], a[24:27], v[60:63]\n"
    "ds_read_b64_tr_b16 a[56:57], v205 offset:24576\n"
    "v_mfma_f32_16x16x32_bf16 v[60:63], a[116:119], a[28:31], v[60:63]\n"
    "buffer_load_dwordx4 v197, s[24:27], s63 offen offset:1024 lds\n"
    "v_mfma_f32_16x16x32_bf16 v[64:67], a[112:115], a[32:35], v[64:67]\n"
    "ds_read_b64_tr_b16 a[58:59], v205 offset:25088\n"
    "v_mfma_f32_16x16x32_bf16 v[64:67], a[116:119], a[36:39], v[64:67]\n"
    "buffer_load_dwordx4 v[230:233], v216, s[24:27], s72 offen offset:1024\n"
    "v_mfma_f32_16x16x32_bf16 v[68:71], a[112:115], a[40:43], v[68:71]\n"
    "ds_read_b64_tr_b16 a[60:61], v205 offset:28672\n"
    "v_mfma_f32_16x16x32_bf16 v[68:71], a[116:119], a[44:47], v[68:71]\n"
    "ds_read_b64_tr_b16 a[62:63], v205 offset:29184\n"
    "s_waitcnt lgkmcnt(10)\n"
    "v_mfma_f32_16x16x32_bf16 v[72:75], a[0:3], a[96:99], v[72:75]\n"
    "ds_read_b128 a[112:115], v200 offset:10240\n"
    "v_mfma_f32_16x16x32_bf16 v[72:75], a[4:7], a[100:103], v[72:75]\n"
    "ds_read_b128 a[116:119], v201 offset:10240\n"
    "ds_read_b64_tr_b16 a[64:65], v204 offset:32768\n"
    "v_mfma_f32_16x16x32_bf16 v[76:79], a[8:11], a[96:99], v[76:79]\n"
    "s_add_u32 m0, s61, 114688\n"
    "v_mfma_f32_16x16x32_bf16 v[76:79], a[12:15], a[100:103], v[76:79]\n"
    "ds_read_b64_tr_b16 a[66:67], v204 offset:33280\n"
    "s_add_u32 s63, s62, 5509120\n"
    "v_mfma_f32_16x16x32_bf16 v[80:83], a[96:99], a[16:19], v[80:83]\n"
    "ds_read_b64_tr_b16 a[68:69], v204 offset:36864\n"
    "buffer_load_dwordx4 v198, s[20:23], s63 offen lds\n"
    "v_mfma_f32_16x16x32_bf16 v[80:83], a[100:103], a[20:23], v[80:83]\n"
    "v_mfma_f32_16x16x32_bf16 v[84:87], a[96:99], a[24:27], v[84:87]\n"
    "ds_read_b64_tr_b16 a[70:71], v204 offset:37376\n"
    "v_mfma_f32_16x16x32_bf16 v[84:87], a[100:103], a[28:31], v[84:87]\n"
    "buffer_load_dwordx4 v199, s[20:23], s63 offen offset:1024 lds\n"
    "v_mfma_f32_16x16x32_bf16 v[88:91], a[96:99], a[32:35], v[88:91]\n"
    "ds_read_b64_tr_b16 a[72:73], v205 offset:32768\n"
    "v_mfma_f32_16x16x32_bf16 v[88:91], a[100:103], a[36:39], v[88:91]\n"
    "ds_read_b64_tr_b16 a[74:75], v205 offset:33280\n"
    "v_mfma_f32_16x16x32_bf16 v[92:95], a[96:99], a[40:43], v[92:95]\n"
    "v_mfma_f32_16x16x32_bf16 v[92:95], a[100:103], a[44:47], v[92:95]\n"
    "ds_read_b64_tr_b16 a[76:77], v205 offset:36864\n"
    "s_waitcnt lgkmcnt(14)\n"
    "v_mfma_f32_16x16x32_bf16 v[96:99], a[0:3], a[104:107], v[96:99]\n"
    "ds_read_b128 a[96:99], v200 offset:12288\n"
    "v_mfma_f32_16x16x32_bf16 v[96:99], a[4:7], a[108:111], v[96:99]\n"
    "ds_read_b128 a[100:103], v201 offset:12288\n"
    "ds_read_b64_tr_b16 a[78:79], v205 offset:37376\n"
    "v_mfma_f32_16x16x32_bf16 v[100:103], a[8:11], a[104:107], v[100:103]\n"
    "ds_read_b64_tr_b16 a[80:81], v206 offset:40960\n"
    "s_add_u32 m0, s59, 67584\n"
    "v_mfma_f32_16x16x32_bf16 v[100:103], a[12:15], a[108:111], v[100:103]\n"
    "s_xor_b32 s63, s60, 34560\n"
    "v_mfma_f32_16x16x32_bf16 v[104:107], a[104:107], a[16:19], v[104:107]\n"
    "ds_read_b64_tr_b16 a[82:83], v206 offset:41472\n"
    "buffer_load_dwordx4 v196, s[24:27], s63 offen lds\n"
    "v_mfma_f32_16x16x32_bf16 v[104:107], a[108:111], a[20:23], v[104:107]\n"
    "v_mfma_f32_16x16x32_bf16 v[108:111], a[104:107], a[24:27], v[108:111]\n"
    "ds_read_b64_tr_b16 a[84:85], v206 offset:45056\n"
    "v_mfma_f32_16x16x32_bf16 v[108:111], a[108:111], a[28:31], v[108:111]\n"
    "ds_read_b64_tr_b16 a[86:87], v206 offset:45568\n"
    "buffer_load_dwordx4 v197, s[24:27], s63 offen offset:1024 lds\n"
    "v_mfma_f32_16x16x32_bf16 v[112:115], a[104:107], a[32:35], v[112:115]\n"
    "buffer_load_dwordx4 v[238:241], v216, s[24:27], s72 offen offset:3072\n"
    "v_mfma_f32_16x16x32_bf16 v[112:115], a[108:111], a[36:39], v[112:115]\n"
    "ds_read_b64_tr_b16 a[88:89], v207 offset:40960\n"
    "v_mfma_f32_16x16x32_bf16 v[116:119], a[104:107], a[40:43], v[116:119]\n"
    "v_mfma_f32_16x16x32_bf16 v[116:119], a[108:111], a[44:47], v[116:119]\n"
    "ds_read_b64_tr_b16 a[90:91], v207 offset:41472\n"
    "s_waitcnt lgkmcnt(14)\n"
    "v_mfma_f32_16x16x32_bf16 v[120:123], a[0:3], a[112:115], v[120:123]\n"
    "ds_read_b128 a[104:107], v200 offset:14336\n"
    "ds_read_b64_tr_b16 a[92:93], v207 offset:45056\n"
    "v_mfma_f32_16x16x32_bf16 v[120:123], a[4:7], a[116:119], v[120:123]\n"
    "ds_read_b128 a[108:111], v201 offset:14336\n"
    "s_add_u32 m0, s59, 69632\n"
    "v_mfma_f32_16x16x32_bf16 v[124:127], a[8:11], a[112:115], v[124:127]\n"
    "ds_read_b64_tr_b16 a[94:95], v207 offset:45568\n"
    "s_xor_b32 s63, s60, 67328\n"
    "v_mfma_f32_16x16x32_bf16 v[124:127], a[12:15], a[116:119], v[124:127]\n"
    "buffer_load_dwordx4 v196, s[24:27], s63 offen lds\n"
    "v_mfma_f32_16x16x32_bf16 v[128:131], a[112:115], a[16:19], v[128:131]\n"
    "v_mfma_f32_16x16x32_bf16 v[128:131], a[116:119], a[20:23], v[128:131]\n"
    "buffer_load_dwordx4 v197, s[24:27], s63 offen offset:1024 lds\n"
    "v_mfma_f32_16x16x32_bf16 v[132:135], a[112:115], a[24:27], v[132:135]\n"
    "v_mfma_f32_16x16x32_bf16 v[132:135], a[116:119], a[28:31], v[132:135]\n"
    "v_mfma_f32_16x16x32_bf16 v[136:139], a[112:115], a[32:35], v[136:139]\n"
    "v_mfma_f32_16x16x32_bf16 v[136:139], a[116:119], a[36:39], v[136:139]\n"
    "v_mfma_f32_16x16x32_bf16 v[140:143], a[112:115], a[40:43], v[140:143]\n"
    "v_mfma_f32_16x16x32_bf16 v[140:143], a[116:119], a[44:47], v[140:143]\n"
    "s_waitcnt vmcnt(16) lgkmcnt(0)\n"
    "s_barrier\n"
    "v_mfma_f32_16x16x32_bf16 v[144:147], a[0:3], a[96:99], v[144:147]\n"
    "ds_read_b128 a[112:115], v200 offset:32768\n"
    "v_mfma_f32_16x16x32_bf16 v[144:147], a[4:7], a[100:103], v[144:147]\n"
    "ds_read_b128 a[116:119], v201 offset:32768\n"
    "v_mfma_f32_16x16x32_bf16 v[148:151], a[8:11], a[96:99], v[148:151]\n"
    "s_waitcnt vmcnt(10)\n"
    "ds_read_b64_tr_b16 a[240:241], v212 offset:49152\n"
    "s_add_u32 m0, s61, 122880\n"
    "v_mfma_f32_16x16x32_bf16 v[148:151], a[12:15], a[100:103], v[148:151]\n"
    "s_add_u32 s63, s62, 5898240\n"
    "v_mfma_f32_16x16x32_bf16 v[152:155], a[96:99], a[16:19], v[152:155]\n"
    "ds_read_b64_tr_b16 a[242:243], v212 offset:51200\n"
    "buffer_load_dwordx4 v198, s[20:23], s63 offen lds\n"
    "v_mfma_f32_16x16x32_bf16 v[152:155], a[100:103], a[20:23], v[152:155]\n"
    "v_mfma_f32_16x16x32_bf16 v[156:159], a[96:99], a[24:27], v[156:159]\n"
    "ds_read_b64_tr_b16 a[244:245], v213 offset:49152\n"
    "v_mfma_f32_16x16x32_bf16 v[156:159], a[100:103], a[28:31], v[156:159]\n"
    "buffer_load_dwordx4 v199, s[20:23], s63 offen offset:1024 lds\n"
    "v_mfma_f32_16x16x32_bf16 v[160:163], a[96:99], a[32:35], v[160:163]\n"
    "ds_read_b64_tr_b16 a[246:247], v213 offset:51200\n"
    "v_mfma_f32_16x16x32_bf16 v[160:163], a[100:103], a[36:39], v[160:163]\n"
    "v_mfma_f32_16x16x32_bf16 v[164:167], a[96:99], a[40:43], v[164:167]\n"
    "ds_read_b64_tr_b16 a[248:249], v214 offset:49152\n"
    "v_mfma_f32_16x16x32_bf16 v[164:167], a[100:103], a[44:47], v[164:167]\n"
    "v_mfma_f32_16x16x32_bf16 v[168:171], a[0:3], a[104:107], v[168:171]\n"
    "ds_read_b128 a[96:99], v200 offset:34816\n"
    "ds_read_b64_tr_b16 a[250:251], v214 offset:51200\n"
    "v_mfma_f32_16x16x32_bf16 v[168:171], a[4:7], a[108:111], v[168:171]\n"
    "ds_read_b128 a[100:103], v201 offset:34816\n"
    "s_add_u32 m0, s59, 71680\n"
    "v_mfma_f32_16x16x32_bf16 v[172:175], a[8:11], a[104:107], v[172:175]\n"
    "ds_read_b64_tr_b16 a[252:253], v215 offset:49152\n"
    "s_xor_b32 s63, s60, 100096\n"
    "v_mfma_f32_16x16x32_bf16 v[172:175], a[12:15], a[108:111], v[172:175]\n"
    "buffer_load_dwordx4 v196, s[24:27], s63 offen lds\n"
    "v_mfma_f32_16x16x32_bf16 v[176:179], a[104:107], a[16:19], v[176:179]\n"
    "ds_read_b64_tr_b16 a[254:255], v215 offset:51200\n"
    "v_mfma_f32_16x16x32_bf16 v[176:179], a[108:111], a[20:23], v[176:179]\n"
    "v_mfma_f32_16x16x32_bf16 v[180:183], a[104:107], a[24:27], v[180:183]\n"
    "buffer_load_dwordx4 v197, s[24:27], s63 offen offset:1024 lds\n"
    "v_mfma_f32_16x16x32_bf16 v[180:183], a[108:111], a[28:31], v[180:183]\n"
    "v_mfma_f32_16x16x32_bf16 v[184:187], a[104:107], a[32:35], v[184:187]\n"
    "v_mfma_f32_16x16x32_bf16 v[184:187], a[108:111], a[36:39], v[184:187]\n"
    "v_mfma_f32_16x16x32_bf16 v[188:191], a[104:107], a[40:43], v[188:191]\n"
    "v_mfma_f32_16x16x32_bf16 v[188:191], a[108:111], a[44:47], v[188:191]\n"
    "s_waitcnt lgkmcnt(10)\n"
    "v_mfma_f32_16x16x32_bf16 v[0:3], a[48:51], a[112:115], v[0:3]\n"
    "ds_read_b128 a[104:107], v200 offset:36864\n"
    "s_waitcnt lgkmcnt(1)\n"
    "s_add_u32 s63, s65, 458880\n"
    "v_mfma_f32_16x16x32_bf16 v[0:3], a[52:55], a[116:119], v[0:3]\n"
    "ds_read_b128 a[108:111], v201 offset:36864\n"
    "s_and_b32 s63, s63, 0x7ffff\n"
    "v_mfma_f32_16x16x32_bf16 v[4:7], a[56:59], a[112:115], v[4:7]\n"
    "s_add_u32 m0, s64, 147456\n"
    "v_lshlrev_b32 v193, 16, v222\n"
    "v_mfma_f32_16x16x32_bf16 v[4:7], a[60:63], a[116:119], v[4:7]\n"
    "buffer_load_dwordx4 v208, s[32:35], s63 offen offset:0 lds\n"
    "v_and_b32 v222, 0xffff0000, v222\n"
    "v_mfma_f32_16x16x32_bf16 v[8:11], a[112:115], a[64:67], v[8:11]\n"
    "buffer_load_dwordx4 v209, s[32:35], s63 offen offset:1024 lds\n"
    "s_waitcnt vmcnt(13)\n"
    "v_lshlrev_b32 v194, 16, v230\n"
    "v_mfma_f32_16x16x32_bf16 v[8:11], a[116:119], a[68:71], v[8:11]\n"
    "v_and_b32 v230, 0xffff0000, v230\n"
    "v_mfma_f32_16x16x32_bf16 v[12:15], a[112:115], a[72:75], v[12:15]\n"
    "v_fmac_f32 v242, v193, v194\n"
    "v_mfma_f32_16x16x32_bf16 v[12:15], a[116:119], a[76:79], v[12:15]\n"
    "v_fmac_f32 v242, v222, v230\n"
    "v_mfma_f32_16x16x32_bf16 v[16:19], a[112:115], a[80:83], v[16:19]\n"
    "s_add_u32 m0, s61, 131072\n"
    "s_waitcnt vmcnt(8)\n"
    "v_lshlrev_b32 v194, 16, v238\n"
    "v_mfma_f32_16x16x32_bf16 v[16:19], a[116:119], a[84:87], v[16:19]\n"
    "s_add_u32 s63, s62, 5900288\n"
    "v_and_b32 v238, 0xffff0000, v238\n"
    "v_mfma_f32_16x16x32_bf16 v[20:23], a[112:115], a[88:91], v[20:23]\n"
    "buffer_load_dwordx4 v198, s[20:23], s63 offen lds\n"
    "v_fmac_f32 v243, v193, v194\n"
    "v_mfma_f32_16x16x32_bf16 v[20:23], a[116:119], a[92:95], v[20:23]\n"
    "v_fmac_f32 v243, v222, v238\n"
    "v_mfma_f32_16x16x32_bf16 v[24:27], a[48:51], a[96:99], v[24:27]\n"
    "ds_read_b128 a[112:115], v200 offset:38912\n"
    "v_lshlrev_b32 v193, 16, v223\n"
    "v_mfma_f32_16x16x32_bf16 v[24:27], a[52:55], a[100:103], v[24:27]\n"
    "ds_read_b128 a[116:119], v201 offset:38912\n"
    "buffer_load_dwordx4 v199, s[20:23], s63 offen offset:1024 lds\n"
    "v_mfma_f32_16x16x32_bf16 v[28:31], a[56:59], a[96:99], v[28:31]\n"
    "v_and_b32 v223, 0xffff0000, v223\n"
    "v_mfma_f32_16x16x32_bf16 v[28:31], a[60:63], a[100:103], v[28:31]\n"
    "v_lshlrev_b32 v194, 16, v231\n"
    "v_mfma_f32_16x16x32_bf16 v[32:35], a[96:99], a[64:67], v[32:35]\n"
    "s_add_u32 s63, s65, 458880\n"
    "v_and_b32 v231, 0xffff0000, v231\n"
    "v_mfma_f32_16x16x32_bf16 v[32:35], a[100:103], a[68:71], v[32:35]\n"
    "s_and_b32 s63, s63, 0x7ffff\n"
    "v_fmac_f32 v242, v193, v194\n"
    "v_mfma_f32_16x16x32_bf16 v[36:39], a[96:99], a[72:75], v[36:39]\n"
    "s_add_u32 m0, s64, 147456\n"
    "v_fmac_f32 v242, v223, v231\n"
    "v_mfma_f32_16x16x32_bf16 v[36:39], a[100:103], a[76:79], v[36:39]\n"
    "buffer_load_dwordx4 v210, s[32:35], s63 offen offset:2048 lds\n"
    "v_lshlrev_b32 v194, 16, v239\n"
    "v_mfma_f32_16x16x32_bf16 v[40:43], a[96:99], a[80:83], v[40:43]\n"
    "buffer_load_dwordx4 v211, s[32:35], s63 offen offset:3072 lds\n"
    "v_and_b32 v239, 0xffff0000, v239\n"
    "v_mfma_f32_16x16x32_bf16 v[40:43], a[100:103], a[84:87], v[40:43]\n"
    "v_fmac_f32 v243, v193, v194\n"
    "v_mfma_f32_16x16x32_bf16 v[44:47], a[96:99], a[88:91], v[44:47]\n"
    "v_fmac_f32 v243, v223, v239\n"
    "v_mfma_f32_16x16x32_bf16 v[44:47], a[100:103], a[92:95], v[44:47]\n"
    "s_waitcnt lgkmcnt(2)\n"
    "s_barrier\n"
    "v_mfma_f32_16x16x32_bf16 v[48:51], a[48:51], a[104:107], v[48:51]\n"
    "ds_read_b128 a[96:99], v200 offset:40960\n"
    "ds_read_b64_tr_b16 a[0:1], v204 offset:0\n"
    "v_mfma_f32_16x16x32_bf16 v[48:51], a[52:55], a[108:111], v[48:51]\n"
    "ds_read_b128 a[100:103], v201 offset:40960\n"
    "ds_read_b64_tr_b16 a[2:3], v204 offset:512\n"
    "v_mfma_f32_16x16x32_bf16 v[52:55], a[56:59], a[104:107], v[52:55]\n"
    "s_add_u32 m0, s59, 0\n"
    "v_lshlrev_b32 v193, 16, v224\n"
    "v_mfma_f32_16x16x32_bf16 v[52:55], a[60:63], a[108:111], v[52:55]\n"
    "ds_read_b64_tr_b16 a[4:5], v204 offset:4096\n"
    "s_xor_b32 s63, s60, 1920\n"
    "v_mfma_f32_16x16x32_bf16 v[56:59], a[104:107], a[64:67], v[56:59]\n"
    "buffer_load_dwordx4 v196, s[24:27], s63 offen lds\n"
    "v_and_b32 v224, 0xffff0000, v224\n"
    "v_mfma_f32_16x16x32_bf16 v[56:59], a[108:111], a[68:71], v[56:59]\n"
    "ds_read_b64_tr_b16 a[6:7], v204 offset:4608\n"
    "v_lshlrev_b32 v194, 16, v232\n"
    "v_mfma_f32_16x16x32_bf16 v[60:63], a[104:107], a[72:75], v[60:63]\n"
    "ds_read_b64_tr_b16 a[8:9], v205 offset:0\n"
    "v_and_b32 v232, 0xffff0000, v232\n"
    "v_mfma_f32_16x16x32_bf16 v[60:63], a[108:111], a[76:79], v[60:63]\n"
    "buffer_load_dwordx4 v197, s[24:27], s63 offen offset:1024 lds\n"
    "v_fmac_f32 v242, v193, v194\n"
    "v_mfma_f32_16x16x32_bf16 v[64:67], a[104:107], a[80:83], v[64:67]\n"
    "ds_read_b64_tr_b16 a[10:11], v205 offset:512\n"
    "v_fmac_f32 v242, v224, v232\n"
    "v_mfma_f32_16x16x32_bf16 v[64:67], a[108:111], a[84:87], v[64:67]\n"
    "v_lshlrev_b32 v194, 16, v240\n"
    "v_mfma_f32_16x16x32_bf16 v[68:71], a[104:107], a[88:91], v[68:71]\n"
    "ds_read_b64_tr_b16 a[12:13], v205 offset:4096\n"
    "v_and_b32 v240, 0xffff0000, v240\n"
    "v_mfma_f32_16x16x32_bf16 v[68:71], a[108:111], a[92:95], v[68:71]\n"
    "ds_read_b64_tr_b16 a[14:15], v205 offset:4608\n"
    "s_waitcnt lgkmcnt(10)\n"
    "v_mfma_f32_16x16x32_bf16 v[72:75], a[48:51], a[112:115], v[72:75]\n"
    "ds_read_b128 a[104:107], v200 offset:43008\n"
    "v_fmac_f32 v243, v193, v194\n"
    "v_mfma_f32_16x16x32_bf16 v[72:75], a[52:55], a[116:119], v[72:75]\n"
    "ds_read_b128 a[108:111], v201 offset:43008\n"
    "ds_read_b64_tr_b16 a[16:17], v204 offset:8192\n"
    "v_mfma_f32_16x16x32_bf16 v[76:79], a[56:59], a[112:115], v[76:79]\n"
    "s_add_u32 m0, s61, 139264\n"
    "v_fmac_f32 v243, v224, v240\n"
    "v_mfma_f32_16x16x32_bf16 v[76:79], a[60:63], a[116:119], v[76:79]\n"
    "ds_read_b64_tr_b16 a[18:19], v204 offset:8704\n"
    "s_add_u32 s63, s62, 5902336\n"
    "v_mfma_f32_16x16x32_bf16 v[80:83], a[112:115], a[64:67], v[80:83]\n"
    "ds_read_b64_tr_b16 a[20:21], v204 offset:12288\n"
    "buffer_load_dwordx4 v198, s[20:23], s63 offen lds\n"
    "v_mfma_f32_16x16x32_bf16 v[80:83], a[116:119], a[68:71], v[80:83]\n"
    "v_lshlrev_b32 v193, 16, v225\n"
    "v_mfma_f32_16x16x32_bf16 v[84:87], a[112:115], a[72:75], v[84:87]\n"
    "ds_read_b64_tr_b16 a[22:23], v204 offset:12800\n"
    "v_and_b32 v225, 0xffff0000, v225\n"
    "v_mfma_f32_16x16x32_bf16 v[84:87], a[116:119], a[76:79], v[84:87]\n"
    "buffer_load_dwordx4 v199, s[20:23], s63 offen offset:1024 lds\n"
    "v_lshlrev_b32 v194, 16, v233\n"
    "v_mfma_f32_16x16x32_bf16 v[88:91], a[112:115], a[80:83], v[88:91]\n"
    "ds_read_b64_tr_b16 a[24:25], v205 offset:8192\n"
    "v_and_b32 v233, 0xffff0000, v233\n"
    "v_mfma_f32_16x16x32_bf16 v[88:91], a[116:119], a[84:87], v[88:91]\n"
    "ds_read_b64_tr_b16 a[26:27], v205 offset:8704\n"
    "v_fmac_f32 v242, v193, v194\n"
    "v_mfma_f32_16x16x32_bf16 v[92:95], a[112:115], a[88:91], v[92:95]\n"
    "v_fmac_f32 v242, v225, v233\n"
    "v_mfma_f32_16x16x32_bf16 v[92:95], a[116:119], a[92:95], v[92:95]\n"
    "ds_read_b64_tr_b16 a[28:29], v205 offset:12288\n"
    "s_waitcnt lgkmcnt(14)\n"
    "v_mfma_f32_16x16x32_bf16 v[96:99], a[48:51], a[96:99], v[96:99]\n"
    "ds_read_b128 a[112:115], v200 offset:45056\n"
    "v_lshlrev_b32 v194, 16, v241\n"
    "v_mfma_f32_16x16x32_bf16 v[96:99], a[52:55], a[100:103], v[96:99]\n"
    "ds_read_b128 a[116:119], v201 offset:45056\n"
    "ds_read_b64_tr_b16 a[30:31], v205 offset:12800\n"
    "v_mfma_f32_16x16x32_bf16 v[100:103], a[56:59], a[96:99], v[100:103]\n"
    "ds_read_b64_tr_b16 a[32:33], v206 offset:16384\n"
    "s_add_u32 m0, s59, 2048\n"
    "v_mfma_f32_16x16x32_bf16 v[100:103], a[60:63], a[100:103], v[100:103]\n"
    "s_xor_b32 s63, s60, 34688\n"
    "v_and_b32 v241, 0xffff0000, v241\n"
    "v_mfma_f32_16x16x32_bf16 v[104:107], a[96:99], a[64:67], v[104:107]\n"
    "ds_read_b64_tr_b16 a[34:35], v206 offset:16896\n"
    "buffer_load_dwordx4 v196, s[24:27], s63 offen lds\n"
    "v_mfma_f32_16x16x32_bf16 v[104:107], a[100:103], a[68:71], v[104:107]\n"
    "v_fmac_f32 v243, v193, v194\n"
    "v_mfma_f32_16x16x32_bf16 v[108:111], a[96:99], a[72:75], v[108:111]\n"
    "ds_read_b64_tr_b16 a[36:37], v206 offset:20480\n"
    "v_fmac_f32 v243, v225, v241\n"
    "v_mfma_f32_16x16x32_bf16 v[108:111], a[100:103], a[76:79], v[108:111]\n"
    "ds_read_b64_tr_b16 a[38:39], v206 offset:20992\n"
    "buffer_load_dwordx4 v197, s[24:27], s63 offen offset:1024 lds\n"
    "v_mfma_f32_16x16x32_bf16 v[112:115], a[96:99], a[80:83], v[112:115]\n"
    "v_add_f32_dpp v242, v242, v242 row_shr:1 row_mask:0xf bank_mask:0xf\n"
    "v_min_u32 v195, 15, v192\n"
    "v_mfma_f32_16x16x32_bf16 v[112:115], a[100:103], a[84:87], v[112:115]\n"
    "ds_read_b64_tr_b16 a[40:41], v207 offset:16384\n"
    "v_add_f32_dpp v243, v243, v243 row_shr:1 row_mask:0xf bank_mask:0xf\n"
    "v_mfma_f32_16x16x32_bf16 v[116:119], a[96:99], a[88:91], v[116:119]\n"
    "v_add_f32_dpp v242, v242, v242 row_shr:2 row_mask:0xf bank_mask:0xf\n"
    "v_lshlrev_b32 v195, 7, v195\n"
    "v_mfma_f32_16x16x32_bf16 v[116:119], a[100:103], a[92:95], v[116:119]\n"
    "ds_read_b64_tr_b16 a[42:43], v207 offset:16896\n"
    "s_waitcnt lgkmcnt(14)\n"
    "v_mfma_f32_16x16x32_bf16 v[120:123], a[48:51], a[104:107], v[120:123]\n"
    "ds_read_b128 a[96:99], v200 offset:47104\n"
    "ds_read_b64_tr_b16 a[44:45], v207 offset:20480\n"
    "v_mfma_f32_16x16x32_bf16 v[120:123], a[52:55], a[108:111], v[120:123]\n"
    "ds_read_b128 a[100:103], v201 offset:47104\n"
    "s_add_u32 m0, s59, 4096\n"
    "v_mfma_f32_16x16x32_bf16 v[124:127], a[56:59], a[104:107], v[124:127]\n"
    "ds_read_b64_tr_b16 a[46:47], v207 offset:20992\n"
    "s_xor_b32 s63, s60, 67456\n"
    "v_mfma_f32_16x16x32_bf16 v[124:127], a[60:63], a[108:111], v[124:127]\n"
    "buffer_load_dwordx4 v196, s[24:27], s63 offen lds\n"
    "v_add_f32_dpp v243, v243, v243 row_shr:2 row_mask:0xf bank_mask:0xf\n"
    "v_mfma_f32_16x16x32_bf16 v[128:131], a[104:107], a[64:67], v[128:131]\n"
    "v_add_f32_dpp v242, v242, v242 row_shr:4 row_mask:0xf bank_mask:0xf\n"
    "v_add_u32 v195, s87, v195\n"
    "v_mfma_f32_16x16x32_bf16 v[128:131], a[108:111], a[68:71], v[128:131]\n"
    "buffer_load_dwordx4 v197, s[24:27], s63 offen offset:1024 lds\n"
    "v_add_f32_dpp v243, v243, v243 row_shr:4 row_mask:0xf bank_mask:0xf\n"
    "v_mfma_f32_16x16x32_bf16 v[132:135], a[104:107], a[72:75], v[132:135]\n"
    "v_add_f32_dpp v242, v242, v242 row_shr:8 row_mask:0xf bank_mask:0xf\n"
    "v_subrev_u32 v254, 48, v192\n"
    "v_mfma_f32_16x16x32_bf16 v[132:135], a[108:111], a[76:79], v[132:135]\n"
    "v_add_f32_dpp v243, v243, v243 row_shr:8 row_mask:0xf bank_mask:0xf\n"
    "v_min_u32 v254, 11, v254\n"
    "v_mfma_f32_16x16x32_bf16 v[136:139], a[104:107], a[80:83], v[136:139]\n"
    "s_add_u32 m0, s59, 6144\n"
    "v_add_f32_dpp v242, v242, v242 row_bcast:15 row_mask:0xa bank_mask:0xf\n"
    "v_mfma_f32_16x16x32_bf16 v[136:139], a[108:111], a[84:87], v[136:139]\n"
    "s_xor_b32 s63, s60, 100224\n"
    "v_add_f32_dpp v243, v243, v243 row_bcast:15 row_mask:0xa bank_mask:0xf\n"
    "v_mfma_f32_16x16x32_bf16 v[140:143], a[104:107], a[88:91], v[140:143]\n"
    "buffer_load_dwordx4 v196, s[24:27], s63 offen lds\n"
    "v_add_f32_dpp v242, v242, v242 row_bcast:31 row_mask:0xc bank_mask:0xf\n"
    "v_mfma_f32_16x16x32_bf16 v[140:143], a[108:111], a[92:95], v[140:143]\n"
    "s_waitcnt vmcnt(15) lgkmcnt(0)\n"
    "s_barrier\n"
    "v_mfma_f32_16x16x32_bf16 v[144:147], a[48:51], a[112:115], v[144:147]\n"
    "ds_read_b128 a[104:107], v202 offset:0\n"
    "v_add_f32_dpp v243, v243, v243 row_bcast:31 row_mask:0xc bank_mask:0xf\n"
    "v_mfma_f32_16x16x32_bf16 v[144:147], a[52:55], a[116:119], v[144:147]\n"
    "ds_read_b128 a[108:111], v203 offset:0\n"
    "buffer_load_dwordx4 v197, s[24:27], s63 offen offset:1024 lds\n"
    "v_mfma_f32_16x16x32_bf16 v[148:151], a[56:59], a[112:115], v[148:151]\n"
    "v_mul_f32 v242, 0xbfb8aa3b, v242\n"
    "v_lshlrev_b32 v254, 7, v254\n"
    "v_mfma_f32_16x16x32_bf16 v[148:151], a[60:63], a[116:119], v[148:151]\n"
    "v_mul_f32 v243, 0xbfb8aa3b, v243\n"
    "v_add_u32 v254, s85, v254\n"
    "v_mfma_f32_16x16x32_bf16 v[152:155], a[112:115], a[64:67], v[152:155]\n"
    "v_exp_f32 v242, v242\n"
    "v_mfma_f32_16x16x32_bf16 v[152:155], a[116:119], a[68:71], v[152:155]\n"
    "v_exp_f32 v243, v243\n"
    "v_mfma_f32_16x16x32_bf16 v[156:159], a[112:115], a[72:75], v[156:159]\n"
    "v_add_f32 v242, 1.0, v242\n"
    "v_cmp_gt_u32 s[44:45], 48, v192\n"
    "v_mfma_f32_16x16x32_bf16 v[156:159], a[116:119], a[76:79], v[156:159]\n"
    "v_add_f32 v243, 1.0, v243\n"
    "v_cndmask_b32 v195, v254, v195, s[44:45]\n"
    "v_mfma_f32_16x16x32_bf16 v[160:163], a[112:115], a[80:83], v[160:163]\n"
    "v_rcp_f32 v218, v242\n"
    "v_mfma_f32_16x16x32_bf16 v[160:163], a[116:119], a[84:87], v[160:163]\n"
    "v_rcp_f32 v220, v243\n"
    "v_mfma_f32_16x16x32_bf16 v[164:167], a[112:115], a[88:91], v[164:167]\n"
    "v_mov_b32 v219, s56\n"
    "v_writelane_b32 v195, s83, 62\n"
    "v_mfma_f32_16x16x32_bf16 v[164:167], a[116:119], a[92:95], v[164:167]\n"
    "v_mov_b32 v221, s56\n"
    "v_mfma_f32_16x16x32_bf16 v[168:171], a[48:51], a[96:99], v[168:171]\n"
    "ds_read_b128 a[112:115], v202 offset:2048\n"
    "v_mov_b32 v223, s56\n"
    "v_mfma_f32_16x16x32_bf16 v[168:171], a[52:55], a[100:103], v[168:171]\n"
    "ds_read_b128 a[116:119], v203 offset:2048\n"
    "v_mov_b32 v225, s56\n"
    "v_mfma_f32_16x16x32_bf16 v[172:175], a[56:59], a[96:99], v[172:175]\n"
    "v_sub_f32 v222, 1.0, v218\n"
    "v_writelane_b32 v195, s97, 63\n"
    "v_mfma_f32_16x16x32_bf16 v[172:175], a[60:63], a[100:103], v[172:175]\n"
    "v_sub_f32 v224, 1.0, v220\n"
    "v_writelane_b32 v195, s85, 40\n"
    "v_mfma_f32_16x16x32_bf16 v[176:179], a[96:99], a[64:67], v[176:179]\n"
    "s_add_u32 s72, s94, 16384\n"
    "v_writelane_b32 v195, s12, 41\n"
    "v_mfma_f32_16x16x32_bf16 v[176:179], a[100:103], a[68:71], v[176:179]\n"
    "buffer_store_dwordx2 v[218:219], v251, s[28:31], s94 offen offset:0 sc1\n"
    "v_writelane_b32 v195, s14, 42\n"
    "v_mfma_f32_16x16x32_bf16 v[180:183], a[96:99], a[72:75], v[180:183]\n"
    "buffer_store_dwordx2 v[220:221], v251, s[28:31], s94 offen offset:64 sc1\n"
    "v_writelane_b32 v195, s16, 43\n"
    "v_mfma_f32_16x16x32_bf16 v[180:183], a[100:103], a[76:79], v[180:183]\n"
    "buffer_store_dwordx2 v[222:223], v251, s[28:31], s72 offen offset:0 sc1\n"
    "v_writelane_b32 v195, s95, 44\n"
    "v_mfma_f32_16x16x32_bf16 v[184:187], a[96:99], a[80:83], v[184:187]\n"
    "buffer_store_dwordx2 v[224:225], v251, s[28:31], s72 offen offset:64 sc1\n"
    "v_writelane_b32 v195, s88, 45\n"
    "v_mfma_f32_16x16x32_bf16 v[184:187], a[100:103], a[84:87], v[184:187]\n"
    "buffer_load_dword v249, v195, s[28:31], 0 offen sc1\n"
    "v_mfma_f32_16x16x32_bf16 v[188:191], a[96:99], a[88:91], v[188:191]\n"
    "buffer_load_dword v249, v251, s[36:39], s90 offen\n"
    "v_mfma_f32_16x16x32_bf16 v[188:191], a[100:103], a[92:95], v[188:191]\n"
    "s_waitcnt lgkmcnt(2)\n"
    "v_mfma_f32_16x16x32_bf16 v[8:11], a[104:107], a[16:19], v[8:11]\n"
    "ds_read_b128 a[96:99], v202 offset:4096\n"
    "v_mfma_f32_16x16x32_bf16 v[8:11], a[108:111], a[20:23], v[8:11]\n"
    "ds_read_b128 a[100:103], v203 offset:4096\n"
    "v_mfma_f32_16x16x32_bf16 v[12:15], a[104:107], a[24:27], v[12:15]\n"
    "v_mov_b32 v244, 0x3f803f80\n"
    "v_mfma_f32_16x16x32_bf16 v[12:15], a[108:111], a[28:31], v[12:15]\n"
    "v_mov_b32 v245, 0x3f803f80\n"
    "v_mfma_f32_16x16x32_bf16 v[16:19], a[104:107], a[32:35], v[16:19]\n"
    "v_mov_b32 v246, 0x3f803f80\n"
    "v_mfma_f32_16x16x32_bf16 v[16:19], a[108:111], a[36:39], v[16:19]\n"
    "v_mov_b32 v247, 0x3f803f80\n"
    "v_mfma_f32_16x16x32_bf16 v[20:23], a[104:107], a[40:43], v[20:23]\n"
    "s_add_u32 s75, s64, 147456\n"
    "v_mfma_f32_16x16x32_bf16 v[20:23], a[108:111], a[44:47], v[20:23]\n"
    "v_add_u32 v217, s75, v216\n"
    "s_waitcnt lgkmcnt(2)\n"
    "v_mfma_f32_16x16x32_bf16 v[32:35], a[112:115], a[16:19], v[32:35]\n"
    "ds_read_b128 a[104:107], v202 offset:6144\n"
    "v_lshrrev_b32 v193, 4, v192\n"
    "v_mfma_f32_16x16x32_bf16 v[32:35], a[116:119], a[20:23], v[32:35]\n"
    "ds_read_b128 a[108:111], v203 offset:6144\n"
    "v_mov_b32 v194, 1.0\n"
    "v_mfma_f32_16x16x32_bf16 v[36:39], a[112:115], a[24:27], v[36:39]\n"
    "v_cmp_eq_u32 vcc, 0, v193\n"
    "v_mfma_f32_16x16x32_bf16 v[36:39], a[116:119], a[28:31], v[36:39]\n"
    "s_nop 0\n"
    "v_cndmask_b32 v248, 0, v194, vcc\n"
    "v_mfma_f32_16x16x32_bf16 v[40:43], a[112:115], a[32:35], v[40:43]\n"
    "v_cmp_eq_u32 vcc, 1, v193\n"
    "v_mfma_f32_16x16x32_bf16 v[40:43], a[116:119], a[36:39], v[40:43]\n"
    "s_nop 0\n"
    "v_cndmask_b32 v254, 0, v194, vcc\n"
    "v_mfma_f32_16x16x32_bf16 v[44:47], a[112:115], a[40:43], v[44:47]\n"
    "v_mfma_f32_16x16x32_bf16 v[44:47], a[116:119], a[44:47], v[44:47]\n"
    "s_waitcnt lgkmcnt(2)\n"
    "s_waitcnt vmcnt(12)\n"
    "s_barrier\n"
    "v_mfma_f32_16x16x32_bf16 v[56:59], a[96:99], a[16:19], v[56:59]\n"
    "ds_read_b128 a[112:115], v202 offset:8192\n"
    "ds_read_b64_tr_b16 a[48:49], v204 offset:24576\n"
    "v_mfma_f32_16x16x32_bf16 v[56:59], a[100:103], a[20:23], v[56:59]\n"
    "ds_read_b128 a[116:119], v203 offset:8192\n"
    "ds_read_b64_tr_b16 a[50:51], v204 offset:25088\n"
    "v_mfma_f32_16x16x32_bf16 v[60:63], a[96:99], a[24:27], v[60:63]\n"
    "ds_read_b64_tr_b16 a[52:53], v204 offset:28672\n"
    "v_mfma_f32_16x16x32_bf16 v[60:63], a[100:103], a[28:31], v[60:63]\n"
    "ds_read_b64_tr_b16 a[54:55], v204 offset:29184\n"
    "v_mfma_f32_16x16x32_bf16 v[64:67], a[96:99], a[32:35], v[64:67]\n"
    "ds_read_b64_tr_b16 a[56:57], v205 offset:24576\n"
    "v_mfma_f32_16x16x32_bf16 v[64:67], a[100:103], a[36:39], v[64:67]\n"
    "ds_read_b64_tr_b16 a[58:59], v205 offset:25088\n"
    "v_mfma_f32_16x16x32_bf16 v[68:71], a[96:99], a[40:43], v[68:71]\n"
    "ds_read_b64_tr_b16 a[60:61], v205 offset:28672\n"
    "v_mfma_f32_16x16x32_bf16 v[68:71], a[100:103], a[44:47], v[68:71]\n"
    "ds_read_b64_tr_b16 a[62:63], v205 offset:29184\n"
    "s_waitcnt lgkmcnt(10)\n"
    "v_mfma_f32_16x16x32_bf16 v[80:83], a[104:107], a[16:19], v[80:83]\n"
    "ds_read_b128 a[96:99], v202 offset:10240\n"
    "v_mfma_f32_16x16x32_bf16 v[80:83], a[108:111], a[20:23], v[80:83]\n"
    "ds_read_b128 a[100:103], v203 offset:10240\n"
    "ds_read_b64_tr_b16 a[64:65], v204 offset:32768\n"
    "v_mfma_f32_16x16x32_bf16 v[84:87], a[104:107], a[24:27], v[84:87]\n"
    "ds_read_b64_tr_b16 a[66:67], v204 offset:33280\n"
    "v_mfma_f32_16x16x32_bf16 v[84:87], a[108:111], a[28:31], v[84:87]\n"
    "ds_read_b64_tr_b16 a[68:69], v204 offset:36864\n"
    "v_mfma_f32_16x16x32_bf16 v[88:91], a[104:107], a[32:35], v[88:91]\n"
    "ds_read_b64_tr_b16 a[70:71], v204 offset:37376\n"
    "v_mfma_f32_16x16x32_bf16 v[88:91], a[108:111], a[36:39], v[88:91]\n"
    "ds_read_b64_tr_b16 a[72:73], v205 offset:32768\n"
    "v_mfma_f32_16x16x32_bf16 v[92:95], a[104:107], a[40:43], v[92:95]\n"
    "ds_read_b64_tr_b16 a[74:75], v205 offset:33280\n"
    "s_getpc_b64 s[70:71]\n"
    "Lpd%=:\n"
    "v_mfma_f32_16x16x32_bf16 v[92:95], a[108:111], a[44:47], v[92:95]\n"
    "ds_read_b64_tr_b16 a[76:77], v205 offset:36864\n"
    "s_waitcnt lgkmcnt(14)\n"
    "v_mfma_f32_16x16x32_bf16 v[104:107], a[112:115], a[16:19], v[104:107]\n"
    "ds_read_b128 a[104:107], v202 offset:12288\n"
    "ds_read_b64_tr_b16 a[78:79], v205 offset:37376\n"
    "v_mfma_f32_16x16x32_bf16 v[104:107], a[116:119], a[20:23], v[104:107]\n"
    "ds_read_b128 a[108:111], v203 offset:12288\n"
    "s_lshr_b32 s77, s53, 3\n"
    "v_mfma_f32_16x16x32_bf16 v[108:111], a[112:115], a[24:27], v[108:111]\n"
    "ds_read_b64_tr_b16 a[80:81], v206 offset:40960\n"
    "s_lshl_b32 s77, s77, 2\n"
    "v_mfma_f32_16x16x32_bf16 v[108:111], a[116:119], a[28:31], v[108:111]\n"
    "ds_read_b64_tr_b16 a[82:83], v206 offset:41472\n"
    "s_add_u32 s77, s77, s48\n"
    "v_mfma_f32_16x16x32_bf16 v[112:115], a[112:115], a[32:35], v[112:115]\n"
    "ds_read_b64_tr_b16 a[84:85], v206 offset:45056\n"
    "s_lshl_b32 s77, s77, 8\n"
    "v_mfma_f32_16x16x32_bf16 v[112:115], a[116:119], a[36:39], v[112:115]\n"
    "ds_read_b64_tr_b16 a[86:87], v206 offset:45568\n"
    "v_lshrrev_b32 v195, 5, v192\n"
    "v_mfma_f32_16x16x32_bf16 v[116:119], a[112:115], a[40:43], v[116:119]\n"
    "ds_read_b64_tr_b16 a[88:89], v207 offset:40960\n"
    "v_lshlrev_b32 v195, 7, v195\n"
    "v_mfma_f32_16x16x32_bf16 v[116:119], a[116:119], a[44:47], v[116:119]\n"
    "ds_read_b64_tr_b16 a[90:91], v207 offset:41472\n"
    "s_waitcnt lgkmcnt(14)\n"
    "v_mfma_f32_16x16x32_bf16 v[128:131], a[96:99], a[16:19], v[128:131]\n"
    "ds_read_b128 a[112:115], v202 offset:14336\n"
    "ds_read_b64_tr_b16 a[92:93], v207 offset:45056\n"
    "v_mfma_f32_16x16x32_bf16 v[128:131], a[100:103], a[20:23], v[128:131]\n"
    "ds_read_b128 a[116:119], v203 offset:14336\n"
    "ds_read_b64_tr_b16 a[94:95], v207 offset:45568\n"
    "v_mfma_f32_16x16x32_bf16 v[132:135], a[96:99], a[24:27], v[132:135]\n"
    "v_add_u32 v195, s77, v195\n"
    "v_min_u32 v195, Lfin%=-Lpd%=, v195\n"
    "v_mfma_f32_16x16x32_bf16 v[132:135], a[100:103], a[28:31], v[132:135]\n"
    "global_load_dword v249, v195, s[70:71]\n"
    "v_mfma_f32_16x16x32_bf16 v[136:139], a[96:99], a[32:35], v[136:139]\n"
    "v_mfma_f32_16x16x32_bf16 v[136:139], a[100:103], a[36:39], v[136:139]\n"
    "v_mfma_f32_16x16x32_bf16 v[140:143], a[96:99], a[40:43], v[140:143]\n"
    "v_mfma_f32_16x16x32_bf16 v[140:143], a[100:103], a[44:47], v[140:143]\n"
    "s_waitcnt vmcnt(7) lgkmcnt(0)\n"
    "s_barrier\n"
    "v_mfma_f32_16x16x32_bf16 v[152:155], a[104:107], a[16:19], v[152:155]\n"
    "ds_read_b128 a[96:99], v200 offset:0\n"
    "v_mfma_f32_16x16x32_bf16 v[152:155], a[108:111], a[20:23], v[152:155]\n"
    "ds_read_b128 a[100:103], v201 offset:0\n"
    "v_mfma_f32_16x16x32_bf16 v[156:159], a[104:107], a[24:27], v[156:159]\n"
    "v_mfma_f32_16x16x32_bf16 v[156:159], a[108:111], a[28:31], v[156:159]\n"
    "v_mfma_f32_16x16x32_bf16 v[160:163], a[104:107], a[32:35], v[160:163]\n"
    "v_mfma_f32_16x16x32_bf16 v[160:163], a[108:111], a[36:39], v[160:163]\n"
    "v_mfma_f32_16x16x32_bf16 v[164:167], a[104:107], a[40:43], v[164:167]\n"
    "v_mfma_f32_16x16x32_bf16 v[164:167], a[108:111], a[44:47], v[164:167]\n"
    "v_mfma_f32_16x16x32_bf16 v[176:179], a[112:115], a[16:19], v[176:179]\n"
    "ds_read_b128 a[104:107], v200 offset:2048\n"
    "v_mfma_f32_16x16x32_bf16 v[176:179], a[116:119], a[20:23], v[176:179]\n"
    "ds_read_b128 a[108:111], v201 offset:2048\n"
    "v_mfma_f32_16x16x32_bf16 v[180:183], a[112:115], a[24:27], v[180:183]\n"
    "v_mfma_f32_16x16x32_bf16 v[180:183], a[116:119], a[28:31], v[180:183]\n"
    "v_mfma_f32_16x16x32_bf16 v[184:187], a[112:115], a[32:35], v[184:187]\n"
    "v_mfma_f32_16x16x32_bf16 v[184:187], a[116:119], a[36:39], v[184:187]\n"
    "v_mfma_f32_16x16x32_bf16 v[188:191], a[112:115], a[40:43], v[188:191]\n"
    "v_mfma_f32_16x16x32_bf16 v[188:191], a[116:119], a[44:47], v[188:191]\n"
    "s_waitcnt lgkmcnt(2)\n"
    "v_mfma_f32_16x16x32_bf16 v[8:11], a[96:99], a[64:67], v[8:11]\n"
    "ds_read_b128 a[112:115], v200 offset:4096\n"
    "ds_read_b64_tr_b16 a[40:41], v212 offset:49152\n"
    "v_mfma_f32_16x16x32_bf16 v[8:11], a[100:103], a[68:71], v[8:11]\n"
    "ds_read_b128 a[116:119], v201 offset:4096\n"
    "ds_read_b64_tr_b16 a[42:43], v212 offset:51200\n"
    "v_mfma_f32_16x16x32_bf16 v[12:15], a[96:99], a[72:75], v[12:15]\n"
    "ds_read_b64_tr_b16 a[44:45], v213 offset:49152\n"
    "ds_read_b64_tr_b16 a[46:47], v213 offset:51200\n"
    "v_mfma_f32_16x16x32_bf16 v[12:15], a[100:103], a[76:79], v[12:15]\n"
    "ds_read_b64_tr_b16 a[120:121], v214 offset:49152\n"
    "ds_read_b64_tr_b16 a[122:123], v214 offset:51200\n"
    "v_mfma_f32_16x16x32_bf16 v[16:19], a[96:99], a[80:83], v[16:19]\n"
    "ds_read_b64_tr_b16 a[124:125], v215 offset:49152\n"
    "ds_read_b64_tr_b16 a[126:127], v215 offset:51200\n"
    "v_mfma_f32_16x16x32_bf16 v[16:19], a[100:103], a[84:87], v[16:19]\n"
    "v_mfma_f32_16x16x32_bf16 v[20:23], a[96:99], a[88:91], v[20:23]\n"
    "v_mfma_f32_16x16x32_bf16 v[20:23], a[100:103], a[92:95], v[20:23]\n"
    "s_waitcnt lgkmcnt(10)\n"
    "v_mfma_f32_16x16x32_bf16 v[32:35], a[104:107], a[64:67], v[32:35]\n"
    "ds_read_b128 a[96:99], v200 offset:6144\n"
    "v_mfma_f32_16x16x32_bf16 v[32:35], a[108:111], a[68:71], v[32:35]\n"
    "ds_read_b128 a[100:103], v201 offset:6144\n"
    "v_mfma_f32_16x16x32_bf16 v[36:39], a[104:107], a[72:75], v[36:39]\n"
    "s_add_u32 s63, s65, 327680\n"
    "v_mfma_f32_16x16x32_bf16 v[36:39], a[108:111], a[76:79], v[36:39]\n"
    "s_and_b32 s63, s63, 0x7ffff\n"
    "v_mfma_f32_16x16x32_bf16 v[40:43], a[104:107], a[80:83], v[40:43]\n"
    "v_mfma_f32_16x16x32_bf16 v[40:43], a[108:111], a[84:87], v[40:43]\n"
    "s_add_u32 m0, s64, 98304\n"
    "v_mfma_f32_16x16x32_bf16 v[44:47], a[104:107], a[88:91], v[44:47]\n"
    "buffer_load_dwordx4 v208, s[32:35], s63 offen offset:0 lds\n"
    "v_mfma_f32_16x16x32_bf16 v[44:47], a[108:111], a[92:95], v[44:47]\n"
    "s_waitcnt lgkmcnt(9)\n"
    "v_mfma_f32_16x16x32_bf16 v[56:59], a[112:115], a[64:67], v[56:59]\n"
    "ds_read_b128 a[104:107], v200 offset:8192\n"
    "buffer_load_dwordx4 v209, s[32:35], s63 offen offset:1024 lds\n"
    "v_mfma_f32_16x16x32_bf16 v[56:59], a[116:119], a[68:71], v[56:59]\n"
    "ds_read_b128 a[108:111], v201 offset:8192\n"
    "buffer_load_dwordx4 v210, s[32:35], s63 offen offset:2048 lds\n"
    "v_mfma_f32_16x16x32_bf16 v[60:63], a[112:115], a[72:75], v[60:63]\n"
    "v_mfma_f32_16x16x32_bf16 v[60:63], a[116:119], a[76:79], v[60:63]\n"
    "buffer_load_dwordx4 v211, s[32:35], s63 offen offset:3072 lds\n"
    "v_pk_fma_f32 v[234:235], v[8:9], s[66:67], s[66:67] neg_lo:[0,0,1] neg_hi:[0,0,1]\n"
    "v_pk_fma_f32 v[236:237], v[10:11], s[66:67], s[66:67] neg_lo:[0,0,1] neg_hi:[0,0,1]\n"
    "v_pk_fma_f32 v[238:239], v[32:33], s[66:67], s[66:67] neg_lo:[0,0,1] neg_hi:[0,0,1]\n"
    "v_pk_fma_f32 v[240:241], v[34:35], s[66:67], s[66:67] neg_lo:[0,0,1] neg_hi:[0,0,1]\n"
    "v_mfma_f32_16x16x32_bf16 v[64:67], a[112:115], a[80:83], v[64:67]\n"
    "s_add_u32 s63, s65, 327808\n"
    "v_exp_f32_e64 v234, v234 clamp\n"
    "v_mfma_f32_16x16x32_bf16 v[64:67], a[116:119], a[84:87], v[64:67]\n"
    "v_exp_f32_e64 v235, v235 clamp\n"
    "v_mfma_f32_16x16x32_bf16 v[68:71], a[112:115], a[88:91], v[68:71]\n"
    "s_and_b32 s63, s63, 0x7ffff\n"
    "v_exp_f32_e64 v236, v236 clamp\n"
    "v_mfma_f32_16x16x32_bf16 v[68:71], a[116:119], a[92:95], v[68:71]\n"
    "s_add_u32 m0, s64, 114688\n"
    "v_exp_f32_e64 v237, v237 clamp\n"
    "s_waitcnt lgkmcnt(2)\n"
    "v_mfma_f32_16x16x32_bf16 v[80:83], a[96:99], a[64:67], v[80:83]\n"
    "ds_read_b128 a[112:115], v200 offset:10240\n"
    "v_exp_f32_e64 v238, v238 clamp\n"
    "v_mfma_f32_16x16x32_bf16 v[80:83], a[100:103], a[68:71], v[80:83]\n"
    "ds_read_b128 a[116:119], v201 offset:10240\n"
    "buffer_load_dwordx4 v208, s[32:35], s63 offen offset:0 lds\n"
    "v_exp_f32_e64 v239, v239 clamp\n"
    "v_mfma_f32_16x16x32_bf16 v[84:87], a[96:99], a[72:75], v[84:87]\n"
    "buffer_load_dwordx4 v209, s[32:35], s63 offen offset:1024 lds\n"
    "v_exp_f32_e64 v240, v240 clamp\n"
    "v_mfma_f32_16x16x32_bf16 v[84:87], a[100:103], a[76:79], v[84:87]\n"
    "v_exp_f32_e64 v241, v241 clamp\n"
    "v_cvt_pk_bf16_f32 v16, v16, v17\n"
    "v_cvt_pk_bf16_f32 v17, v18, v19\n"
    "v_cvt_pk_bf16_f32 v18, v40, v41\n"
    "v_cvt_pk_bf16_f32 v19, v42, v43\n"
    "v_cvt_pk_bf16_f32 v20, v20, v21\n"
    "v_cvt_pk_bf16_f32 v21, v22, v23\n"
    "v_cvt_pk_bf16_f32 v22, v44, v45\n"
    "v_cvt_pk_bf16_f32 v23, v46, v47\n"
    "v_max_f32 v8, v234, v8\n"
    "v_max_f32 v9, v235, v9\n"
    "v_max_f32 v10, v236, v10\n"
    "v_max_f32 v11, v237, v11\n"
    "v_max_f32 v32, v238, v32\n"
    "v_max_f32 v33, v239, v33\n"
    "v_max_f32 v34, v240, v34\n"
    "v_max_f32 v35, v241, v35\n"
    "v_pk_add_f32 v[8:9], v[8:9], s[68:69]\n"
    "v_pk_add_f32 v[10:11], v[10:11], s[68:69]\n"
    "v_pk_add_f32 v[32:33], v[32:33], s[68:69]\n"
    "v_pk_add_f32 v[34:35], v[34:35], s[68:69]\n"
    "v_cvt_pk_bf16_f32 v8, v8, v9\n"
    "v_cvt_pk_bf16_f32 v9, v10, v11\n"
    "v_cvt_pk_bf16_f32 v10, v32, v33\n"
    "v_cvt_pk_bf16_f32 v11, v34, v35\n"
    "v_pk_fma_f32 v[234:235], v[12:13], s[66:67], s[66:67] neg_lo:[0,0,1] neg_hi:[0,0,1]\n"
    "v_pk_fma_f32 v[236:237], v[14:15], s[66:67], s[66:67] neg_lo:[0,0,1] neg_hi:[0,0,1]\n"
    "v_pk_fma_f32 v[238:239], v[36:37], s[66:67], s[66:67] neg_lo:[0,0,1] neg_hi:[0,0,1]\n"
    "v_pk_fma_f32 v[240:241], v[38:39], s[66:67], s[66:67] neg_lo:[0,0,1] neg_hi:[0,0,1]\n"
    "v_mfma_f32_16x16x32_bf16 v[226:229], v[8:11], v[20:23], 0\n"
    "v_exp_f32_e64 v234, v234 clamp\n"
    "v_mfma_f32_16x16x32_bf16 v[218:221], v[8:11], v[16:19], 0\n"
    "v_exp_f32_e64 v235, v235 clamp\n"
    "v_mfma_f32_16x16x32_bf16 v[196:199], v[244:247], v[8:11], 0\n"
    "v_exp_f32_e64 v236, v236 clamp\n"
    "v_mfma_f32_16x16x32_bf16 v[88:91], a[96:99], a[80:83], v[88:91]\n"
    "buffer_load_dwordx4 v210, s[32:35], s63 offen offset:2048 lds\n"
    "v_exp_f32_e64 v237, v237 clamp\n"
    "v_mfma_f32_16x16x32_bf16 v[88:91], a[100:103], a[84:87], v[88:91]\n"
    "buffer_load_dwordx4 v211, s[32:35], s63 offen offset:3072 lds\n"
    "v_exp_f32_e64 v238, v238 clamp\n"
    "v_mfma_f32_16x16x32_bf16 v[92:95], a[96:99], a[88:91], v[92:95]\n"
    "v_exp_f32_e64 v239, v239 clamp\n"
    "v_mfma_f32_16x16x32_bf16 v[92:95], a[100:103], a[92:95], v[92:95]\n"
    "s_add_u32 s63, s65, 393216\n"
    "v_exp_f32_e64 v240, v240 clamp\n"
    "s_waitcnt lgkmcnt(2)\n"
    "v_mfma_f32_16x16x32_bf16 v[104:107], a[104:107], a[64:67], v[104:107]\n"
    "ds_read_b128 a[96:99], v200 offset:12288\n"
    "s_and_b32 s63, s63, 0x7ffff\n"
    "v_exp_f32_e64 v241, v241 clamp\n"
    "v_mfma_f32_16x16x32_bf16 v[104:107], a[108:111], a[68:71], v[104:107]\n"
    "ds_read_b128 a[100:103], v201 offset:12288\n"
    "v_max_f32 v12, v234, v12\n"
    "v_max_f32 v13, v235, v13\n"
    "v_mfma_f32_16x16x32_bf16 v[108:111], a[104:107], a[72:75], v[108:111]\n"
    "s_add_u32 m0, s64, 131072\n"
    "v_max_f32 v14, v236, v14\n"
    "v_max_f32 v15, v237, v15\n"
    "v_mfma_f32_16x16x32_bf16 v[108:111], a[108:111], a[76:79], v[108:111]\n"
    "buffer_load_dwordx4 v208, s[32:35], s63 offen offset:0 lds\n"
    "v_max_f32 v36, v238, v36\n"
    "v_max_f32 v37, v239, v37\n"
    "v_max_f32 v38, v240, v38\n"
    "v_max_f32 v39, v241, v39\n"
    "v_pk_add_f32 v[12:13], v[12:13], s[68:69]\n"
    "v_pk_add_f32 v[14:15], v[14:15], s[68:69]\n"
    "v_pk_add_f32 v[36:37], v[36:37], s[68:69]\n"
    "v_pk_add_f32 v[38:39], v[38:39], s[68:69]\n"
    "v_cvt_pk_bf16_f32 v12, v12, v13\n"
    "v_cvt_pk_bf16_f32 v13, v14, v15\n"
    "v_cvt_pk_bf16_f32 v14, v36, v37\n"
    "v_cvt_pk_bf16_f32 v15, v38, v39\n"
    "v_pk_fma_f32 v[234:235], v[56:57], s[66:67], s[66:67] neg_lo:[0,0,1] neg_hi:[0,0,1]\n"
    "v_pk_fma_f32 v[236:237], v[58:59], s[66:67], s[66:67] neg_lo:[0,0,1] neg_hi:[0,0,1]\n"
    "v_pk_fma_f32 v[238:239], v[80:81], s[66:67], s[66:67] neg_lo:[0,0,1] neg_hi:[0,0,1]\n"
    "v_pk_fma_f32 v[240:241], v[82:83], s[66:67], s[66:67] neg_lo:[0,0,1] neg_hi:[0,0,1]\n"
    "v_mfma_f32_16x16x32_bf16 v[230:233], v[12:15], v[20:23], 0\n"
    "v_exp_f32_e64 v234, v234 clamp\n"
    "v_mfma_f32_16x16x32_bf16 v[222:225], v[12:15], v[16:19], 0\n"
    "v_exp_f32_e64 v235, v235 clamp\n"
    "v_mfma_f32_16x16x32_bf16 v[204:207], v[244:247], v[12:15], 0\n"
    "v_exp_f32_e64 v236, v236 clamp\n"
    "v_mfma_f32_16x16x32_bf16 v[112:115], a[104:107], a[80:83], v[112:115]\n"
    "v_exp_f32_e64 v237, v237 clamp\n"
    "v_mfma_f32_16x16x32_bf16 v[112:115], a[108:111], a[84:87], v[112:115]\n"
    "buffer_load_dwordx4 v209, s[32:35], s63 offen offset:1024 lds\n"
    "v_exp_f32_e64 v238, v238 clamp\n"
    "v_mfma_f32_16x16x32_bf16 v[116:119], a[104:107], a[88:91], v[116:119]\n"
    "buffer_load_dwordx4 v210, s[32:35], s63 offen offset:2048 lds\n"
    "v_exp_f32_e64 v239, v239 clamp\n"
    "v_mfma_f32_16x16x32_bf16 v[116:119], a[108:111], a[92:95], v[116:119]\n"
    "v_exp_f32_e64 v240, v240 clamp\n"
    "s_waitcnt lgkmcnt(2)\n"
    "v_mfma_f32_16x16x32_bf16 v[128:131], a[112:115], a[64:67], v[128:131]\n"
    "ds_read_b128 a[104:107], v200 offset:14336\n"
    "buffer_load_dwordx4 v211, s[32:35], s63 offen offset:3072 lds\n"
    "v_exp_f32_e64 v241, v241 clamp\n"
    "v_mfma_f32_16x16x32_bf16 v[128:131], a[116:119], a[68:71], v[128:131]\n"
    "ds_read_b128 a[108:111], v201 offset:14336\n"
    "s_add_u32 s63, s65, 262144\n"
    "v_cvt_pk_bf16_f32 v64, v64, v65\n"
    "v_cvt_pk_bf16_f32 v65, v66, v67\n"
    "v_mfma_f32_16x16x32_bf16 v[132:135], a[112:115], a[72:75], v[132:135]\n"
    "v_cvt_pk_bf16_f32 v66, v88, v89\n"
    "v_cvt_pk_bf16_f32 v67, v90, v91\n"
    "v_mfma_f32_16x16x32_bf16 v[132:135], a[116:119], a[76:79], v[132:135]\n"
    "s_and_b32 s63, s63, 0x7ffff\n"
    "v_cvt_pk_bf16_f32 v68, v68, v69\n"
    "v_cvt_pk_bf16_f32 v69, v70, v71\n"
    "v_cvt_pk_bf16_f32 v70, v92, v93\n"
    "v_cvt_pk_bf16_f32 v71, v94, v95\n"
    "v_max_f32 v56, v234, v56\n"
    "v_max_f32 v57, v235, v57\n"
    "v_max_f32 v58, v236, v58\n"
    "v_max_f32 v59, v237, v59\n"
    "v_max_f32 v80, v238, v80\n"
    "v_max_f32 v81, v239, v81\n"
    "v_max_f32 v82, v240, v82\n"
    "v_max_f32 v83, v241, v83\n"
    "v_pk_add_f32 v[56:57], v[56:57], s[68:69]\n"
    "v_pk_add_f32 v[58:59], v[58:59], s[68:69]\n"
    "v_pk_add_f32 v[80:81], v[80:81], s[68:69]\n"
    "v_pk_add_f32 v[82:83], v[82:83], s[68:69]\n"
    "v_cvt_pk_bf16_f32 v56, v56, v57\n"
    "v_cvt_pk_bf16_f32 v57, v58, v59\n"
    "v_cvt_pk_bf16_f32 v58, v80, v81\n"
    "v_cvt_pk_bf16_f32 v59, v82, v83\n"
    "v_pk_fma_f32 v[234:235], v[60:61], s[66:67], s[66:67] neg_lo:[0,0,1] neg_hi:[0,0,1]\n"
    "v_pk_fma_f32 v[236:237], v[62:63], s[66:67], s[66:67] neg_lo:[0,0,1] neg_hi:[0,0,1]\n"
    "v_pk_fma_f32 v[238:239], v[84:85], s[66:67], s[66:67] neg_lo:[0,0,1] neg_hi:[0,0,1]\n"
    "v_pk_fma_f32 v[240:241], v[86:87], s[66:67], s[66:67] neg_lo:[0,0,1] neg_hi:[0,0,1]\n"
    "v_mfma_f32_16x16x32_bf16 v[226:229], v[56:59], v[68:71], v[226:229]\n"
    "v_exp_f32_e64 v234, v234 clamp\n"
    "v_mfma_f32_16x16x32_bf16 v[218:221], v[56:59], v[64:67], v[218:221]\n"
    "v_exp_f32_e64 v235, v235 clamp\n"
    "v_mfma_f32_16x16x32_bf16 v[196:199], v[244:247], v[56:59], v[196:199]\n"
    "v_exp_f32_e64 v236, v236 clamp\n"
    "v_mfma_f32_16x16x32_bf16 v[136:139], a[112:115], a[80:83], v[136:139]\n"
    "s_add_u32 m0, s64, 32768\n"
    "v_exp_f32_e64 v237, v237 clamp\n"
    "v_mfma_f32_16x16x32_bf16 v[136:139], a[116:119], a[84:87], v[136:139]\n"
    "v_exp_f32_e64 v238, v238 clamp\n"
    "v_mfma_f32_16x16x32_bf16 v[140:143], a[112:115], a[88:91], v[140:143]\n"
    "buffer_load_dwordx4 v208, s[32:35], s63 offen offset:0 lds\n"
    "v_exp_f32_e64 v239, v239 clamp\n"
    "v_mfma_f32_16x16x32_bf16 v[140:143], a[116:119], a[92:95], v[140:143]\n"
    "buffer_load_dwordx4 v209, s[32:35], s63 offen offset:1024 lds\n"
    "v_exp_f32_e64 v240, v240 clamp\n"
    "s_waitcnt lgkmcnt(2)\n"
    "v_mfma_f32_16x16x32_bf16 v[152:155], a[96:99], a[64:67], v[152:155]\n"
    "v_exp_f32_e64 v241, v241 clamp\n"
    "v_mfma_f32_16x16x32_bf16 v[152:155], a[100:103], a[68:71], v[152:155]\n"
    "buffer_load_dwordx4 v210, s[32:35], s63 offen offset:2048 lds\n"
    "v_max_f32 v60, v234, v60\n"
    "v_max_f32 v61, v235, v61\n"
    "v_mfma_f32_16x16x32_bf16 v[156:159], a[96:99], a[72:75], v[156:159]\n"
    "buffer_load_dwordx4 v211, s[32:35], s63 offen offset:3072 lds\n"
    "v_max_f32 v62, v236, v62\n"
    "v_max_f32 v63, v237, v63\n"
    "v_mfma_f32_16x16x32_bf16 v[156:159], a[100:103], a[76:79], v[156:159]\n"
    "v_max_f32 v84, v238, v84\n"
    "v_max_f32 v85, v239, v85\n"
    "v_max_f32 v86, v240, v86\n"
    "v_max_f32 v87, v241, v87\n"
    "v_pk_add_f32 v[60:61], v[60:61], s[68:69]\n"
    "v_pk_add_f32 v[62:63], v[62:63], s[68:69]\n"
    "v_pk_add_f32 v[84:85], v[84:85], s[68:69]\n"
    "v_pk_add_f32 v[86:87], v[86:87], s[68:69]\n"
    "v_cvt_pk_bf16_f32 v60, v60, v61\n"
    "v_cvt_pk_bf16_f32 v61, v62, v63\n"
    "v_cvt_pk_bf16_f32 v62, v84, v85\n"
    "v_cvt_pk_bf16_f32 v63, v86, v87\n"
    "v_pk_fma_f32 v[234:235], v[104:105], s[66:67], s[66:67] neg_lo:[0,0,1] neg_hi:[0,0,1]\n"
    "v_pk_fma_f32 v[236:237], v[106:107], s[66:67], s[66:67] neg_lo:[0,0,1] neg_hi:[0,0,1]\n"
    "v_pk_fma_f32 v[238:239], v[128:129], s[66:67], s[66:67] neg_lo:[0,0,1] neg_hi:[0,0,1]\n"
    "v_pk_fma_f32 v[240:241], v[130:131], s[66:67], s[66:67] neg_lo:[0,0,1] neg_hi:[0,0,1]\n"
    "v_mfma_f32_16x16x32_bf16 v[230:233], v[60:63], v[68:71], v[230:233]\n"
    "v_exp_f32_e64 v234, v234 clamp\n"
    "v_mfma_f32_16x16x32_bf16 v[222:225], v[60:63], v[64:67], v[222:225]\n"
    "v_exp_f32_e64 v235, v235 clamp\n"
    "v_mfma_f32_16x16x32_bf16 v[204:207], v[244:247], v[60:63], v[204:207]\n"
    "v_exp_f32_e64 v236, v236 clamp\n"
    "v_mfma_f32_16x16x32_bf16 v[160:163], a[96:99], a[80:83], v[160:163]\n"
    "s_add_u32 s63, s65, 262272\n"
    "v_exp_f32_e64 v237, v237 clamp\n"
    "v_mfma_f32_16x16x32_bf16 v[160:163], a[100:103], a[84:87], v[160:163]\n"
    "s_and_b32 s63, s63, 0x7ffff\n"
    "v_exp_f32_e64 v238, v238 clamp\n"
    "v_mfma_f32_16x16x32_bf16 v[164:167], a[96:99], a[88:91], v[164:167]\n"
    "v_exp_f32_e64 v239, v239 clamp\n"
    "v_mfma_f32_16x16x32_bf16 v[164:167], a[100:103], a[92:95], v[164:167]\n"
    "s_add_u32 m0, s64, 49152\n"
    "v_exp_f32_e64 v240, v240 clamp\n"
    "s_waitcnt lgkmcnt(0)\n"
    "v_mfma_f32_16x16x32_bf16 v[176:179], a[104:107], a[64:67], v[176:179]\n"
    "buffer_load_dwordx4 v208, s[32:35], s63 offen offset:0 lds\n"
    "v_exp_f32_e64 v241, v241 clamp\n"
    "v_mfma_f32_16x16x32_bf16 v[176:179], a[108:111], a[68:71], v[176:179]\n"
    "v_cvt_pk_bf16_f32 v112, v112, v113\n"
    "v_cvt_pk_bf16_f32 v113, v114, v115\n"
    "v_mfma_f32_16x16x32_bf16 v[180:183], a[104:107], a[72:75], v[180:183]\n"
    "buffer_load_dwordx4 v209, s[32:35], s63 offen offset:1024 lds\n"
    "v_cvt_pk_bf16_f32 v114, v136, v137\n"
    "v_cvt_pk_bf16_f32 v115, v138, v139\n"
    "v_mfma_f32_16x16x32_bf16 v[180:183], a[108:111], a[76:79], v[180:183]\n"
    "buffer_load_dwordx4 v210, s[32:35], s63 offen offset:2048 lds\n"
    "v_cvt_pk_bf16_f32 v116, v116, v117\n"
    "v_cvt_pk_bf16_f32 v117, v118, v119\n"
    "v_cvt_pk_bf16_f32 v118, v140, v141\n"
    "v_cvt_pk_bf16_f32 v119, v142, v143\n"
    "v_max_f32 v104, v234, v104\n"
    "v_max_f32 v105, v235, v105\n"
    "v_max_f32 v106, v236, v106\n"
    "v_max_f32 v107, v237, v107\n"
    "v_max_f32 v128, v238, v128\n"
    "v_max_f32 v129, v239, v129\n"
    "v_max_f32 v130, v240, v130\n"
    "v_max_f32 v131, v241, v131\n"
    "v_pk_add_f32 v[104:105], v[104:105], s[68:69]\n"
    "v_pk_add_f32 v[106:107], v[106:107], s[68:69]\n"
    "v_pk_add_f32 v[128:129], v[128:129], s[68:69]\n"
    "v_pk_add_f32 v[130:131], v[130:131], s[68:69]\n"
    "v_cvt_pk_bf16_f32 v104, v104, v105\n"
    "v_cvt_pk_bf16_f32 v105, v106, v107\n"
    "v_cvt_pk_bf16_f32 v106, v128, v129\n"
    "v_cvt_pk_bf16_f32 v107, v130, v131\n"
    "v_pk_fma_f32 v[234:235], v[108:109], s[66:67], s[66:67] neg_lo:[0,0,1] neg_hi:[0,0,1]\n"
    "v_pk_fma_f32 v[236:237], v[110:111], s[66:67], s[66:67] neg_lo:[0,0,1] neg_hi:[0,0,1]\n"
    "v_pk_fma_f32 v[238:239], v[132:133], s[66:67], s[66:67] neg_lo:[0,0,1] neg_hi:[0,0,1]\n"
    "v_pk_fma_f32 v[240:241], v[134:135], s[66:67], s[66:67] neg_lo:[0,0,1] neg_hi:[0,0,1]\n"
    "v_mfma_f32_16x16x32_bf16 v[226:229], v[104:107], v[116:119], v[226:229]\n"
    "v_exp_f32_e64 v234, v234 clamp\n"
    "v_mfma_f32_16x16x32_bf16 v[218:221], v[104:107], v[112:115], v[218:221]\n"
    "v_exp_f32_e64 v235, v235 clamp\n"
    "v_mfma_f32_16x16x32_bf16 v[196:199], v[244:247], v[104:107], v[196:199]\n"
    "v_exp_f32_e64 v236, v236 clamp\n"
    "v_mfma_f32_16x16x32_bf16 v[184:187], a[104:107], a[80:83], v[184:187]\n"
    "v_exp_f32_e64 v237, v237 clamp\n"
    "v_mfma_f32_16x16x32_bf16 v[184:187], a[108:111], a[84:87], v[184:187]\n"
    "buffer_load_dwordx4 v211, s[32:35], s63 offen offset:3072 lds\n"
    "v_exp_f32_e64 v238, v238 clamp\n"
    "v_mfma_f32_16x16x32_bf16 v[188:191], a[104:107], a[88:91], v[188:191]\n"
    "v_exp_f32_e64 v239, v239 clamp\n"
    "v_mfma_f32_16x16x32_bf16 v[188:191], a[108:111], a[92:95], v[188:191]\n"
    "v_exp_f32_e64 v240, v240 clamp\n"
    "v_exp_f32_e64 v241, v241 clamp\n"
    "v_max_f32 v108, v234, v108\n"
    "v_max_f32 v109, v235, v109\n"
    "v_max_f32 v110, v236, v110\n"
    "v_max_f32 v111, v237, v111\n"
    "v_max_f32 v132, v238, v132\n"
    "v_max_f32 v133, v239, v133\n"
    "v_max_f32 v134, v240, v134\n"
    "v_max_f32 v135, v241, v135\n"
    "v_pk_add_f32 v[108:109], v[108:109], s[68:69]\n"
    "v_pk_add_f32 v[110:111], v[110:111], s[68:69]\n"
    "v_pk_add_f32 v[132:133], v[132:133], s[68:69]\n"
    "v_pk_add_f32 v[134:135], v[134:135], s[68:69]\n"
    "v_cvt_pk_bf16_f32 v108, v108, v109\n"
    "v_cvt_pk_bf16_f32 v109, v110, v111\n"
    "v_cvt_pk_bf16_f32 v110, v132, v133\n"
    "v_cvt_pk_bf16_f32 v111, v134, v135\n"
    "v_pk_fma_f32 v[234:235], v[152:153], s[66:67], s[66:67] neg_lo:[0,0,1] neg_hi:[0,0,1]\n"
    "v_pk_fma_f32 v[236:237], v[154:155], s[66:67], s[66:67] neg_lo:[0,0,1] neg_hi:[0,0,1]\n"
    "v_pk_fma_f32 v[238:239], v[176:177], s[66:67], s[66:67] neg_lo:[0,0,1] neg_hi:[0,0,1]\n"
    "v_pk_fma_f32 v[240:241], v[178:179], s[66:67], s[66:67] neg_lo:[0,0,1] neg_hi:[0,0,1]\n"
    "v_mfma_f32_16x16x32_bf16 v[230:233], v[108:111], v[116:119], v[230:233]\n"
    "v_exp_f32_e64 v234, v234 clamp\n"
    "v_mfma_f32_16x16x32_bf16 v[222:225], v[108:111], v[112:115], v[222:225]\n"
    "v_exp_f32_e64 v235, v235 clamp\n"
    "v_mfma_f32_16x16x32_bf16 v[204:207], v[244:247], v[108:111], v[204:207]\n"
    "v_exp_f32_e64 v236, v236 clamp\n"
    "v_exp_f32_e64 v237, v237 clamp\n"
    "v_exp_f32_e64 v238, v238 clamp\n"
    "v_exp_f32_e64 v239, v239 clamp\n"
    "v_exp_f32_e64 v240, v240 clamp\n"
    "v_exp_f32_e64 v241, v241 clamp\n"
    "v_cvt_pk_bf16_f32 v160, v160, v161\n"
    "v_cvt_pk_bf16_f32 v161, v162, v163\n"
    "v_cvt_pk_bf16_f32 v162, v184, v185\n"
    "v_cvt_pk_bf16_f32 v163, v186, v187\n"
    "v_cvt_pk_bf16_f32 v164, v164, v165\n"
    "v_cvt_pk_bf16_f32 v165, v166, v167\n"
    "v_cvt_pk_bf16_f32 v166, v188, v189\n"
    "v_cvt_pk_bf16_f32 v167, v190, v191\n"
    "v_max_f32 v152, v234, v152\n"
    "v_max_f32 v153, v235, v153\n"
    "v_max_f32 v154, v236, v154\n"
    "v_max_f32 v155, v237, v155\n"
    "v_max_f32 v176, v238, v176\n"
    "v_max_f32 v177, v239, v177\n"
    "v_max_f32 v178, v240, v178\n"
    "v_max_f32 v179, v241, v179\n"
    "v_pk_add_f32 v[152:153], v[152:153], s[68:69]\n"
    "v_pk_add_f32 v[154:155], v[154:155], s[68:69]\n"
    "v_pk_add_f32 v[176:177], v[176:177], s[68:69]\n"
    "v_pk_add_f32 v[178:179], v[178:179], s[68:69]\n"
    "v_cvt_pk_bf16_f32 v152, v152, v153\n"
    "v_cvt_pk_bf16_f32 v153, v154, v155\n"
    "v_cvt_pk_bf16_f32 v154, v176, v177\n"
    "v_cvt_pk_bf16_f32 v155, v178, v179\n"
    "v_pk_fma_f32 v[234:235], v[156:157], s[66:67], s[66:67] neg_lo:[0,0,1] neg_hi:[0,0,1]\n"
    "v_pk_fma_f32 v[236:237], v[158:159], s[66:67], s[66:67] neg_lo:[0,0,1] neg_hi:[0,0,1]\n"
    "v_pk_fma_f32 v[238:239], v[180:181], s[66:67], s[66:67] neg_lo:[0,0,1] neg_hi:[0,0,1]\n"
    "v_pk_fma_f32 v[240:241], v[182:183], s[66:67], s[66:67] neg_lo:[0,0,1] neg_hi:[0,0,1]\n"
    "v_mfma_f32_16x16x32_bf16 v[226:229], v[152:155], v[164:167], v[226:229]\n"
    "v_exp_f32_e64 v234, v234 clamp\n"
    "v_mfma_f32_16x16x32_bf16 v[218:221], v[152:155], v[160:163], v[218:221]\n"
    "v_exp_f32_e64 v235, v235 clamp\n"
    "v_mfma_f32_16x16x32_bf16 v[196:199], v[244:247], v[152:155], v[196:199]\n"
    "v_exp_f32_e64 v236, v236 clamp\n"
    "v_exp_f32_e64 v237, v237 clamp\n"
    "v_exp_f32_e64 v238, v238 clamp\n"
    "v_exp_f32_e64 v239, v239 clamp\n"
    "v_exp_f32_e64 v240, v240 clamp\n"
    "v_exp_f32_e64 v241, v241 clamp\n"
    "v_max_f32 v156, v234, v156\n"
    "v_max_f32 v157, v235, v157\n"
    "v_max_f32 v158, v236, v158\n"
    "v_max_f32 v159, v237, v159\n"
    "v_max_f32 v180, v238, v180\n"
    "v_max_f32 v181, v239, v181\n"
    "v_max_f32 v182, v240, v182\n"
    "v_max_f32 v183, v241, v183\n"
    "v_pk_add_f32 v[156:157], v[156:157], s[68:69]\n"
    "v_pk_add_f32 v[158:159], v[158:159], s[68:69]\n"
    "v_pk_add_f32 v[180:181], v[180:181], s[68:69]\n"
    "v_pk_add_f32 v[182:183], v[182:183], s[68:69]\n"
    "v_cvt_pk_bf16_f32 v156, v156, v157\n"
    "v_cvt_pk_bf16_f32 v157, v158, v159\n"
    "v_cvt_pk_bf16_f32 v158, v180, v181\n"
    "v_cvt_pk_bf16_f32 v159, v182, v183\n"
    "s_nop 1\n"
    "v_mfma_f32_16x16x32_bf16 v[230:233], v[156:159], v[164:167], v[230:233]\n"
    "v_mfma_f32_16x16x32_bf16 v[222:225], v[156:159], v[160:163], v[222:225]\n"
    "v_mfma_f32_16x16x32_bf16 v[204:207], v[244:247], v[156:159], v[204:207]\n"
    "v_xor_b32 v193, 0x2000, v217\n"
    "v_bfe_u32 v195, v192, 4, 1\n"
    "v_cmp_eq_u32 vcc, 1, v195\n"
    "ds_write_b128 v217, v[226:229] offset:0\n"
    "s_nop 1\n"
    "ds_write_b128 v217, v[230:233] offset:1024\n"
    "s_nop 0\n"
    "v_cndmask_b32 v242, v196, v204, vcc\n"
    "ds_write_b32 v217, v242 offset:3072\n"
    "v_cmp_gt_u32 vcc, 32, v192\n"
    "s_waitcnt lgkmcnt(0)\n"
    "s_barrier\n"
    "ds_read_b128 v[226:229], v193 offset:0\n"
    "ds_read_b128 v[230:233], v193 offset:1024\n"
    "ds_read_b32 v195, v193 offset:3072\n"
    "ds_read_b128 a[64:67], v202 offset:0\n"
    "ds_read_b128 a[68:71], v203 offset:0\n"
    "ds_read_b128 a[72:75], v200 offset:0\n"
    "ds_read_b128 a[76:79], v201 offset:0\n"
    "ds_read_b128 a[80:83], v202 offset:2048\n"
    "ds_read_b128 a[84:87], v203 offset:2048\n"
    "ds_read_b128 a[88:91], v200 offset:2048\n"
    "ds_read_b128 a[92:95], v201 offset:2048\n"
    "s_waitcnt lgkmcnt(10)\n"
    "v_pk_add_f32 v[218:219], v[218:219], v[226:227]\n"
    "v_pk_add_f32 v[220:221], v[220:221], v[228:229]\n"
    "s_waitcnt lgkmcnt(9)\n"
    "v_pk_add_f32 v[222:223], v[222:223], v[230:231]\n"
    "v_pk_add_f32 v[224:225], v[224:225], v[232:233]\n"
    "v_cvt_pk_bf16_f32 v218, v218, v219\n"
    "v_cvt_pk_bf16_f32 v219, v220, v221\n"
    "v_cvt_pk_bf16_f32 v220, v222, v223\n"
    "v_cvt_pk_bf16_f32 v221, v224, v225\n"
    "buffer_store_dwordx4 v[218:221], v216, s[28:31], s85 offen sc1\n"
    "s_waitcnt lgkmcnt(8)\n"
    "v_add_f32 v242, v242, v195\n"
    "v_add3_u32 v194, v218, v219, v220\n"
    "v_cndmask_b32 v242, v250, v242, vcc\n"
    "v_add3_u32 v194, v194, v221, v242\n"
    "v_add_u32 v194, v194, v250\n"
    "v_sub_u32 v243, 0, v194\n"
    "buffer_store_dwordx2 v[242:243], v252, s[28:31], s85 offen offset:1024 sc1\n"
    "s_waitcnt lgkmcnt(4)\n"
    "v_mfma_f32_16x16x32_bf16 v[0:3], a[0:3], a[64:67], v[0:3]\n"
    "ds_read_b128 a[96:99], v202 offset:4096\n"
    "v_mfma_f32_16x16x32_bf16 v[0:3], a[4:7], a[68:71], v[0:3]\n"
    "v_lshlrev_b32 v194, 4, v253\n"
    "s_lshr_b32 s75, s93, 1\n"
    "s_lshl_b32 s75, s75, 4\n"
    "v_or_b32 v194, s75, v194\n"
    "v_xor_b32 v195, 0, v194\n"
    "buffer_load_dwordx4 v[226:229], v195, s[28:31], s89 offen sc1\n"
    "v_mfma_f32_16x16x32_bf16 v[0:3], a[48:51], a[72:75], v[0:3]\n"
    "ds_read_b128 a[100:103], v203 offset:4096\n"
    "v_mfma_f32_16x16x32_bf16 v[0:3], a[52:55], a[76:79], v[0:3]\n"
    "v_xor_b32 v195, 16, v194\n"
    "buffer_load_dwordx4 v[230:233], v195, s[28:31], s89 offen sc1\n"
    "v_mfma_f32_16x16x32_bf16 v[4:7], a[8:11], a[64:67], v[4:7]\n"
    "ds_read_b128 a[104:107], v200 offset:4096\n"
    "v_mfma_f32_16x16x32_bf16 v[4:7], a[12:15], a[68:71], v[4:7]\n"
    "v_xor_b32 v195, 32, v194\n"
    "buffer_load_dwordx4 v[234:237], v195, s[28:31], s89 offen sc1\n"
    "v_mfma_f32_16x16x32_bf16 v[4:7], a[56:59], a[72:75], v[4:7]\n"
    "ds_read_b128 a[108:111], v201 offset:4096\n"
    "v_mfma_f32_16x16x32_bf16 v[4:7], a[60:63], a[76:79], v[4:7]\n"
    "v_xor_b32 v195, 48, v194\n"
    "buffer_load_dwordx4 v[238:241], v195, s[28:31], s89 offen sc1\n"
    "v_lshrrev_b32 v246, 4, v192\n"
    "v_bfe_u32 v242, v192, 2, 2\n"
    "v_cmp_eq_u32 s[26:27], v242, v246\n"
    "v_and_b32 v242, 1, v192\n"
    "v_lshlrev_b32 v242, 4, v242\n"
    "v_mov_b32 v243, 0x3f80\n"
    "v_lshlrev_b32 v243, v242, v243\n"
    "v_cndmask_b32 v243, 0, v243, s[26:27]\n"
    "v_and_b32 v242, 2, v192\n"
    "v_cmp_eq_u32 s[26:27], 0, v242\n"
    "v_mov_b32 v246, 0\n"
    "v_mov_b32 v247, 0\n"
    "v_cndmask_b32 v244, 0, v243, s[26:27]\n"
    "v_cndmask_b32 v245, v243, 0, s[26:27]\n"
    "v_mov_b32 v242, 0\n"
    "v_mov_b32 v243, 0\n"
    "s_waitcnt lgkmcnt(4)\n"
    "v_mfma_f32_16x16x32_bf16 v[24:27], a[0:3], a[80:83], v[24:27]\n"
    "ds_read_b128 a[64:67], v202 offset:6144\n"
    "v_mfma_f32_16x16x32_bf16 v[24:27], a[4:7], a[84:87], v[24:27]\n"
    "v_pk_fma_f32 v[160:161], v[0:1], s[66:67], s[66:67] neg_lo:[0,0,1] neg_hi:[0,0,1]\n"
    "v_pk_fma_f32 v[162:163], v[2:3], s[66:67], s[66:67] neg_lo:[0,0,1] neg_hi:[0,0,1]\n"
    "v_pk_fma_f32 v[164:165], v[4:5], s[66:67], s[66:67] neg_lo:[0,0,1] neg_hi:[0,0,1]\n"
    "v_pk_fma_f32 v[166:167], v[6:7], s[66:67], s[66:67] neg_lo:[0,0,1] neg_hi:[0,0,1]\n"
    "v_mfma_f32_16x16x32_bf16 v[24:27], a[48:51], a[88:91], v[24:27]\n"
    "ds_read_b128 a[68:71], v203 offset:6144\n"
    "v_exp_f32_e64 v160, v160 clamp\n"
    "v_mfma_f32_16x16x32_bf16 v[24:27], a[52:55], a[92:95], v[24:27]\n"
    "v_exp_f32_e64 v161, v161 clamp\n"
    "v_mfma_f32_16x16x32_bf16 v[28:31], a[8:11], a[80:83], v[28:31]\n"
    "ds_read_b128 a[72:75], v200 offset:6144\n"
    "v_exp_f32_e64 v162, v162 clamp\n"
    "v_mfma_f32_16x16x32_bf16 v[28:31], a[12:15], a[84:87], v[28:31]\n"
    "v_exp_f32_e64 v163, v163 clamp\n"
    "v_mfma_f32_16x16x32_bf16 v[28:31], a[56:59], a[88:91], v[28:31]\n"
    "ds_read_b128 a[76:79], v201 offset:6144\n"
    "v_exp_f32_e64 v164, v164 clamp\n"
    "v_mfma_f32_16x16x32_bf16 v[28:31], a[60:63], a[92:95], v[28:31]\n"
    "v_exp_f32_e64 v165, v165 clamp\n"
    "v_exp_f32_e64 v166, v166 clamp\n"
    "v_exp_f32_e64 v167, v167 clamp\n"
    "v_max_f32 v0, v160, v0\n"
    "v_max_f32 v1, v161, v1\n"
    "v_max_f32 v2, v162, v2\n"
    "v_max_f32 v3, v163, v3\n"
    "v_max_f32 v4, v164, v4\n"
    "v_max_f32 v5, v165, v5\n"
    "v_max_f32 v6, v166, v6\n"
    "v_max_f32 v7, v167, v7\n"
    "v_pk_add_f32 v[0:1], v[0:1], s[68:69]\n"
    "v_pk_add_f32 v[2:3], v[2:3], s[68:69]\n"
    "v_pk_add_f32 v[4:5], v[4:5], s[68:69]\n"
    "v_pk_add_f32 v[6:7], v[6:7], s[68:69]\n"
    "v_cvt_pk_bf16_f32 v0, v0, v1\n"
    "v_cvt_pk_bf16_f32 v1, v2, v3\n"
    "v_cvt_pk_bf16_f32 v2, v4, v5\n"
    "v_cvt_pk_bf16_f32 v3, v6, v7\n"
    "s_waitcnt lgkmcnt(4)\n"
    "v_mfma_f32_16x16x32_bf16 v[48:51], a[0:3], a[96:99], v[48:51]\n"
    "ds_read_b128 a[80:83], v202 offset:8192\n"
    "v_mfma_f32_16x16x32_bf16 v[48:51], a[4:7], a[100:103], v[48:51]\n"
    "v_pk_fma_f32 v[184:185], v[24:25], s[66:67], s[66:67] neg_lo:[0,0,1] neg_hi:[0,0,1]\n"
    "v_pk_fma_f32 v[186:187], v[26:27], s[66:67], s[66:67] neg_lo:[0,0,1] neg_hi:[0,0,1]\n"
    "v_pk_fma_f32 v[188:189], v[28:29], s[66:67], s[66:67] neg_lo:[0,0,1] neg_hi:[0,0,1]\n"
    "v_pk_fma_f32 v[190:191], v[30:31], s[66:67], s[66:67] neg_lo:[0,0,1] neg_hi:[0,0,1]\n"
    "v_mfma_f32_16x16x32_bf16 v[48:51], a[48:51], a[104:107], v[48:51]\n"
    "ds_read_b128 a[84:87], v203 offset:8192\n"
    "v_exp_f32_e64 v184, v184 clamp\n"
    "v_mfma_f32_16x16x32_bf16 v[48:51], a[52:55], a[108:111], v[48:51]\n"
    "v_exp_f32_e64 v185, v185 clamp\n"
    "v_mfma_f32_16x16x32_bf16 v[52:55], a[8:11], a[96:99], v[52:55]\n"
    "ds_read_b128 a[88:91], v200 offset:8192\n"
    "v_exp_f32_e64 v186, v186 clamp\n"
    "v_mfma_f32_16x16x32_bf16 v[52:55], a[12:15], a[100:103], v[52:55]\n"
    "v_exp_f32_e64 v187, v187 clamp\n"
    "v_mfma_f32_16x16x32_bf16 v[52:55], a[56:59], a[104:107], v[52:55]\n"
    "ds_read_b128 a[92:95], v201 offset:8192\n"
    "v_exp_f32_e64 v188, v188 clamp\n"
    "v_mfma_f32_16x16x32_bf16 v[52:55], a[60:63], a[108:111], v[52:55]\n"
    "v_exp_f32_e64 v189, v189 clamp\n"
    "v_exp_f32_e64 v190, v190 clamp\n"
    "v_exp_f32_e64 v191, v191 clamp\n"
    "v_max_f32 v24, v184, v24\n"
    "s_add_u32 s63, s84, 0\n"
    "buffer_load_dwordx4 v[8:11], v216, s[28:31], s63 offen offset:0 sc1\n"
    "v_max_f32 v25, v185, v25\n"
    "v_max_f32 v26, v186, v26\n"
    "v_max_f32 v27, v187, v27\n"
    "buffer_load_dwordx2 v[112:113], v252, s[28:31], s63 offen offset:1024 sc1\n"
    "v_max_f32 v28, v188, v28\n"
    "v_max_f32 v29, v189, v29\n"
    "v_max_f32 v30, v190, v30\n"
    "s_add_u32 s63, s84, 3072\n"
    "buffer_load_dwordx4 v[32:35], v216, s[28:31], s63 offen offset:0 sc1\n"
    "v_max_f32 v31, v191, v31\n"
    "v_pk_add_f32 v[24:25], v[24:25], s[68:69]\n"
    "v_pk_add_f32 v[26:27], v[26:27], s[68:69]\n"
    "buffer_load_dwordx2 v[114:115], v252, s[28:31], s63 offen offset:1024 sc1\n"
    "v_pk_add_f32 v[28:29], v[28:29], s[68:69]\n"
    "v_pk_add_f32 v[30:31], v[30:31], s[68:69]\n"
    "v_cvt_pk_bf16_f32 v24, v24, v25\n"
    "s_add_u32 s63, s84, 6144\n"
    "buffer_load_dwordx4 v[56:59], v216, s[28:31], s63 offen offset:0 sc1\n"
    "v_cvt_pk_bf16_f32 v25, v26, v27\n"
    "v_cvt_pk_bf16_f32 v26, v28, v29\n"
    "v_cvt_pk_bf16_f32 v27, v30, v31\n"
    "s_waitcnt lgkmcnt(4)\n"
    "v_mfma_f32_16x16x32_bf16 v[72:75], a[0:3], a[64:67], v[72:75]\n"
    "ds_read_b128 a[96:99], v202 offset:10240\n"
    "v_mfma_f32_16x16x32_bf16 v[72:75], a[4:7], a[68:71], v[72:75]\n"
    "v_pk_fma_f32 v[160:161], v[48:49], s[66:67], s[66:67] neg_lo:[0,0,1] neg_hi:[0,0,1]\n"
    "v_pk_fma_f32 v[162:163], v[50:51], s[66:67], s[66:67] neg_lo:[0,0,1] neg_hi:[0,0,1]\n"
    "v_pk_fma_f32 v[164:165], v[52:53], s[66:67], s[66:67] neg_lo:[0,0,1] neg_hi:[0,0,1]\n"
    "v_pk_fma_f32 v[166:167], v[54:55], s[66:67], s[66:67] neg_lo:[0,0,1] neg_hi:[0,0,1]\n"
    "v_mfma_f32_16x16x32_bf16 v[72:75], a[48:51], a[72:75], v[72:75]\n"
    "ds_read_b128 a[100:103], v203 offset:10240\n"
    "v_exp_f32_e64 v160, v160 clamp\n"
    "v_mfma_f32_16x16x32_bf16 v[72:75], a[52:55], a[76:79], v[72:75]\n"
    "v_exp_f32_e64 v161, v161 clamp\n"
    "v_mfma_f32_16x16x32_bf16 v[76:79], a[8:11], a[64:67], v[76:79]\n"
    "ds_read_b128 a[104:107], v200 offset:10240\n"
    "v_exp_f32_e64 v162, v162 clamp\n"
    "v_mfma_f32_16x16x32_bf16 v[76:79], a[12:15], a[68:71], v[76:79]\n"
    "v_exp_f32_e64 v163, v163 clamp\n"
    "v_mfma_f32_16x16x32_bf16 v[76:79], a[56:59], a[72:75], v[76:79]\n"
    "ds_read_b128 a[108:111], v201 offset:10240\n"
    "v_exp_f32_e64 v164, v164 clamp\n"
    "v_mfma_f32_16x16x32_bf16 v[76:79], a[60:63], a[76:79], v[76:79]\n"
    "v_exp_f32_e64 v165, v165 clamp\n"
    "v_exp_f32_e64 v166, v166 clamp\n"
    "v_exp_f32_e64 v167, v167 clamp\n"
    "v_max_f32 v48, v160, v48\n"
    "buffer_load_dwordx2 v[116:117], v252, s[28:31], s63 offen offset:1024 sc1\n"
    "v_max_f32 v49, v161, v49\n"
    "v_max_f32 v50, v162, v50\n"
    "v_max_f32 v51, v163, v51\n"
    "s_add_u32 s63, s84, 9216\n"
    "buffer_load_dwordx4 v[80:83], v216, s[28:31], s63 offen offset:0 sc1\n"
    "v_max_f32 v52, v164, v52\n"
    "v_max_f32 v53, v165, v53\n"
    "v_max_f32 v54, v166, v54\n"
    "buffer_load_dwordx2 v[118:119], v252, s[28:31], s63 offen offset:1024 sc1\n"
    "v_max_f32 v55, v167, v55\n"
    "v_pk_add_f32 v[48:49], v[48:49], s[68:69]\n"
    "v_pk_add_f32 v[50:51], v[50:51], s[68:69]\n"
    "s_add_u32 s63, s84, 12288\n"
    "buffer_load_dwordx4 v[104:107], v216, s[28:31], s63 offen offset:0 sc1\n"
    "v_pk_add_f32 v[52:53], v[52:53], s[68:69]\n"
    "v_pk_add_f32 v[54:55], v[54:55], s[68:69]\n"
    "v_cvt_pk_bf16_f32 v48, v48, v49\n"
    "buffer_load_dwordx2 v[136:137], v252, s[28:31], s63 offen offset:1024 sc1\n"
    "v_cvt_pk_bf16_f32 v49, v50, v51\n"
    "v_cvt_pk_bf16_f32 v50, v52, v53\n"
    "v_cvt_pk_bf16_f32 v51, v54, v55\n"
    "v_pk_fma_f32 v[184:185], v[72:73], s[66:67], s[66:67] neg_lo:[0,0,1] neg_hi:[0,0,1]\n"
    "v_pk_fma_f32 v[186:187], v[74:75], s[66:67], s[66:67] neg_lo:[0,0,1] neg_hi:[0,0,1]\n"
    "v_pk_fma_f32 v[188:189], v[76:77], s[66:67], s[66:67] neg_lo:[0,0,1] neg_hi:[0,0,1]\n"
    "v_pk_fma_f32 v[190:191], v[78:79], s[66:67], s[66:67] neg_lo:[0,0,1] neg_hi:[0,0,1]\n"
    "v_exp_f32_e64 v184, v184 clamp\n"
    "v_exp_f32_e64 v185, v185 clamp\n"
    "v_exp_f32_e64 v186, v186 clamp\n"
    "v_exp_f32_e64 v187, v187 clamp\n"
    "s_add_u32 s63, s84, 15360\n"
    "buffer_load_dwordx4 v[128:131], v216, s[28:31], s63 offen offset:0 sc1\n"
    "v_exp_f32_e64 v188, v188 clamp\n"
    "v_exp_f32_e64 v189, v189 clamp\n"
    "v_exp_f32_e64 v190, v190 clamp\n"
    "v_exp_f32_e64 v191, v191 clamp\n"
    "buffer_load_dwordx2 v[138:139], v252, s[28:31], s63 offen offset:1024 sc1\n"
    "v_max_f32 v72, v184, v72\n"
    "v_max_f32 v73, v185, v73\n"
    "v_max_f32 v74, v186, v74\n"
    "v_max_f32 v75, v187, v75\n"
    "s_add_u32 s63, s84, 18432\n"
    "buffer_load_dwordx4 v[152:155], v216, s[28:31], s63 offen offset:0 sc1\n"
    "v_max_f32 v76, v188, v76\n"
    "v_max_f32 v77, v189, v77\n"
    "v_max_f32 v78, v190, v78\n"
    "v_max_f32 v79, v191, v79\n"
    "buffer_load_dwordx2 v[140:141], v252, s[28:31], s63 offen offset:1024 sc1\n"
    "v_pk_add_f32 v[72:73], v[72:73], s[68:69]\n"
    "v_pk_add_f32 v[74:75], v[74:75], s[68:69]\n"
    "v_pk_add_f32 v[76:77], v[76:77], s[68:69]\n"
    "v_pk_add_f32 v[78:79], v[78:79], s[68:69]\n"
    "s_add_u32 s63, s84, 21504\n"
    "buffer_load_dwordx4 v[176:179], v216, s[28:31], s63 offen offset:0 sc1\n"
    "v_cvt_pk_bf16_f32 v72, v72, v73\n"
    "v_cvt_pk_bf16_f32 v73, v74, v75\n"
    "v_cvt_pk_bf16_f32 v74, v76, v77\n"
    "v_cvt_pk_bf16_f32 v75, v78, v79\n"
    "buffer_load_dwordx2 v[142:143], v252, s[28:31], s63 offen offset:1024 sc1\n"
    "s_waitcnt vmcnt(38)\n"
    "ds_read_b64_tr_b16 a[16:17], v212 offset:0\n"
    "ds_read_b64_tr_b16 a[18:19], v212 offset:2048\n"
    "ds_read_b64_tr_b16 a[20:21], v213 offset:0\n"
    "ds_read_b64_tr_b16 a[22:23], v213 offset:2048\n"
    "ds_read_b64_tr_b16 a[24:25], v214 offset:0\n"
    "ds_read_b64_tr_b16 a[26:27], v214 offset:2048\n"
    "ds_read_b64_tr_b16 a[28:29], v215 offset:0\n"
    "ds_read_b64_tr_b16 a[30:31], v215 offset:2048\n"
    "s_waitcnt vmcnt(34)\n"
    "ds_read_b64_tr_b16 a[32:33], v212 offset:16384\n"
    "ds_read_b64_tr_b16 a[34:35], v212 offset:18432\n"
    "ds_read_b64_tr_b16 a[36:37], v213 offset:16384\n"
    "ds_read_b64_tr_b16 a[38:39], v213 offset:18432\n"
    "ds_read_b64_tr_b16 a[112:113], v214 offset:16384\n"
    "ds_read_b64_tr_b16 a[114:115], v214 offset:18432\n"
    "ds_read_b64_tr_b16 a[116:117], v215 offset:16384\n"
    "ds_read_b64_tr_b16 a[118:119], v215 offset:18432\n"
    "s_waitcnt lgkmcnt(0)\n"
    "s_add_u32 s63, s65, 393344\n"
    "s_and_b32 s63, s63, 0x7ffff\n"
    "s_add_u32 m0, s64, 98304\n"
    "s_nop 0\n"
    "buffer_load_dwordx4 v208, s[32:35], s63 offen offset:0 lds\n"
    "buffer_load_dwordx4 v209, s[32:35], s63 offen offset:1024 lds\n"
    "buffer_load_dwordx4 v210, s[32:35], s63 offen offset:2048 lds\n"
    "buffer_load_dwordx4 v211, s[32:35], s63 offen offset:3072 lds\n"
    "s_add_u32 s63, s65, 458752\n"
    "s_and_b32 s63, s63, 0x7ffff\n"
    "s_add_u32 m0, s64, 114688\n"
    "s_nop 0\n"
    "buffer_load_dwordx4 v208, s[32:35], s63 offen offset:0 lds\n"
    "buffer_load_dwordx4 v209, s[32:35], s63 offen offset:1024 lds\n"
    "buffer_load_dwordx4 v210, s[32:35], s63 offen offset:2048 lds\n"
    "buffer_load_dwordx4 v211, s[32:35], s63 offen offset:3072 lds\n"
    "Lsum%=:\n"
    "v_cmp_gt_u32 vcc, 32, v192\n"
    "s_waitcnt vmcnt(20)\n"
    "v_mfma_f32_16x16x32_bf16 v[16:19], v[244:247], v[8:11], 0\n"
    "v_add_f32 v52, v112, v114\n"
    "v_add3_u32 v4, v8, v9, v10\n"
    "v_mfma_f32_16x16x32_bf16 v[20:23], v[242:245], v[8:11], 0\n"
    "v_add3_u32 v5, v32, v33, v34\n"
    "v_add3_u32 v4, v4, v11, v112\n"
    "v_mfma_f32_16x16x32_bf16 v[16:19], v[244:247], v[32:35], v[16:19]\n"
    "v_add3_u32 v5, v5, v35, v114\n"
    "v_add3_u32 v4, v4, v113, v250\n"
    "v_mfma_f32_16x16x32_bf16 v[20:23], v[242:245], v[32:35], v[20:23]\n"
    "v_add3_u32 v5, v5, v115, v250\n"
    "s_waitcnt vmcnt(16)\n"
    "v_mfma_f32_16x16x32_bf16 v[16:19], v[244:247], v[56:59], v[16:19]\n"
    "v_add_f32 v53, v116, v118\n"
    "v_add3_u32 v6, v56, v57, v58\n"
    "v_mfma_f32_16x16x32_bf16 v[20:23], v[242:245], v[56:59], v[20:23]\n"
    "v_add3_u32 v7, v80, v81, v82\n"
    "v_add3_u32 v6, v6, v59, v116\n"
    "v_mfma_f32_16x16x32_bf16 v[16:19], v[244:247], v[80:83], v[16:19]\n"
    "v_add3_u32 v7, v7, v83, v118\n"
    "v_add3_u32 v6, v6, v117, v250\n"
    "v_mfma_f32_16x16x32_bf16 v[20:23], v[242:245], v[80:83], v[20:23]\n"
    "v_add3_u32 v7, v7, v119, v250\n"
    "v_add_f32 v52, v52, v53\n"
    "v_or3_b32 v194, v4, v5, v6\n"
    "s_waitcnt vmcnt(12)\n"
    "v_mfma_f32_16x16x32_bf16 v[16:19], v[244:247], v[104:107], v[16:19]\n"
    "v_add_f32 v54, v136, v138\n"
    "v_add3_u32 v28, v104, v105, v106\n"
    "v_mfma_f32_16x16x32_bf16 v[20:23], v[242:245], v[104:107], v[20:23]\n"
    "v_add3_u32 v29, v128, v129, v130\n"
    "v_add3_u32 v28, v28, v107, v136\n"
    "v_mfma_f32_16x16x32_bf16 v[16:19], v[244:247], v[128:131], v[16:19]\n"
    "v_add3_u32 v29, v29, v131, v138\n"
    "v_add3_u32 v28, v28, v137, v250\n"
    "v_mfma_f32_16x16x32_bf16 v[20:23], v[242:245], v[128:131], v[20:23]\n"
    "v_add3_u32 v29, v29, v139, v250\n"
    "v_or3_b32 v194, v194, v7, v28\n"
    "v_or_b32 v194, v194, v29\n"
    "s_waitcnt vmcnt(8)\n"
    "v_mfma_f32_16x16x32_bf16 v[16:19], v[244:247], v[152:155], v[16:19]\n"
    "v_add_f32 v55, v140, v142\n"
    "v_add_f32 v54, v54, v55\n"
    "v_mfma_f32_16x16x32_bf16 v[20:23], v[242:245], v[152:155], v[20:23]\n"
    "v_add_f32 v52, v52, v54\n"
    "v_cndmask_b32 v52, 0, v52, vcc\n"
    "v_mfma_f32_16x16x32_bf16 v[16:19], v[244:247], v[176:179], v[16:19]\n"
    "v_add3_u32 v30, v152, v153, v154\n"
    "v_add3_u32 v31, v176, v177, v178\n"
    "v_mfma_f32_16x16x32_bf16 v[20:23], v[242:245], v[176:179], v[20:23]\n"
    "v_add3_u32 v30, v30, v155, v140\n"
    "v_add3_u32 v31, v31, v179, v142\n"
    "v_mfma_f32_16x16x4_f32 v[196:199], v52, v248, 0\n"
    "v_mfma_f32_16x16x4_f32 v[204:207], v52, v254, 0\n"
    "v_add3_u32 v30, v30, v141, v250\n"
    "v_add3_u32 v31, v31, v143, v250\n"
    "v_or3_b32 v194, v194, v30, v31\n"
    "v_cmp_ne_u32 vcc, 0, v194\n"
    "s_nop 4\n"
    "s_cbranch_vccnz Lks%=\n"
    "Lkok%=:\n"
    "v_cvt_pk_bf16_f32 v218, v16, v17\n"
    "v_cvt_pk_bf16_f32 v219, v18, v19\n"
    "v_cvt_pk_bf16_f32 v220, v20, v21\n"
    "v_cvt_pk_bf16_f32 v221, v22, v23\n"
    "ds_write_b128 v217, v[218:221] offset:2048\n"
    "v_cvt_pk_bf16_f32 v196, v196, v197\n"
    "v_cvt_pk_bf16_f32 v197, v198, v199\n"
    "v_cvt_pk_bf16_f32 v198, v204, v205\n"
    "v_cvt_pk_bf16_f32 v199, v206, v207\n"
    "s_nop 1\n"
    "v_mfma_f32_16x16x32_bf16 v[16:19], v[196:199], v[0:3], 0\n"
    "v_mfma_f32_16x16x32_bf16 v[40:43], v[196:199], v[24:27], 0\n"
    "v_mfma_f32_16x16x32_bf16 v[64:67], v[196:199], v[48:51], 0\n"
    "v_mfma_f32_16x16x32_bf16 v[88:91], v[196:199], v[72:75], 0\n"
    "s_waitcnt lgkmcnt(0)\n"
    "s_barrier\n"
    "ds_read_b128 v[222:225], v193 offset:2048\n"
    "v_or3_b32 v194, v227, v229, v231\n"
    "v_bitop3_b32 v195, v227, v229, v231 bitop3:0x80\n"
    "v_or3_b32 v194, v194, v233, v235\n"
    "v_bitop3_b32 v195, v195, v233, v235 bitop3:0x80\n"
    "v_or3_b32 v194, v194, v237, v239\n"
    "v_bitop3_b32 v195, v195, v237, v239 bitop3:0x80\n"
    "v_or3_b32 v194, v194, v241, v250\n"
    "v_bitop3_b32 v195, v195, v241, v250 bitop3:0x80\n"
    "v_cmp_ne_u32 vcc, v194, v195\n"
    "s_nop 4\n"
    "s_cbranch_vccnz Lgs%=\n"
    "Lgok%=:\n"
    "s_bitcmp1_b32 s93, 0\n"
    "s_cbranch_scc0 Lgsw%=\n"
    "v_swap_b32 v226, v228\n"
    "v_swap_b32 v230, v232\n"
    "v_swap_b32 v234, v236\n"
    "v_swap_b32 v238, v240\n"
    "Lgsw%=:\n"
    "v_mfma_f32_16x16x32_bf16 v[8:11], v[218:221], v[0:3], 0\n"
    "v_add_f32 v16, s68, v16\n"
    "v_add_f32 v40, s68, v40\n"
    "v_mfma_f32_16x16x32_bf16 v[32:35], v[218:221], v[24:27], 0\n"
    "v_add_f32 v64, s68, v64\n"
    "v_add_f32 v88, s68, v88\n"
    "v_mfma_f32_16x16x32_bf16 v[56:59], v[218:221], v[48:51], 0\n"
    "v_rcp_f32 v16, v16\n"
    "v_mfma_f32_16x16x32_bf16 v[80:83], v[218:221], v[72:75], 0\n"
    "v_rcp_f32 v40, v40\n"
    "s_waitcnt lgkmcnt(0)\n"
    "v_mfma_f32_16x16x32_bf16 v[12:15], v[222:225], v[0:3], 0\n"
    "v_rcp_f32 v64, v64\n"
    "v_mfma_f32_16x16x32_bf16 v[36:39], v[222:225], v[24:27], 0\n"
    "v_rcp_f32 v88, v88\n"
    "v_mfma_f32_16x16x32_bf16 v[60:63], v[222:225], v[48:51], 0\n"
    "v_mul_f32 v16, v16, v226\n"
    "v_mul_f32 v40, v40, v228\n"
    "v_mfma_f32_16x16x32_bf16 v[84:87], v[222:225], v[72:75], 0\n"
    "v_mul_f32 v64, v64, v230\n"
    "v_mul_f32 v88, v88, v232\n"
    "v_pk_mul_f32 v[8:9], v[8:9], v[16:17] op_sel_hi:[1,0]\n"
    "v_pk_mul_f32 v[10:11], v[10:11], v[16:17] op_sel_hi:[1,0]\n"
    "v_pk_mul_f32 v[12:13], v[12:13], v[16:17] op_sel_hi:[1,0]\n"
    "v_pk_mul_f32 v[14:15], v[14:15], v[16:17] op_sel_hi:[1,0]\n"
    "v_cvt_pk_bf16_f32 v20, v8, v9\n"
    "v_cvt_pk_bf16_f32 v21, v10, v11\n"
    "v_cvt_pk_bf16_f32 v22, v12, v13\n"
    "v_cvt_pk_bf16_f32 v23, v14, v15\n"
    "buffer_store_dwordx4 v[20:23], v216, s[28:31], s87 offen offset:0 sc1\n"
    "v_pk_mul_f32 v[32:33], v[32:33], v[40:41] op_sel_hi:[1,0]\n"
    "v_pk_mul_f32 v[34:35], v[34:35], v[40:41] op_sel_hi:[1,0]\n"
    "v_pk_mul_f32 v[36:37], v[36:37], v[40:41] op_sel_hi:[1,0]\n"
    "v_pk_mul_f32 v[38:39], v[38:39], v[40:41] op_sel_hi:[1,0]\n"
    "v_cvt_pk_bf16_f32 v44, v32, v33\n"
    "v_cvt_pk_bf16_f32 v45, v34, v35\n"
    "v_cvt_pk_bf16_f32 v46, v36, v37\n"
    "v_cvt_pk_bf16_f32 v47, v38, v39\n"
    "buffer_store_dwordx4 v[44:47], v216, s[28:31], s87 offen offset:1024 sc1\n"
    "v_pk_mul_f32 v[56:57], v[56:57], v[64:65] op_sel_hi:[1,0]\n"
    "v_pk_mul_f32 v[58:59], v[58:59], v[64:65] op_sel_hi:[1,0]\n"
    "v_pk_mul_f32 v[60:61], v[60:61], v[64:65] op_sel_hi:[1,0]\n"
    "v_pk_mul_f32 v[62:63], v[62:63], v[64:65] op_sel_hi:[1,0]\n"
    "v_cvt_pk_bf16_f32 v68, v56, v57\n"
    "v_cvt_pk_bf16_f32 v69, v58, v59\n"
    "v_cvt_pk_bf16_f32 v70, v60, v61\n"
    "v_cvt_pk_bf16_f32 v71, v62, v63\n"
    "buffer_store_dwordx4 v[68:71], v216, s[28:31], s87 offen offset:2048 sc1\n"
    "buffer_store_dword v250, v251, s[28:31], s83 offen sc1\n"
    "v_pk_mul_f32 v[80:81], v[80:81], v[88:89] op_sel_hi:[1,0]\n"
    "v_pk_mul_f32 v[82:83], v[82:83], v[88:89] op_sel_hi:[1,0]\n"
    "v_pk_mul_f32 v[84:85], v[84:85], v[88:89] op_sel_hi:[1,0]\n"
    "v_pk_mul_f32 v[86:87], v[86:87], v[88:89] op_sel_hi:[1,0]\n"
    "v_cvt_pk_bf16_f32 v92, v80, v81\n"
    "v_cvt_pk_bf16_f32 v93, v82, v83\n"
    "v_cvt_pk_bf16_f32 v94, v84, v85\n"
    "v_cvt_pk_bf16_f32 v95, v86, v87\n"
    "buffer_store_dwordx4 v[92:95], v216, s[28:31], s87 offen offset:3072 sc1\n"
    "v_mfma_f32_16x16x32_bf16 v[96:99], a[0:3], a[80:83], v[96:99]\n"
    "ds_read_b128 a[64:67], v202 offset:12288\n"
    "v_add3_u32 v204, v20, v21, 0\n"
    "v_mfma_f32_16x16x32_bf16 v[96:99], a[4:7], a[84:87], v[96:99]\n"
    "v_add3_u32 v204, v22, v23, v204\n"
    "v_mfma_f32_16x16x32_bf16 v[96:99], a[48:51], a[88:91], v[96:99]\n"
    "ds_read_b128 a[68:71], v203 offset:12288\n"
    "v_add3_u32 v204, v44, v45, v204\n"
    "v_mfma_f32_16x16x32_bf16 v[96:99], a[52:55], a[92:95], v[96:99]\n"
    "v_add3_u32 v204, v46, v47, v204\n"
    "v_mfma_f32_16x16x32_bf16 v[100:103], a[8:11], a[80:83], v[100:103]\n"
    "ds_read_b128 a[72:75], v200 offset:12288\n"
    "v_add3_u32 v204, v68, v69, v204\n"
    "v_mfma_f32_16x16x32_bf16 v[100:103], a[12:15], a[84:87], v[100:103]\n"
    "v_add3_u32 v204, v70, v71, v204\n"
    "v_mfma_f32_16x16x32_bf16 v[100:103], a[56:59], a[88:91], v[100:103]\n"
    "ds_read_b128 a[76:79], v201 offset:12288\n"
    "v_add3_u32 v204, v92, v93, v204\n"
    "v_mfma_f32_16x16x32_bf16 v[100:103], a[60:63], a[92:95], v[100:103]\n"
    "v_add3_u32 v204, v94, v95, v204\n"
    "v_mfma_f32_16x16x32_bf16 v[120:123], a[0:3], a[96:99], v[120:123]\n"
    "ds_read_b128 a[80:83], v202 offset:14336\n"
    "v_mfma_f32_16x16x32_bf16 v[120:123], a[4:7], a[100:103], v[120:123]\n"
    "v_pk_fma_f32 v[80:81], v[96:97], s[66:67], s[66:67] neg_lo:[0,0,1] neg_hi:[0,0,1]\n"
    "v_pk_fma_f32 v[82:83], v[98:99], s[66:67], s[66:67] neg_lo:[0,0,1] neg_hi:[0,0,1]\n"
    "s_nop 1\n"
    "v_pk_fma_f32 v[84:85], v[100:101], s[66:67], s[66:67] neg_lo:[0,0,1] neg_hi:[0,0,1]\n"
    "v_pk_fma_f32 v[86:87], v[102:103], s[66:67], s[66:67] neg_lo:[0,0,1] neg_hi:[0,0,1]\n"
    "v_mfma_f32_16x16x32_bf16 v[120:123], a[48:51], a[104:107], v[120:123]\n"
    "ds_read_b128 a[84:87], v203 offset:14336\n"
    "v_exp_f32_e64 v80, v80 clamp\n"
    "v_mfma_f32_16x16x32_bf16 v[120:123], a[52:55], a[108:111], v[120:123]\n"
    "v_exp_f32_e64 v81, v81 clamp\n"
    "v_mfma_f32_16x16x32_bf16 v[124:127], a[8:11], a[96:99], v[124:127]\n"
    "ds_read_b128 a[88:91], v200 offset:14336\n"
    "v_exp_f32_e64 v82, v82 clamp\n"
    "v_mfma_f32_16x16x32_bf16 v[124:127], a[12:15], a[100:103], v[124:127]\n"
    "v_exp_f32_e64 v83, v83 clamp\n"
    "v_mfma_f32_16x16x32_bf16 v[124:127], a[56:59], a[104:107], v[124:127]\n"
    "ds_read_b128 a[92:95], v201 offset:14336\n"
    "v_exp_f32_e64 v84, v84 clamp\n"
    "v_mfma_f32_16x16x32_bf16 v[124:127], a[60:63], a[108:111], v[124:127]\n"
    "v_exp_f32_e64 v85, v85 clamp\n"
    "v_exp_f32_e64 v86, v86 clamp\n"
    "v_exp_f32_e64 v87, v87 clamp\n"
    "v_max_f32 v96, v80, v96\n"
    "v_max_f32 v97, v81, v97\n"
    "v_max_f32 v98, v82, v98\n"
    "v_max_f32 v99, v83, v99\n"
    "v_max_f32 v100, v84, v100\n"
    "v_max_f32 v101, v85, v101\n"
    "v_max_f32 v102, v86, v102\n"
    "v_max_f32 v103, v87, v103\n"
    "v_pk_add_f32 v[96:97], v[96:97], s[68:69]\n"
    "v_pk_add_f32 v[98:99], v[98:99], s[68:69]\n"
    "v_pk_add_f32 v[100:101], v[100:101], s[68:69]\n"
    "v_pk_add_f32 v[102:103], v[102:103], s[68:69]\n"
    "v_cvt_pk_bf16_f32 v96, v96, v97\n"
    "v_cvt_pk_bf16_f32 v97, v98, v99\n"
    "v_cvt_pk_bf16_f32 v98, v100, v101\n"
    "v_cvt_pk_bf16_f32 v99, v102, v103\n"
    "s_waitcnt lgkmcnt(4)\n"
    "v_mfma_f32_16x16x32_bf16 v[144:147], a[0:3], a[64:67], v[144:147]\n"
    "v_mfma_f32_16x16x32_bf16 v[144:147], a[4:7], a[68:71], v[144:147]\n"
    "v_pk_fma_f32 v[88:89], v[120:121], s[66:67], s[66:67] neg_lo:[0,0,1] neg_hi:[0,0,1]\n"
    "v_pk_fma_f32 v[90:91], v[122:123], s[66:67], s[66:67] neg_lo:[0,0,1] neg_hi:[0,0,1]\n"
    "v_pk_fma_f32 v[92:93], v[124:125], s[66:67], s[66:67] neg_lo:[0,0,1] neg_hi:[0,0,1]\n"
    "v_pk_fma_f32 v[94:95], v[126:127], s[66:67], s[66:67] neg_lo:[0,0,1] neg_hi:[0,0,1]\n"
    "v_mfma_f32_16x16x32_bf16 v[112:115], v[196:199], v[96:99], 0\n"
    "v_exp_f32_e64 v88, v88 clamp\n"
    "v_mfma_f32_16x16x32_bf16 v[144:147], a[48:51], a[72:75], v[144:147]\n"
    "v_exp_f32_e64 v89, v89 clamp\n"
    "v_mfma_f32_16x16x32_bf16 v[144:147], a[52:55], a[76:79], v[144:147]\n"
    "v_exp_f32_e64 v90, v90 clamp\n"
    "v_mfma_f32_16x16x32_bf16 v[148:151], a[8:11], a[64:67], v[148:151]\n"
    "v_exp_f32_e64 v91, v91 clamp\n"
    "v_mfma_f32_16x16x32_bf16 v[148:151], a[12:15], a[68:71], v[148:151]\n"
    "v_exp_f32_e64 v92, v92 clamp\n"
    "v_mfma_f32_16x16x32_bf16 v[148:151], a[56:59], a[72:75], v[148:151]\n"
    "v_exp_f32_e64 v93, v93 clamp\n"
    "v_mfma_f32_16x16x32_bf16 v[148:151], a[60:63], a[76:79], v[148:151]\n"
    "v_exp_f32_e64 v94, v94 clamp\n"
    "v_exp_f32_e64 v95, v95 clamp\n"
    "v_max_f32 v120, v88, v120\n"
    "v_max_f32 v121, v89, v121\n"
    "v_max_f32 v122, v90, v122\n"
    "v_max_f32 v123, v91, v123\n"
    "v_max_f32 v124, v92, v124\n"
    "v_max_f32 v125, v93, v125\n"
    "v_max_f32 v126, v94, v126\n"
    "v_max_f32 v127, v95, v127\n"
    "v_pk_add_f32 v[120:121], v[120:121], s[68:69]\n"
    "v_pk_add_f32 v[122:123], v[122:123], s[68:69]\n"
    "v_pk_add_f32 v[124:125], v[124:125], s[68:69]\n"
    "v_pk_add_f32 v[126:127], v[126:127], s[68:69]\n"
    "v_cvt_pk_bf16_f32 v120, v120, v121\n"
    "v_cvt_pk_bf16_f32 v121, v122, v123\n"
    "v_cvt_pk_bf16_f32 v122, v124, v125\n"
    "v_cvt_pk_bf16_f32 v123, v126, v127\n"
    "s_waitcnt lgkmcnt(0)\n"
    "v_mfma_f32_16x16x32_bf16 v[168:171], a[0:3], a[80:83], v[168:171]\n"
    "v_mfma_f32_16x16x32_bf16 v[168:171], a[4:7], a[84:87], v[168:171]\n"
    "v_pk_fma_f32 v[80:81], v[144:145], s[66:67], s[66:67] neg_lo:[0,0,1] neg_hi:[0,0,1]\n"
    "v_pk_fma_f32 v[82:83], v[146:147], s[66:67], s[66:67] neg_lo:[0,0,1] neg_hi:[0,0,1]\n"
    "v_pk_fma_f32 v[84:85], v[148:149], s[66:67], s[66:67] neg_lo:[0,0,1] neg_hi:[0,0,1]\n"
    "v_pk_fma_f32 v[86:87], v[150:151], s[66:67], s[66:67] neg_lo:[0,0,1] neg_hi:[0,0,1]\n"
    "v_mfma_f32_16x16x32_bf16 v[136:139], v[196:199], v[120:123], 0\n"
    "v_exp_f32_e64 v80, v80 clamp\n"
    "v_mfma_f32_16x16x32_bf16 v[168:171], a[48:51], a[88:91], v[168:171]\n"
    "v_exp_f32_e64 v81, v81 clamp\n"
    "v_mfma_f32_16x16x32_bf16 v[168:171], a[52:55], a[92:95], v[168:171]\n"
    "v_exp_f32_e64 v82, v82 clamp\n"
    "v_mfma_f32_16x16x32_bf16 v[172:175], a[8:11], a[80:83], v[172:175]\n"
    "v_exp_f32_e64 v83, v83 clamp\n"
    "v_mfma_f32_16x16x32_bf16 v[172:175], a[12:15], a[84:87], v[172:175]\n"
    "v_exp_f32_e64 v84, v84 clamp\n"
    "v_mfma_f32_16x16x32_bf16 v[172:175], a[56:59], a[88:91], v[172:175]\n"
    "v_exp_f32_e64 v85, v85 clamp\n"
    "v_mfma_f32_16x16x32_bf16 v[172:175], a[60:63], a[92:95], v[172:175]\n"
    "v_exp_f32_e64 v86, v86 clamp\n"
    "v_exp_f32_e64 v87, v87 clamp\n"
    "v_max_f32 v144, v80, v144\n"
    "v_max_f32 v145, v81, v145\n"
    "v_max_f32 v146, v82, v146\n"
    "v_max_f32 v147, v83, v147\n"
    "v_max_f32 v148, v84, v148\n"
    "v_max_f32 v149, v85, v149\n"
    "v_max_f32 v150, v86, v150\n"
    "v_max_f32 v151, v87, v151\n"
    "v_pk_add_f32 v[144:145], v[144:145], s[68:69]\n"
    "v_pk_add_f32 v[146:147], v[146:147], s[68:69]\n"
    "v_pk_add_f32 v[148:149], v[148:149], s[68:69]\n"
    "v_pk_add_f32 v[150:151], v[150:151], s[68:69]\n"
    "v_cvt_pk_bf16_f32 v144, v144, v145\n"
    "v_cvt_pk_bf16_f32 v145, v146, v147\n"
    "v_cvt_pk_bf16_f32 v146, v148, v149\n"
    "v_cvt_pk_bf16_f32 v147, v150, v151\n"
    "v_pk_fma_f32 v[88:89], v[168:169], s[66:67], s[66:67] neg_lo:[0,0,1] neg_hi:[0,0,1]\n"
    "v_pk_fma_f32 v[90:91], v[170:171], s[66:67], s[66:67] neg_lo:[0,0,1] neg_hi:[0,0,1]\n"
    "v_pk_fma_f32 v[92:93], v[172:173], s[66:67], s[66:67] neg_lo:[0,0,1] neg_hi:[0,0,1]\n"
    "v_pk_fma_f32 v[94:95], v[174:175], s[66:67], s[66:67] neg_lo:[0,0,1] neg_hi:[0,0,1]\n"
    "v_mfma_f32_16x16x32_bf16 v[160:163], v[196:199], v[144:147], 0\n"
    "v_exp_f32_e64 v88, v88 clamp\n"
    "v_exp_f32_e64 v89, v89 clamp\n"
    "v_exp_f32_e64 v90, v90 clamp\n"
    "v_exp_f32_e64 v91, v91 clamp\n"
    "v_exp_f32_e64 v92, v92 clamp\n"
    "v_exp_f32_e64 v93, v93 clamp\n"
    "v_exp_f32_e64 v94, v94 clamp\n"
    "v_exp_f32_e64 v95, v95 clamp\n"
    "v_max_f32 v168, v88, v168\n"
    "v_max_f32 v169, v89, v169\n"
    "v_max_f32 v170, v90, v170\n"
    "v_max_f32 v171, v91, v171\n"
    "v_max_f32 v172, v92, v172\n"
    "v_max_f32 v173, v93, v173\n"
    "v_max_f32 v174, v94, v174\n"
    "v_max_f32 v175, v95, v175\n"
    "v_pk_add_f32 v[168:169], v[168:169], s[68:69]\n"
    "v_pk_add_f32 v[170:171], v[170:171], s[68:69]\n"
    "v_pk_add_f32 v[172:173], v[172:173], s[68:69]\n"
    "v_pk_add_f32 v[174:175], v[174:175], s[68:69]\n"
    "v_cvt_pk_bf16_f32 v168, v168, v169\n"
    "v_cvt_pk_bf16_f32 v169, v170, v171\n"
    "v_cvt_pk_bf16_f32 v170, v172, v173\n"
    "v_cvt_pk_bf16_f32 v171, v174, v175\n"
    "s_nop 1\n"
    "v_mfma_f32_16x16x32_bf16 v[184:187], v[196:199], v[168:171], 0\n"
    "v_and_b32 v195, 7, v192\n"
    "v_lshlrev_b32 v195, 7, v195\n"
    "buffer_load_dword v194, v195, s[28:31], s86 offen sc1\n"
    "v_mfma_f32_16x16x32_bf16 v[104:107], v[218:221], v[96:99], 0\n"
    "v_add_f32 v112, s68, v112\n"
    "v_add_f32 v136, s68, v136\n"
    "v_mfma_f32_16x16x32_bf16 v[128:131], v[218:221], v[120:123], 0\n"
    "v_add_f32 v160, s68, v160\n"
    "v_add_f32 v184, s68, v184\n"
    "v_mfma_f32_16x16x32_bf16 v[152:155], v[218:221], v[144:147], 0\n"
    "v_rcp_f32 v112, v112\n"
    "v_mfma_f32_16x16x32_bf16 v[176:179], v[218:221], v[168:171], 0\n"
    "v_rcp_f32 v136, v136\n"
    "v_mfma_f32_16x16x32_bf16 v[108:111], v[222:225], v[96:99], 0\n"
    "v_rcp_f32 v160, v160\n"
    "v_mfma_f32_16x16x32_bf16 v[132:135], v[222:225], v[120:123], 0\n"
    "v_rcp_f32 v184, v184\n"
    "v_mfma_f32_16x16x32_bf16 v[156:159], v[222:225], v[144:147], 0\n"
    "v_mul_f32 v112, v112, v234\n"
    "v_mul_f32 v136, v136, v236\n"
    "v_mfma_f32_16x16x32_bf16 v[180:183], v[222:225], v[168:171], 0\n"
    "v_mul_f32 v160, v160, v238\n"
    "v_mul_f32 v184, v184, v240\n"
    "v_pk_mul_f32 v[104:105], v[104:105], v[112:113] op_sel_hi:[1,0]\n"
    "v_pk_mul_f32 v[106:107], v[106:107], v[112:113] op_sel_hi:[1,0]\n"
    "v_pk_mul_f32 v[108:109], v[108:109], v[112:113] op_sel_hi:[1,0]\n"
    "v_pk_mul_f32 v[110:111], v[110:111], v[112:113] op_sel_hi:[1,0]\n"
    "v_cvt_pk_bf16_f32 v116, v104, v105\n"
    "v_cvt_pk_bf16_f32 v117, v106, v107\n"
    "v_cvt_pk_bf16_f32 v118, v108, v109\n"
    "v_cvt_pk_bf16_f32 v119, v110, v111\n"
    "v_add3_u32 v205, v116, v117, 0\n"
    "v_add3_u32 v205, v118, v119, v205\n"
    "v_pk_mul_f32 v[128:129], v[128:129], v[136:137] op_sel_hi:[1,0]\n"
    "v_pk_mul_f32 v[130:131], v[130:131], v[136:137] op_sel_hi:[1,0]\n"
    "v_pk_mul_f32 v[132:133], v[132:133], v[136:137] op_sel_hi:[1,0]\n"
    "v_pk_mul_f32 v[134:135], v[134:135], v[136:137] op_sel_hi:[1,0]\n"
    "v_cvt_pk_bf16_f32 v140, v128, v129\n"
    "v_cvt_pk_bf16_f32 v141, v130, v131\n"
    "v_cvt_pk_bf16_f32 v142, v132, v133\n"
    "v_cvt_pk_bf16_f32 v143, v134, v135\n"
    "v_add3_u32 v205, v140, v141, v205\n"
    "v_add3_u32 v205, v142, v143, v205\n"
    "v_pk_mul_f32 v[152:153], v[152:153], v[160:161] op_sel_hi:[1,0]\n"
    "v_pk_mul_f32 v[154:155], v[154:155], v[160:161] op_sel_hi:[1,0]\n"
    "v_pk_mul_f32 v[156:157], v[156:157], v[160:161] op_sel_hi:[1,0]\n"
    "v_pk_mul_f32 v[158:159], v[158:159], v[160:161] op_sel_hi:[1,0]\n"
    "v_cvt_pk_bf16_f32 v164, v152, v153\n"
    "v_cvt_pk_bf16_f32 v165, v154, v155\n"
    "v_cvt_pk_bf16_f32 v166, v156, v157\n"
    "v_cvt_pk_bf16_f32 v167, v158, v159\n"
    "v_add3_u32 v205, v164, v165, v205\n"
    "v_add3_u32 v205, v166, v167, v205\n"
    "v_pk_mul_f32 v[176:177], v[176:177], v[184:185] op_sel_hi:[1,0]\n"
    "v_pk_mul_f32 v[178:179], v[178:179], v[184:185] op_sel_hi:[1,0]\n"
    "v_pk_mul_f32 v[180:181], v[180:181], v[184:185] op_sel_hi:[1,0]\n"
    "v_pk_mul_f32 v[182:183], v[182:183], v[184:185] op_sel_hi:[1,0]\n"
    "v_cvt_pk_bf16_f32 v188, v176, v177\n"
    "v_cvt_pk_bf16_f32 v189, v178, v179\n"
    "v_cvt_pk_bf16_f32 v190, v180, v181\n"
    "v_cvt_pk_bf16_f32 v191, v182, v183\n"
    "v_add3_u32 v205, v188, v189, v205\n"
    "v_add3_u32 v205, v190, v191, v205\n"
    "v_subrev_u32 v212, 65536, v212\n"
    "v_subrev_u32 v213, 65536, v213\n"
    "v_subrev_u32 v214, 65536, v214\n"
    "v_subrev_u32 v215, 65536, v215\n"
    "v_add_u32_dpp v204, v204, v204 row_shr:1 row_mask:0xf bank_mask:0xf\n"
    "v_add_u32_dpp v205, v205, v205 row_shr:1 row_mask:0xf bank_mask:0xf\n"
    "ds_read_b64_tr_b16 a[0:1], v212 offset:0\n"
    "v_add_u32_dpp v204, v204, v204 row_shr:2 row_mask:0xf bank_mask:0xf\n"
    "v_add_u32_dpp v205, v205, v205 row_shr:2 row_mask:0xf bank_mask:0xf\n"
    "ds_read_b64_tr_b16 a[2:3], v212 offset:2048\n"
    "v_add_u32_dpp v204, v204, v204 row_shr:4 row_mask:0xf bank_mask:0xf\n"
    "v_add_u32_dpp v205, v205, v205 row_shr:4 row_mask:0xf bank_mask:0xf\n"
    "ds_read_b64_tr_b16 a[4:5], v213 offset:0\n"
    "v_add_u32_dpp v204, v204, v204 row_shr:8 row_mask:0xf bank_mask:0xf\n"
    "v_add_u32_dpp v205, v205, v205 row_shr:8 row_mask:0xf bank_mask:0xf\n"
    "ds_read_b64_tr_b16 a[6:7], v213 offset:2048\n"
    "v_add_u32_dpp v204, v204, v204 row_bcast:15 row_mask:0xa bank_mask:0xf\n"
    "v_add_u32_dpp v205, v205, v205 row_bcast:15 row_mask:0xa bank_mask:0xf\n"
    "ds_read_b64_tr_b16 a[8:9], v214 offset:0\n"
    "v_add_u32_dpp v204, v204, v204 row_bcast:31 row_mask:0xc bank_mask:0xf\n"
    "v_add_u32_dpp v205, v205, v205 row_bcast:31 row_mask:0xc bank_mask:0xf\n"
    "ds_read_b64_tr_b16 a[10:11], v214 offset:2048\n"
    "v_add_u32 v204, v204, v250\n"
    "v_sub_u32 v204, 0, v204\n"
    "v_add_u32 v205, v205, v250\n"
    "v_sub_u32 v205, 0, v205\n"
    "v_mov_b32 v206, v250\n"
    "s_waitcnt vmcnt(0)\n"
    "v_cmp_ne_u32 vcc, v194, v250\n"
    "s_mov_b64 s[44:45], s[28:29]\n"
    "s_mov_b32 s47, s31\n"
    "s_cmp_eq_u64 vcc, 0\n"
    "s_cselect_b32 s46, s30, 0\n"
    "buffer_load_dwordx4 v[0:3], v216, s[44:47], s4 offen offset:0 sc1\n"
    "buffer_load_dwordx4 v[4:7], v216, s[44:47], s5 offen offset:0 sc1\n"
    "buffer_load_dwordx4 v[8:11], v216, s[44:47], s6 offen offset:0 sc1\n"
    "buffer_load_dwordx4 v[12:15], v216, s[44:47], s7 offen offset:0 sc1\n"
    "buffer_load_dwordx4 v[16:19], v216, s[44:47], s8 offen offset:0 sc1\n"
    "buffer_load_dwordx4 v[20:23], v216, s[44:47], s9 offen offset:0 sc1\n"
    "buffer_load_dwordx4 v[24:27], v216, s[44:47], s10 offen offset:0 sc1\n"
    "buffer_load_dwordx4 v[28:31], v216, s[44:47], s11 offen offset:0 sc1\n"
    "ds_read_b64_tr_b16 a[12:13], v215 offset:0\n"
    "ds_read_b64_tr_b16 a[14:15], v215 offset:2048\n"
    "ds_read_b64_tr_b16 a[48:49], v212 offset:16384\n"
    "ds_read_b64_tr_b16 a[50:51], v212 offset:18432\n"
    "ds_read_b64_tr_b16 a[52:53], v213 offset:16384\n"
    "ds_read_b64_tr_b16 a[54:55], v213 offset:18432\n"
    "ds_read_b64_tr_b16 a[56:57], v214 offset:16384\n"
    "ds_read_b64_tr_b16 a[58:59], v214 offset:18432\n"
    "ds_read_b64_tr_b16 a[60:61], v215 offset:16384\n"
    "ds_read_b64_tr_b16 a[62:63], v215 offset:18432\n"
    "v_add_u32 v212, 65536, v212\n"
    "v_add_u32 v213, 65536, v213\n"
    "v_add_u32 v214, 65536, v214\n"
    "v_add_u32 v215, 65536, v215\n"
    "v_cmp_gt_u32 s[24:25], 8, v192\n"
    "s_mov_b64 s[20:21], s[28:29]\n"
    "s_mov_b64 s[22:23], s[30:31]\n"
    "s_mov_b32 s92, 0\n"
    "s_waitcnt lgkmcnt(0)\n"
    "s_cmp_eq_u32 s46, 0\n"
    "s_cbranch_scc1 Lh%=\n"
    "Lhm%=:\n"
    "s_barrier\n"
    "buffer_store_dwordx4 v[116:119], v216, s[20:23], s88 offen offset:0 sc1\n"
    "buffer_store_dwordx4 v[140:143], v216, s[20:23], s88 offen offset:1024 sc1\n"
    "buffer_store_dwordx4 v[164:167], v216, s[20:23], s88 offen offset:2048 sc1\n"
    "buffer_store_dwordx4 v[188:191], v216, s[20:23], s88 offen offset:3072 sc1\n"
    "buffer_store_dwordx3 v[204:206], v251, s[20:23], s97 offen sc1\n"
    "s_mov_b32 s22, 0\n"
    "v_cndmask_b32 v218, 0, v250, s[24:25]\n"
    "v_mov_b32 v219, v218\n"
    "s_waitcnt vmcnt(11)\n"
    "v_mfma_f32_16x16x32_bf16 v[80:83], a[128:131], v[0:3], 0\n"
    "ds_read_b64_tr_b16 a[64:65], v212 offset:32768\n"
    "v_mfma_f32_16x16x32_bf16 v[80:83], a[160:163], v[4:7], v[80:83]\n"
    "ds_read_b64_tr_b16 a[66:67], v212 offset:34816\n"
    "s_waitcnt vmcnt(9)\n"
    "v_mfma_f32_16x16x32_bf16 v[80:83], a[192:195], v[8:11], v[80:83]\n"
    "ds_read_b64_tr_b16 a[68:69], v213 offset:32768\n"
    "v_mfma_f32_16x16x32_bf16 v[80:83], a[224:227], v[12:15], v[80:83]\n"
    "ds_read_b64_tr_b16 a[70:71], v213 offset:34816\n"
    "v_add3_u32 v218, v0, v1, v218\n"
    "v_mfma_f32_16x16x32_bf16 v[84:87], a[132:135], v[0:3], 0\n"
    "ds_read_b64_tr_b16 a[72:73], v214 offset:32768\n"
    "v_add3_u32 v218, v2, v3, v218\n"
    "v_mfma_f32_16x16x32_bf16 v[84:87], a[164:167], v[4:7], v[84:87]\n"
    "ds_read_b64_tr_b16 a[74:75], v214 offset:34816\n"
    "v_add3_u32 v218, v4, v5, v218\n"
    "v_mfma_f32_16x16x32_bf16 v[84:87], a[196:199], v[8:11], v[84:87]\n"
    "ds_read_b64_tr_b16 a[76:77], v215 offset:32768\n"
    "v_add3_u32 v218, v6, v7, v218\n"
    "v_mfma_f32_16x16x32_bf16 v[84:87], a[228:231], v[12:15], v[84:87]\n"
    "ds_read_b64_tr_b16 a[78:79], v215 offset:34816\n"
    "v_add3_u32 v218, v8, v9, v218\n"
    "v_mfma_f32_16x16x32_bf16 v[88:91], a[136:139], v[0:3], 0\n"
    "ds_read_b64_tr_b16 a[96:97], v212 offset:16384\n"
    "v_add3_u32 v218, v10, v11, v218\n"
    "v_mfma_f32_16x16x32_bf16 v[88:91], a[168:171], v[4:7], v[88:91]\n"
    "ds_read_b64_tr_b16 a[98:99], v212 offset:18432\n"
    "v_add3_u32 v218, v12, v13, v218\n"
    "v_mfma_f32_16x16x32_bf16 v[88:91], a[200:203], v[8:11], v[88:91]\n"
    "ds_read_b64_tr_b16 a[100:101], v213 offset:16384\n"
    "v_add3_u32 v218, v14, v15, v218\n"
    "v_mfma_f32_16x16x32_bf16 v[88:91], a[232:235], v[12:15], v[88:91]\n"
    "ds_read_b64_tr_b16 a[102:103], v213 offset:18432\n"
    "v_mfma_f32_16x16x32_bf16 v[92:95], a[140:143], v[0:3], 0\n"
    "ds_read_b64_tr_b16 a[104:105], v214 offset:16384\n"
    "v_mfma_f32_16x16x32_bf16 v[92:95], a[172:175], v[4:7], v[92:95]\n"
    "ds_read_b64_tr_b16 a[106:107], v214 offset:18432\n"
    "v_mfma_f32_16x16x32_bf16 v[92:95], a[204:207], v[8:11], v[92:95]\n"
    "ds_read_b64_tr_b16 a[108:109], v215 offset:16384\n"
    "v_mfma_f32_16x16x32_bf16 v[92:95], a[236:239], v[12:15], v[92:95]\n"
    "ds_read_b64_tr_b16 a[110:111], v215 offset:18432\n"
    "v_mfma_f32_16x16x32_bf16 v[96:99], a[144:147], v[0:3], 0\n"
    "ds_read_b64_tr_b16 a[80:81], v212 offset:0\n"
    "v_mfma_f32_16x16x32_bf16 v[96:99], a[176:179], v[4:7], v[96:99]\n"
    "ds_read_b64_tr_b16 a[82:83], v212 offset:2048\n"
    "v_mfma_f32_16x16x32_bf16 v[96:99], a[208:211], v[8:11], v[96:99]\n"
    "ds_read_b64_tr_b16 a[84:85], v213 offset:0\n"
    "v_mfma_f32_16x16x32_bf16 v[96:99], a[240:243], v[12:15], v[96:99]\n"
    "ds_read_b64_tr_b16 a[86:87], v213 offset:2048\n"
    "v_mfma_f32_16x16x32_bf16 v[100:103], a[148:151], v[0:3], 0\n"
    "ds_read_b64_tr_b16 a[88:89], v214 offset:0\n"
    "v_mfma_f32_16x16x32_bf16 v[100:103], a[180:183], v[4:7], v[100:103]\n"
    "ds_read_b64_tr_b16 a[90:91], v214 offset:2048\n"
    "v_mfma_f32_16x16x32_bf16 v[100:103], a[212:215], v[8:11], v[100:103]\n"
    "ds_read_b64_tr_b16 a[92:93], v215 offset:0\n"
    "v_mfma_f32_16x16x32_bf16 v[100:103], a[244:247], v[12:15], v[100:103]\n"
    "ds_read_b64_tr_b16 a[94:95], v215 offset:2048\n"
    "v_mfma_f32_16x16x32_bf16 v[104:107], a[152:155], v[0:3], 0\n"
    "buffer_load_dwordx4 v[32:35], v216, s[28:31], s4 offen offset:1024 sc1\n"
    "v_mfma_f32_16x16x32_bf16 v[104:107], a[184:187], v[4:7], v[104:107]\n"
    "buffer_load_dwordx4 v[36:39], v216, s[28:31], s5 offen offset:1024 sc1\n"
    "v_mfma_f32_16x16x32_bf16 v[104:107], a[216:219], v[8:11], v[104:107]\n"
    "buffer_load_dwordx4 v[40:43], v216, s[28:31], s6 offen offset:1024 sc1\n"
    "v_mfma_f32_16x16x32_bf16 v[104:107], a[248:251], v[12:15], v[104:107]\n"
    "buffer_load_dwordx4 v[44:47], v216, s[28:31], s7 offen offset:1024 sc1\n"
    "v_mfma_f32_16x16x32_bf16 v[108:111], a[156:159], v[0:3], 0\n"
    "v_mfma_f32_16x16x32_bf16 v[108:111], a[188:191], v[4:7], v[108:111]\n"
    "v_mfma_f32_16x16x32_bf16 v[108:111], a[220:223], v[8:11], v[108:111]\n"
    "v_mfma_f32_16x16x32_bf16 v[108:111], a[252:255], v[12:15], v[108:111]\n"
    "s_waitcnt vmcnt(11)\n"
    "v_mfma_f32_16x16x32_bf16 v[80:83], a[0:3], v[16:19], v[80:83]\n"
    "v_mfma_f32_16x16x32_bf16 v[80:83], a[16:19], v[20:23], v[80:83]\n"
    "s_xor_b32 s76, s48, 3\n"
    "s_waitcnt vmcnt(9)\n"
    "s_waitcnt lgkmcnt(14)\n"
    "v_mfma_f32_16x16x32_bf16 v[80:83], a[64:67], v[24:27], v[80:83]\n"
    "s_lshl_b32 s76, s76, 13\n"
    "v_mfma_f32_16x16x32_bf16 v[80:83], a[96:99], v[28:31], v[80:83]\n"
    "s_add_u32 s76, s76, 0\n"
    "v_add3_u32 v218, v16, v17, v218\n"
    "v_mfma_f32_16x16x32_bf16 v[84:87], a[4:7], v[16:19], v[84:87]\n"
    "v_add_u32 v226, s76, v216\n"
    "v_add3_u32 v218, v18, v19, v218\n"
    "v_mfma_f32_16x16x32_bf16 v[84:87], a[20:23], v[20:23], v[84:87]\n"
    "s_xor_b32 s76, s48, 2\n"
    "v_add3_u32 v218, v20, v21, v218\n"
    "v_mfma_f32_16x16x32_bf16 v[84:87], a[68:71], v[24:27], v[84:87]\n"
    "s_lshl_b32 s76, s76, 13\n"
    "v_add3_u32 v218, v22, v23, v218\n"
    "s_waitcnt lgkmcnt(12)\n"
    "v_mfma_f32_16x16x32_bf16 v[84:87], a[100:103], v[28:31], v[84:87]\n"
    "s_add_u32 s76, s76, 32768\n"
    "v_add3_u32 v218, v24, v25, v218\n"
    "v_mfma_f32_16x16x32_bf16 v[88:91], a[8:11], v[16:19], v[88:91]\n"
    "v_add_u32 v227, s76, v216\n"
    "v_add3_u32 v218, v26, v27, v218\n"
    "v_mfma_f32_16x16x32_bf16 v[88:91], a[24:27], v[20:23], v[88:91]\n"
    "s_xor_b32 s76, s48, 1\n"
    "v_add3_u32 v218, v28, v29, v218\n"
    "v_mfma_f32_16x16x32_bf16 v[88:91], a[72:75], v[24:27], v[88:91]\n"
    "s_lshl_b32 s76, s76, 13\n"
    "v_add3_u32 v218, v30, v31, v218\n"
    "s_waitcnt lgkmcnt(10)\n"
    "v_mfma_f32_16x16x32_bf16 v[88:91], a[104:107], v[28:31], v[88:91]\n"
    "s_add_u32 s76, s76, 65536\n"
    "v_mfma_f32_16x16x32_bf16 v[92:95], a[12:15], v[16:19], v[92:95]\n"
    "v_add_u32 v228, s76, v216\n"
    "v_mfma_f32_16x16x32_bf16 v[92:95], a[28:31], v[20:23], v[92:95]\n"
    "v_add_u32 v230, s59, v216\n"
    "v_mfma_f32_16x16x32_bf16 v[92:95], a[76:79], v[24:27], v[92:95]\n"
    "v_add_u32 v231, 0x10000, v230\n"
    "s_waitcnt lgkmcnt(8)\n"
    "v_mfma_f32_16x16x32_bf16 v[92:95], a[108:111], v[28:31], v[92:95]\n"
    "v_add_u32 v254, 4096, v216\n"
    "v_mfma_f32_16x16x32_bf16 v[96:99], a[48:51], v[16:19], v[96:99]\n"
    "v_lshrrev_b32 v193, 4, v192\n"
    "v_mfma_f32_16x16x32_bf16 v[96:99], a[32:35], v[20:23], v[96:99]\n"
    "v_lshlrev_b32 v229, 4, v193\n"
    "s_waitcnt lgkmcnt(6)\n"
    "v_mfma_f32_16x16x32_bf16 v[96:99], a[80:83], v[24:27], v[96:99]\n"
    "v_lshl_add_u32 v229, v253, 9, v229\n"
    "v_mfma_f32_16x16x32_bf16 v[96:99], a[40:43], v[28:31], v[96:99]\n"
    "v_lshrrev_b32 v224, 3, v195\n"
    "v_mfma_f32_16x16x32_bf16 v[100:103], a[52:55], v[16:19], v[100:103]\n"
    "v_lshlrev_b32 v225, 2, v192\n"
    "v_mfma_f32_16x16x32_bf16 v[100:103], a[36:39], v[20:23], v[100:103]\n"
    "s_lshl_b32 s76, s48, 2\n"
    "s_waitcnt lgkmcnt(4)\n"
    "v_mfma_f32_16x16x32_bf16 v[100:103], a[84:87], v[24:27], v[100:103]\n"
    "s_add_u32 s76, s76, 147456\n"
    "v_mfma_f32_16x16x32_bf16 v[100:103], a[44:47], v[28:31], v[100:103]\n"
    "v_mov_b32 v222, s76\n"
    "v_mfma_f32_16x16x32_bf16 v[104:107], a[56:59], v[16:19], v[104:107]\n"
    "v_mov_b32 v223, 147456\n"
    "v_mfma_f32_16x16x32_bf16 v[104:107], a[112:115], v[20:23], v[104:107]\n"
    "s_lshl_b32 s79, s91, 7\n"
    "s_waitcnt lgkmcnt(2)\n"
    "v_mfma_f32_16x16x32_bf16 v[104:107], a[88:91], v[24:27], v[104:107]\n"
    "s_add_u32 s79, s79, 4096\n"
    "v_mfma_f32_16x16x32_bf16 v[104:107], a[120:123], v[28:31], v[104:107]\n"
    "buffer_load_dwordx4 v[48:51], v216, s[28:31], s8 offen offset:1024 sc1\n"
    "v_mfma_f32_16x16x32_bf16 v[108:111], a[60:63], v[16:19], v[108:111]\n"
    "buffer_load_dwordx4 v[52:55], v216, s[28:31], s9 offen offset:1024 sc1\n"
    "v_mfma_f32_16x16x32_bf16 v[108:111], a[116:119], v[20:23], v[108:111]\n"
    "buffer_load_dwordx4 v[56:59], v216, s[28:31], s10 offen offset:1024 sc1\n"
    "s_waitcnt lgkmcnt(0)\n"
    "v_mfma_f32_16x16x32_bf16 v[108:111], a[92:95], v[24:27], v[108:111]\n"
    "buffer_load_dwordx4 v[60:63], v216, s[28:31], s11 offen offset:1024 sc1\n"
    "v_mfma_f32_16x16x32_bf16 v[108:111], a[124:127], v[28:31], v[108:111]\n"
    "s_waitcnt vmcnt(6)\n"
    "v_mfma_f32_16x16x32_bf16 v[112:115], a[128:131], v[32:35], 0\n"
    "ds_write_b64 v226, v[80:81] offset:0\n"
    "v_mfma_f32_16x16x32_bf16 v[112:115], a[160:163], v[36:39], v[112:115]\n"
    "s_waitcnt vmcnt(4)\n"
    "v_mfma_f32_16x16x32_bf16 v[112:115], a[192:195], v[40:43], v[112:115]\n"
    "buffer_load_dwordx4 v[64:67], v216, s[28:31], s4 offen offset:2048 sc1\n"
    "v_mfma_f32_16x16x32_bf16 v[112:115], a[224:227], v[44:47], v[112:115]\n"
    "ds_write_b64 v226, v[82:83] offset:8\n"
    "v_mfma_f32_16x16x32_bf16 v[116:119], a[132:135], v[32:35], 0\n"
    "v_add3_u32 v218, v32, v33, v218\n"
    "v_add3_u32 v218, v34, v35, v218\n"
    "v_mfma_f32_16x16x32_bf16 v[116:119], a[164:167], v[36:39], v[116:119]\n"
    "v_add3_u32 v218, v36, v37, v218\n"
    "v_add3_u32 v218, v38, v39, v218\n"
    "v_mfma_f32_16x16x32_bf16 v[116:119], a[196:199], v[40:43], v[116:119]\n"
    "v_add3_u32 v218, v40, v41, v218\n"
    "v_add3_u32 v218, v42, v43, v218\n"
    "v_mfma_f32_16x16x32_bf16 v[116:119], a[228:231], v[44:47], v[116:119]\n"
    "ds_write_b64 v226, v[84:85] offset:1024\n"
    "v_mfma_f32_16x16x32_bf16 v[120:123], a[136:139], v[32:35], 0\n"
    "v_add3_u32 v218, v44, v45, v218\n"
    "v_add3_u32 v218, v46, v47, v218\n"
    "v_mfma_f32_16x16x32_bf16 v[120:123], a[168:171], v[36:39], v[120:123]\n"
    "buffer_load_dwordx4 v[68:71], v216, s[28:31], s5 offen offset:2048 sc1\n"
    "v_mfma_f32_16x16x32_bf16 v[120:123], a[200:203], v[40:43], v[120:123]\n"
    "v_mfma_f32_16x16x32_bf16 v[120:123], a[232:235], v[44:47], v[120:123]\n"
    "ds_write_b64 v226, v[86:87] offset:1032\n"
    "v_mfma_f32_16x16x32_bf16 v[124:127], a[140:143], v[32:35], 0\n"
    "v_mfma_f32_16x16x32_bf16 v[124:127], a[172:175], v[36:39], v[124:127]\n"
    "v_mfma_f32_16x16x32_bf16 v[124:127], a[204:207], v[40:43], v[124:127]\n"
    "v_mfma_f32_16x16x32_bf16 v[124:127], a[236:239], v[44:47], v[124:127]\n"
    "ds_write_b64 v226, v[88:89] offset:2048\n"
    "v_mfma_f32_16x16x32_bf16 v[128:131], a[144:147], v[32:35], 0\n"
    "buffer_load_dwordx4 v[72:75], v216, s[28:31], s6 offen offset:2048 sc1\n"
    "v_mfma_f32_16x16x32_bf16 v[128:131], a[176:179], v[36:39], v[128:131]\n"
    "v_mfma_f32_16x16x32_bf16 v[128:131], a[208:211], v[40:43], v[128:131]\n"
    "ds_write_b64 v226, v[90:91] offset:2056\n"
    "v_mfma_f32_16x16x32_bf16 v[128:131], a[240:243], v[44:47], v[128:131]\n"
    "v_mfma_f32_16x16x32_bf16 v[132:135], a[148:151], v[32:35], 0\n"
    "v_mfma_f32_16x16x32_bf16 v[132:135], a[180:183], v[36:39], v[132:135]\n"
    "v_mfma_f32_16x16x32_bf16 v[132:135], a[212:215], v[40:43], v[132:135]\n"
    "ds_write_b64 v226, v[92:93] offset:3072\n"
    "v_mfma_f32_16x16x32_bf16 v[132:135], a[244:247], v[44:47], v[132:135]\n"
    "buffer_load_dwordx4 v[76:79], v216, s[28:31], s7 offen offset:2048 sc1\n"
    "v_mfma_f32_16x16x32_bf16 v[136:139], a[152:155], v[32:35], 0\n"
    "v_mfma_f32_16x16x32_bf16 v[136:139], a[184:187], v[36:39], v[136:139]\n"
    "v_mfma_f32_16x16x32_bf16 v[136:139], a[216:219], v[40:43], v[136:139]\n"
    "ds_write_b64 v226, v[94:95] offset:3080\n"
    "v_mfma_f32_16x16x32_bf16 v[136:139], a[248:251], v[44:47], v[136:139]\n"
    "v_mfma_f32_16x16x32_bf16 v[140:143], a[156:159], v[32:35], 0\n"
    "v_mfma_f32_16x16x32_bf16 v[140:143], a[188:191], v[36:39], v[140:143]\n"
    "v_mfma_f32_16x16x32_bf16 v[140:143], a[220:223], v[40:43], v[140:143]\n"
    "ds_write_b64 v226, v[96:97] offset:4096\n"
    "v_mfma_f32_16x16x32_bf16 v[140:143], a[252:255], v[44:47], v[140:143]\n"
    "s_waitcnt vmcnt(6)\n"
    "v_mfma_f32_16x16x32_bf16 v[112:115], a[0:3], v[48:51], v[112:115]\n"
    "buffer_load_dwordx4 v[0:3], v216, s[28:31], s8 offen offset:2048 sc1\n"
    "v_mfma_f32_16x16x32_bf16 v[112:115], a[16:19], v[52:55], v[112:115]\n"
    "s_waitcnt vmcnt(5)\n"
    "v_mfma_f32_16x16x32_bf16 v[112:115], a[64:67], v[56:59], v[112:115]\n"
    "ds_write_b64 v226, v[98:99] offset:4104\n"
    "v_mfma_f32_16x16x32_bf16 v[112:115], a[96:99], v[60:63], v[112:115]\n"
    "v_add3_u32 v218, v48, v49, v218\n"
    "v_add3_u32 v218, v50, v51, v218\n"
    "v_mfma_f32_16x16x32_bf16 v[116:119], a[4:7], v[48:51], v[116:119]\n"
    "v_add3_u32 v218, v52, v53, v218\n"
    "v_add3_u32 v218, v54, v55, v218\n"
    "v_mfma_f32_16x16x32_bf16 v[116:119], a[20:23], v[52:55], v[116:119]\n"
    "ds_write_b64 v226, v[100:101] offset:5120\n"
    "v_mfma_f32_16x16x32_bf16 v[116:119], a[68:71], v[56:59], v[116:119]\n"
    "buffer_load_dwordx4 v[4:7], v216, s[28:31], s9 offen offset:2048 sc1\n"
    "v_mfma_f32_16x16x32_bf16 v[116:119], a[100:103], v[60:63], v[116:119]\n"
    "v_add3_u32 v218, v56, v57, v218\n"
    "v_add3_u32 v218, v58, v59, v218\n"
    "v_mfma_f32_16x16x32_bf16 v[120:123], a[8:11], v[48:51], v[120:123]\n"
    "v_add3_u32 v218, v60, v61, v218\n"
    "v_add3_u32 v218, v62, v63, v218\n"
    "v_mfma_f32_16x16x32_bf16 v[120:123], a[24:27], v[52:55], v[120:123]\n"
    "ds_write_b64 v226, v[102:103] offset:5128\n"
    "v_mfma_f32_16x16x32_bf16 v[120:123], a[72:75], v[56:59], v[120:123]\n"
    "v_mfma_f32_16x16x32_bf16 v[120:123], a[104:107], v[60:63], v[120:123]\n"
    "v_mfma_f32_16x16x32_bf16 v[124:127], a[12:15], v[48:51], v[124:127]\n"
    "buffer_load_dwordx4 v[8:11], v216, s[28:31], s10 offen offset:2048 sc1\n"
    "v_mfma_f32_16x16x32_bf16 v[124:127], a[28:31], v[52:55], v[124:127]\n"
    "ds_write_b64 v226, v[104:105] offset:6144\n"
    "v_mfma_f32_16x16x32_bf16 v[124:127], a[76:79], v[56:59], v[124:127]\n"
    "v_mfma_f32_16x16x32_bf16 v[124:127], a[108:111], v[60:63], v[124:127]\n"
    "v_mfma_f32_16x16x32_bf16 v[128:131], a[48:51], v[48:51], v[128:131]\n"
    "ds_write_b64 v226, v[106:107] offset:6152\n"
    "v_mfma_f32_16x16x32_bf16 v[128:131], a[32:35], v[52:55], v[128:131]\n"
    "v_mfma_f32_16x16x32_bf16 v[128:131], a[80:83], v[56:59], v[128:131]\n"
    "buffer_load_dwordx4 v[12:15], v216, s[28:31], s11 offen offset:2048 sc1\n"
    "v_mfma_f32_16x16x32_bf16 v[128:131], a[40:43], v[60:63], v[128:131]\n"
    "v_mfma_f32_16x16x32_bf16 v[132:135], a[52:55], v[48:51], v[132:135]\n"
    "ds_write_b64 v226, v[108:109] offset:7168\n"
    "v_mfma_f32_16x16x32_bf16 v[132:135], a[36:39], v[52:55], v[132:135]\n"
    "v_mfma_f32_16x16x32_bf16 v[132:135], a[84:87], v[56:59], v[132:135]\n"
    "v_mfma_f32_16x16x32_bf16 v[132:135], a[44:47], v[60:63], v[132:135]\n"
    "v_mfma_f32_16x16x32_bf16 v[136:139], a[56:59], v[48:51], v[136:139]\n"
    "buffer_load_dwordx3 v[238:240], v224, s[28:31], s79 offen sc1\n"
    "v_mfma_f32_16x16x32_bf16 v[136:139], a[112:115], v[52:55], v[136:139]\n"
    "buffer_load_dwordx4 v[16:19], v216, s[28:31], s4 offen offset:3072 sc1\n"
    "v_mfma_f32_16x16x32_bf16 v[136:139], a[88:91], v[56:59], v[136:139]\n"
    "ds_write_b64 v226, v[110:111] offset:7176\n"
    "v_mfma_f32_16x16x32_bf16 v[136:139], a[120:123], v[60:63], v[136:139]\n"
    "ds_write_b64 v227, v[112:113] offset:0\n"
    "v_mfma_f32_16x16x32_bf16 v[140:143], a[60:63], v[48:51], v[140:143]\n"
    "ds_write_b64 v227, v[114:115] offset:8\n"
    "v_mfma_f32_16x16x32_bf16 v[140:143], a[116:119], v[52:55], v[140:143]\n"
    "ds_write_b64 v227, v[116:117] offset:1024\n"
    "v_mfma_f32_16x16x32_bf16 v[140:143], a[92:95], v[56:59], v[140:143]\n"
    "buffer_load_dwordx4 v[20:23], v216, s[28:31], s5 offen offset:3072 sc1\n"
    "v_mfma_f32_16x16x32_bf16 v[140:143], a[124:127], v[60:63], v[140:143]\n"
    "s_waitcnt vmcnt(9)\n"
    "v_mfma_f32_16x16x32_bf16 v[80:83], a[128:131], v[64:67], 0\n"
    "ds_write_b64 v227, v[118:119] offset:1032\n"
    "v_mfma_f32_16x16x32_bf16 v[80:83], a[160:163], v[68:71], v[80:83]\n"
    "s_waitcnt vmcnt(7)\n"
    "v_mfma_f32_16x16x32_bf16 v[80:83], a[192:195], v[72:75], v[80:83]\n"
    "ds_write_b64 v227, v[120:121] offset:2048\n"
    "v_mfma_f32_16x16x32_bf16 v[80:83], a[224:227], v[76:79], v[80:83]\n"
    "ds_write_b64 v227, v[122:123] offset:2056\n"
    "v_mfma_f32_16x16x32_bf16 v[84:87], a[132:135], v[64:67], 0\n"
    "buffer_load_dwordx4 v[24:27], v216, s[28:31], s6 offen offset:3072 sc1\n"
    "v_mfma_f32_16x16x32_bf16 v[84:87], a[164:167], v[68:71], v[84:87]\n"
    "ds_write_b64 v227, v[124:125] offset:3072\n"
    "v_mfma_f32_16x16x32_bf16 v[84:87], a[196:199], v[72:75], v[84:87]\n"
    "v_add3_u32 v218, v64, v65, v218\n"
    "v_add3_u32 v218, v66, v67, v218\n"
    "v_mfma_f32_16x16x32_bf16 v[84:87], a[228:231], v[76:79], v[84:87]\n"
    "ds_write_b64 v227, v[126:127] offset:3080\n"
    "v_mfma_f32_16x16x32_bf16 v[88:91], a[136:139], v[64:67], 0\n"
    "v_add3_u32 v218, v68, v69, v218\n"
    "v_add3_u32 v218, v70, v71, v218\n"
    "v_mfma_f32_16x16x32_bf16 v[88:91], a[168:171], v[68:71], v[88:91]\n"
    "v_add3_u32 v218, v72, v73, v218\n"
    "v_add3_u32 v218, v74, v75, v218\n"
    "v_mfma_f32_16x16x32_bf16 v[88:91], a[200:203], v[72:75], v[88:91]\n"
    "buffer_load_dwordx4 v[28:31], v216, s[28:31], s7 offen offset:3072 sc1\n"
    "v_mfma_f32_16x16x32_bf16 v[88:91], a[232:235], v[76:79], v[88:91]\n"
    "ds_write_b64 v227, v[128:129] offset:4096\n"
    "v_mfma_f32_16x16x32_bf16 v[92:95], a[140:143], v[64:67], 0\n"
    "v_add3_u32 v218, v76, v77, v218\n"
    "v_add3_u32 v218, v78, v79, v218\n"
    "v_mfma_f32_16x16x32_bf16 v[92:95], a[172:175], v[68:71], v[92:95]\n"
    "v_mfma_f32_16x16x32_bf16 v[92:95], a[204:207], v[72:75], v[92:95]\n"
    "ds_write_b64 v227, v[130:131] offset:4104\n"
    "v_mfma_f32_16x16x32_bf16 v[92:95], a[236:239], v[76:79], v[92:95]\n"
    "v_mfma_f32_16x16x32_bf16 v[96:99], a[144:147], v[64:67], 0\n"
    "buffer_load_dwordx4 v[32:35], v216, s[28:31], s8 offen offset:3072 sc1\n"
    "v_mfma_f32_16x16x32_bf16 v[96:99], a[176:179], v[68:71], v[96:99]\n"
    "v_mfma_f32_16x16x32_bf16 v[96:99], a[208:211], v[72:75], v[96:99]\n"
    "ds_write_b64 v227, v[132:133] offset:5120\n"
    "v_mfma_f32_16x16x32_bf16 v[96:99], a[240:243], v[76:79], v[96:99]\n"
    "v_mfma_f32_16x16x32_bf16 v[100:103], a[148:151], v[64:67], 0\n"
    "v_mfma_f32_16x16x32_bf16 v[100:103], a[180:183], v[68:71], v[100:103]\n"
    "buffer_load_dwordx4 v[36:39], v216, s[28:31], s9 offen offset:3072 sc1\n"
    "v_mfma_f32_16x16x32_bf16 v[100:103], a[212:215], v[72:75], v[100:103]\n"
    "ds_write_b64 v227, v[134:135] offset:5128\n"
    "v_mfma_f32_16x16x32_bf16 v[100:103], a[244:247], v[76:79], v[100:103]\n"
    "v_mfma_f32_16x16x32_bf16 v[104:107], a[152:155], v[64:67], 0\n"
    "v_mfma_f32_16x16x32_bf16 v[104:107], a[184:187], v[68:71], v[104:107]\n"
    "v_mfma_f32_16x16x32_bf16 v[104:107], a[216:219], v[72:75], v[104:107]\n"
    "buffer_load_dwordx4 v[40:43], v216, s[28:31], s10 offen offset:3072 sc1\n"
    "v_mfma_f32_16x16x32_bf16 v[104:107], a[248:251], v[76:79], v[104:107]\n"
    "ds_write_b64 v227, v[136:137] offset:6144\n"
    "v_mfma_f32_16x16x32_bf16 v[108:111], a[156:159], v[64:67], 0\n"
    "v_mfma_f32_16x16x32_bf16 v[108:111], a[188:191], v[68:71], v[108:111]\n"
    "v_mfma_f32_16x16x32_bf16 v[108:111], a[220:223], v[72:75], v[108:111]\n"
    "ds_write_b64 v227, v[138:139] offset:6152\n"
    "v_mfma_f32_16x16x32_bf16 v[108:111], a[252:255], v[76:79], v[108:111]\n"
    "s_waitcnt vmcnt(10)\n"
    "v_mfma_f32_16x16x32_bf16 v[80:83], a[0:3], v[0:3], v[80:83]\n"
    "buffer_load_dwordx4 v[44:47], v216, s[28:31], s11 offen offset:3072 sc1\n"
    "v_mfma_f32_16x16x32_bf16 v[80:83], a[16:19], v[4:7], v[80:83]\n"
    "s_waitcnt vmcnt(8)\n"
    "v_cmp_ne_u32 vcc, v240, v250\n"
    "v_mfma_f32_16x16x32_bf16 v[80:83], a[64:67], v[8:11], v[80:83]\n"
    "ds_write_b64 v227, v[140:141] offset:7168\n"
    "v_mfma_f32_16x16x32_bf16 v[80:83], a[96:99], v[12:15], v[80:83]\n"
    "v_add3_u32 v218, v0, v1, v218\n"
    "v_add3_u32 v218, v2, v3, v218\n"
    "v_mfma_f32_16x16x32_bf16 v[84:87], a[4:7], v[0:3], v[84:87]\n"
    "s_cbranch_vccnz Lcs%=\n"
    "Lcok%=:\n"
    "v_add3_u32 v218, v4, v5, v218\n"
    "v_mfma_f32_16x16x32_bf16 v[84:87], a[20:23], v[4:7], v[84:87]\n"
    "v_add3_u32 v218, v6, v7, v218\n"
    "v_add3_u32 v218, v8, v9, v218\n"
    "v_mfma_f32_16x16x32_bf16 v[84:87], a[68:71], v[8:11], v[84:87]\n"
    "buffer_load_dwordx4 v[48:51], v254, s[28:31], s4 offen offset:0 sc1\n"
    "v_mfma_f32_16x16x32_bf16 v[84:87], a[100:103], v[12:15], v[84:87]\n"
    "ds_write_b64 v227, v[142:143] offset:7176\n"
    "v_mfma_f32_16x16x32_bf16 v[88:91], a[8:11], v[0:3], v[88:91]\n"
    "v_add3_u32 v218, v10, v11, v218\n"
    "v_add3_u32 v218, v12, v13, v218\n"
    "v_mfma_f32_16x16x32_bf16 v[88:91], a[24:27], v[4:7], v[88:91]\n"
    "v_add3_u32 v218, v14, v15, v218\n"
    "v_mfma_f32_16x16x32_bf16 v[88:91], a[72:75], v[8:11], v[88:91]\n"
    "v_mfma_f32_16x16x32_bf16 v[88:91], a[104:107], v[12:15], v[88:91]\n"
    "buffer_load_dwordx4 v[52:55], v254, s[28:31], s5 offen offset:0 sc1\n"
    "v_mfma_f32_16x16x32_bf16 v[92:95], a[12:15], v[0:3], v[92:95]\n"
    "v_mfma_f32_16x16x32_bf16 v[92:95], a[28:31], v[4:7], v[92:95]\n"
    "v_mfma_f32_16x16x32_bf16 v[92:95], a[76:79], v[8:11], v[92:95]\n"
    "v_mfma_f32_16x16x32_bf16 v[92:95], a[108:111], v[12:15], v[92:95]\n"
    "s_waitcnt lgkmcnt(0)\n"
    "s_barrier\n"
    "v_mfma_f32_16x16x32_bf16 v[96:99], a[48:51], v[0:3], v[96:99]\n"
    "buffer_load_dwordx4 v[56:59], v254, s[28:31], s6 offen offset:0 sc1\n"
    "v_mfma_f32_16x16x32_bf16 v[96:99], a[32:35], v[4:7], v[96:99]\n"
    "ds_read_b128 v[112:115], v230 offset:0\n"
    "ds_read_b128 v[116:119], v230 offset:1024\n"
    "v_mfma_f32_16x16x32_bf16 v[96:99], a[80:83], v[8:11], v[96:99]\n"
    "ds_read_b128 v[120:123], v230 offset:2048\n"
    "v_mfma_f32_16x16x32_bf16 v[96:99], a[40:43], v[12:15], v[96:99]\n"
    "ds_read_b128 v[124:127], v230 offset:3072\n"
    "v_mfma_f32_16x16x32_bf16 v[100:103], a[52:55], v[0:3], v[100:103]\n"
    "ds_write_b128 v228, v[80:83] offset:0\n"
    "v_mfma_f32_16x16x32_bf16 v[100:103], a[36:39], v[4:7], v[100:103]\n"
    "buffer_load_dwordx4 v[60:63], v254, s[28:31], s7 offen offset:0 sc1\n"
    "v_mfma_f32_16x16x32_bf16 v[100:103], a[84:87], v[8:11], v[100:103]\n"
    "ds_read_b128 v[128:131], v230 offset:4096\n"
    "ds_read_b128 v[132:135], v230 offset:5120\n"
    "v_mfma_f32_16x16x32_bf16 v[100:103], a[44:47], v[12:15], v[100:103]\n"
    "ds_read_b128 v[136:139], v230 offset:6144\n"
    "ds_read_b128 v[140:143], v230 offset:7168\n"
    "v_mfma_f32_16x16x32_bf16 v[104:107], a[56:59], v[0:3], v[104:107]\n"
    "v_mfma_f32_16x16x32_bf16 v[104:107], a[112:115], v[4:7], v[104:107]\n"
    "ds_write_b128 v228, v[84:87] offset:1024\n"
    "v_mfma_f32_16x16x32_bf16 v[104:107], a[88:91], v[8:11], v[104:107]\n"
    "v_mfma_f32_16x16x32_bf16 v[104:107], a[120:123], v[12:15], v[104:107]\n"
    "v_mfma_f32_16x16x32_bf16 v[108:111], a[60:63], v[0:3], v[108:111]\n"
    "buffer_load_dwordx4 v[64:67], v254, s[28:31], s8 offen offset:0 sc1\n"
    "v_mfma_f32_16x16x32_bf16 v[108:111], a[116:119], v[4:7], v[108:111]\n"
    "v_cndmask_b32 v238, 0, v238, s[24:25]\n"
    "v_cndmask_b32 v239, 0, v239, s[24:25]\n"
    "v_mfma_f32_16x16x32_bf16 v[108:111], a[92:95], v[8:11], v[108:111]\n"
    "ds_write_b128 v228, v[88:91] offset:2048\n"
    "v_mfma_f32_16x16x32_bf16 v[108:111], a[124:127], v[12:15], v[108:111]\n"
    "v_add_u32 v218, v218, v238\n"
    "s_waitcnt vmcnt(11)\n"
    "s_waitcnt lgkmcnt(10)\n"
    "v_mfma_f32_16x16x32_bf16 v[112:115], a[128:131], v[16:19], v[112:115]\n"
    "v_add_u32 v219, v219, v239\n"
    "ds_read_b128 v[144:147], v230 offset:32768\n"
    "v_mfma_f32_16x16x32_bf16 v[112:115], a[160:163], v[20:23], v[112:115]\n"
    "ds_read_b128 v[148:151], v230 offset:33792\n"
    "s_waitcnt vmcnt(9)\n"
    "v_mfma_f32_16x16x32_bf16 v[112:115], a[192:195], v[24:27], v[112:115]\n"
    "buffer_load_dwordx4 v[68:71], v254, s[28:31], s9 offen offset:0 sc1\n"
    "v_mfma_f32_16x16x32_bf16 v[112:115], a[224:227], v[28:31], v[112:115]\n"
    "ds_write_b128 v228, v[92:95] offset:3072\n"
    "s_waitcnt lgkmcnt(12)\n"
    "v_mfma_f32_16x16x32_bf16 v[116:119], a[132:135], v[16:19], v[116:119]\n"
    "v_add3_u32 v218, v16, v17, v218\n"
    "v_add3_u32 v218, v18, v19, v218\n"
    "v_mfma_f32_16x16x32_bf16 v[116:119], a[164:167], v[20:23], v[116:119]\n"
    "v_add3_u32 v218, v20, v21, v218\n"
    "v_add3_u32 v218, v22, v23, v218\n"
    "v_mfma_f32_16x16x32_bf16 v[116:119], a[196:199], v[24:27], v[116:119]\n"
    "buffer_load_dwordx4 v[72:75], v254, s[28:31], s10 offen offset:0 sc1\n"
    "v_mfma_f32_16x16x32_bf16 v[116:119], a[228:231], v[28:31], v[116:119]\n"
    "v_add3_u32 v218, v24, v25, v218\n"
    "v_add3_u32 v218, v26, v27, v218\n"
    "s_waitcnt lgkmcnt(11)\n"
    "v_mfma_f32_16x16x32_bf16 v[120:123], a[136:139], v[16:19], v[120:123]\n"
    "ds_write_b128 v228, v[96:99] offset:4096\n"
    "v_mfma_f32_16x16x32_bf16 v[120:123], a[168:171], v[20:23], v[120:123]\n"
    "v_add3_u32 v218, v28, v29, v218\n"
    "v_add3_u32 v218, v30, v31, v218\n"
    "v_mfma_f32_16x16x32_bf16 v[120:123], a[200:203], v[24:27], v[120:123]\n"
    "ds_read_b128 v[152:155], v230 offset:34816\n"
    "ds_read_b128 v[156:159], v230 offset:35840\n"
    "v_mfma_f32_16x16x32_bf16 v[120:123], a[232:235], v[28:31], v[120:123]\n"
    "buffer_load_dwordx4 v[76:79], v254, s[28:31], s11 offen offset:0 sc1\n"
    "s_waitcnt lgkmcnt(13)\n"
    "v_mfma_f32_16x16x32_bf16 v[124:127], a[140:143], v[16:19], v[124:127]\n"
    "s_waitcnt vmcnt(11)\n"
    "v_add3_u32 v218, v32, v33, v218\n"
    "v_add3_u32 v218, v34, v35, v218\n"
    "v_mfma_f32_16x16x32_bf16 v[124:127], a[172:175], v[20:23], v[124:127]\n"
    "ds_write_b128 v228, v[100:103] offset:5120\n"
    "v_mfma_f32_16x16x32_bf16 v[124:127], a[204:207], v[24:27], v[124:127]\n"
    "s_waitcnt vmcnt(10)\n"
    "v_add3_u32 v218, v36, v37, v218\n"
    "v_add3_u32 v218, v38, v39, v218\n"
    "v_mfma_f32_16x16x32_bf16 v[124:127], a[236:239], v[28:31], v[124:127]\n"
    "s_waitcnt vmcnt(9)\n"
    "v_add3_u32 v218, v40, v41, v218\n"
    "v_add3_u32 v218, v42, v43, v218\n"
    "s_waitcnt lgkmcnt(12)\n"
    "v_mfma_f32_16x16x32_bf16 v[128:131], a[144:147], v[16:19], v[128:131]\n"
    "s_waitcnt vmcnt(8)\n"
    "v_add3_u32 v218, v44, v45, v218\n"
    "v_add3_u32 v218, v46, v47, v218\n"
    "v_mfma_f32_16x16x32_bf16 v[128:131], a[176:179], v[20:23], v[128:131]\n"
    "s_nop 0\n"
    "v_add_u32_dpp v218, v218, v218 row_shr:1 row_mask:0xf bank_mask:0xf\n"
    "ds_read_b128 v[160:163], v230 offset:36864\n"
    "v_mfma_f32_16x16x32_bf16 v[128:131], a[208:211], v[24:27], v[128:131]\n"
    "buffer_load_dwordx4 v[0:3], v254, s[28:31], s4 offen offset:1024 sc1\n"
    "v_mfma_f32_16x16x32_bf16 v[128:131], a[240:243], v[28:31], v[128:131]\n"
    "ds_write_b128 v228, v[104:107] offset:6144\n"
    "s_waitcnt lgkmcnt(13)\n"
    "v_mfma_f32_16x16x32_bf16 v[132:135], a[148:151], v[16:19], v[132:135]\n"
    "v_add_u32_dpp v218, v218, v218 row_shr:2 row_mask:0xf bank_mask:0xf\n"
    "ds_read_b128 v[164:167], v230 offset:37888\n"
    "v_mfma_f32_16x16x32_bf16 v[132:135], a[180:183], v[20:23], v[132:135]\n"
    "v_add_u32_dpp v218, v218, v218 row_shr:4 row_mask:0xf bank_mask:0xf\n"
    "ds_read_b128 v[168:171], v230 offset:38912\n"
    "v_mfma_f32_16x16x32_bf16 v[132:135], a[212:215], v[24:27], v[132:135]\n"
    "v_add_u32_dpp v218, v218, v218 row_shr:8 row_mask:0xf bank_mask:0xf\n"
    "ds_read_b128 v[172:175], v230 offset:39936\n"
    "v_mfma_f32_16x16x32_bf16 v[132:135], a[244:247], v[28:31], v[132:135]\n"
    "buffer_load_dwordx4 v[4:7], v254, s[28:31], s5 offen offset:1024 sc1\n"
    "s_waitcnt lgkmcnt(14)\n"
    "v_mfma_f32_16x16x32_bf16 v[136:139], a[152:155], v[16:19], v[136:139]\n"
    "ds_write_b128 v228, v[108:111] offset:7168\n"
    "v_mfma_f32_16x16x32_bf16 v[136:139], a[184:187], v[20:23], v[136:139]\n"
    "v_add_u32_dpp v218, v218, v218 row_bcast:15 row_mask:0xa bank_mask:0xf\n"
    "v_mfma_f32_16x16x32_bf16 v[136:139], a[216:219], v[24:27], v[136:139]\n"
    "s_nop 0\n"
    "v_add_u32_dpp v218, v218, v218 row_bcast:31 row_mask:0xc bank_mask:0xf\n"
    "v_mfma_f32_16x16x32_bf16 v[136:139], a[248:251], v[28:31], v[136:139]\n"
    "v_readlane_b32 s75, v218, 63\n"
    "v_mfma_f32_16x16x32_bf16 v[140:143], a[156:159], v[16:19], v[140:143]\n"
    "buffer_load_dwordx4 v[8:11], v254, s[28:31], s6 offen offset:1024 sc1\n"
    "v_mfma_f32_16x16x32_bf16 v[140:143], a[188:191], v[20:23], v[140:143]\n"
    "s_cmp_eq_u32 s74, 0\n"
    "s_cselect_b32 s75, 0, s75\n"
    "v_mfma_f32_16x16x32_bf16 v[140:143], a[220:223], v[24:27], v[140:143]\n"
    "v_mov_b32 v218, s75\n"
    "ds_write_b32 v222, v218\n"
    "v_mfma_f32_16x16x32_bf16 v[140:143], a[252:255], v[28:31], v[140:143]\n"
    "v_mfma_f32_16x16x32_bf16 v[112:115], a[0:3], v[32:35], v[112:115]\n"
    "v_mfma_f32_16x16x32_bf16 v[112:115], a[16:19], v[36:39], v[112:115]\n"
    "v_mfma_f32_16x16x32_bf16 v[112:115], a[64:67], v[40:43], v[112:115]\n"
    "buffer_load_dwordx4 v[12:15], v254, s[28:31], s7 offen offset:1024 sc1\n"
    "v_mfma_f32_16x16x32_bf16 v[112:115], a[96:99], v[44:47], v[112:115]\n"
    "v_mfma_f32_16x16x32_bf16 v[116:119], a[4:7], v[32:35], v[116:119]\n"
    "v_mfma_f32_16x16x32_bf16 v[116:119], a[20:23], v[36:39], v[116:119]\n"
    "v_mfma_f32_16x16x32_bf16 v[116:119], a[68:71], v[40:43], v[116:119]\n"
    "v_mfma_f32_16x16x32_bf16 v[116:119], a[100:103], v[44:47], v[116:119]\n"
    "s_waitcnt lgkmcnt(0)\n"
    "s_barrier\n"
    "v_mfma_f32_16x16x32_bf16 v[120:123], a[8:11], v[32:35], v[120:123]\n"
    "buffer_load_dwordx4 v[16:19], v254, s[28:31], s8 offen offset:1024 sc1\n"
    "v_mfma_f32_16x16x32_bf16 v[120:123], a[24:27], v[36:39], v[120:123]\n"
    "ds_read_b128 v[234:237], v223\n"
    "ds_read_b128 v[176:179], v231 offset:0\n"
    "v_mfma_f32_16x16x32_bf16 v[120:123], a[72:75], v[40:43], v[120:123]\n"
    "ds_read_b128 v[180:183], v231 offset:1024\n"
    "ds_read_b128 v[184:187], v231 offset:2048\n"
    "v_mfma_f32_16x16x32_bf16 v[120:123], a[104:107], v[44:47], v[120:123]\n"
    "ds_read_b128 v[188:191], v231 offset:3072\n"
    "ds_read_b128 v[196:199], v231 offset:4096\n"
    "v_mfma_f32_16x16x32_bf16 v[124:127], a[12:15], v[32:35], v[124:127]\n"
    "ds_read_b128 v[200:203], v231 offset:5120\n"
    "ds_read_b128 v[204:207], v231 offset:6144\n"
    "v_mfma_f32_16x16x32_bf16 v[124:127], a[28:31], v[36:39], v[124:127]\n"
    "buffer_load_dwordx4 v[20:23], v254, s[28:31], s9 offen offset:1024 sc1\n"
    "v_mfma_f32_16x16x32_bf16 v[124:127], a[76:79], v[40:43], v[124:127]\n"
    "ds_read_b128 v[208:211], v231 offset:7168\n"
    "v_mfma_f32_16x16x32_bf16 v[124:127], a[108:111], v[44:47], v[124:127]\n"
    "v_mfma_f32_16x16x32_bf16 v[128:131], a[48:51], v[32:35], v[128:131]\n"
    "s_waitcnt lgkmcnt(8)\n"
    "v_or3_b32 v234, v234, v235, v236\n"
    "v_or_b32 v234, v234, v237\n"
    "v_mfma_f32_16x16x32_bf16 v[128:131], a[32:35], v[36:39], v[128:131]\n"
    "v_cmp_ne_u32 vcc, 0, v234\n"
    "v_mfma_f32_16x16x32_bf16 v[128:131], a[80:83], v[40:43], v[128:131]\n"
    "buffer_load_dwordx4 v[24:27], v254, s[28:31], s10 offen offset:1024 sc1\n"
    "v_mfma_f32_16x16x32_bf16 v[128:131], a[40:43], v[44:47], v[128:131]\n"
    "v_mfma_f32_16x16x32_bf16 v[132:135], a[52:55], v[32:35], v[132:135]\n"
    "s_nop 0\n"
    "s_cbranch_vccnz Lredo%=\n"
    "s_waitcnt lgkmcnt(7)\n"
    "v_add_f32 v144, v144, v176\n"
    "v_mfma_f32_16x16x32_bf16 v[132:135], a[36:39], v[36:39], v[132:135]\n"
    "v_add_f32 v145, v145, v177\n"
    "v_add_f32 v146, v146, v178\n"
    "v_mfma_f32_16x16x32_bf16 v[132:135], a[84:87], v[40:43], v[132:135]\n"
    "v_add_f32 v147, v147, v179\n"
    "s_waitcnt lgkmcnt(6)\n"
    "v_add_f32 v148, v148, v180\n"
    "v_mfma_f32_16x16x32_bf16 v[132:135], a[44:47], v[44:47], v[132:135]\n"
    "buffer_load_dwordx4 v[28:31], v254, s[28:31], s11 offen offset:1024 sc1\n"
    "v_mfma_f32_16x16x32_bf16 v[136:139], a[56:59], v[32:35], v[136:139]\n"
    "v_add_f32 v149, v149, v181\n"
    "v_add_f32 v150, v150, v182\n"
    "v_mfma_f32_16x16x32_bf16 v[136:139], a[112:115], v[36:39], v[136:139]\n"
    "v_add_f32 v151, v151, v183\n"
    "s_waitcnt lgkmcnt(5)\n"
    "v_add_f32 v152, v152, v184\n"
    "v_mfma_f32_16x16x32_bf16 v[136:139], a[88:91], v[40:43], v[136:139]\n"
    "v_add_f32 v153, v153, v185\n"
    "v_add_f32 v154, v154, v186\n"
    "v_mfma_f32_16x16x32_bf16 v[136:139], a[120:123], v[44:47], v[136:139]\n"
    "v_add_f32 v155, v155, v187\n"
    "s_waitcnt lgkmcnt(4)\n"
    "v_add_f32 v156, v156, v188\n"
    "v_mfma_f32_16x16x32_bf16 v[140:143], a[60:63], v[32:35], v[140:143]\n"
    "v_add_f32 v157, v157, v189\n"
    "v_add_f32 v158, v158, v190\n"
    "v_mfma_f32_16x16x32_bf16 v[140:143], a[116:119], v[36:39], v[140:143]\n"
    "v_add_f32 v159, v159, v191\n"
    "s_waitcnt lgkmcnt(3)\n"
    "v_add_f32 v160, v160, v196\n"
    "v_mfma_f32_16x16x32_bf16 v[140:143], a[92:95], v[40:43], v[140:143]\n"
    "v_add_f32 v161, v161, v197\n"
    "v_add_f32 v162, v162, v198\n"
    "v_mfma_f32_16x16x32_bf16 v[140:143], a[124:127], v[44:47], v[140:143]\n"
    "v_add_f32 v163, v163, v199\n"
    "s_waitcnt vmcnt(14)\n"
    "v_mfma_f32_16x16x32_bf16 v[80:83], a[128:131], v[48:51], 0\n"
    "s_waitcnt lgkmcnt(2)\n"
    "v_add_f32 v164, v164, v200\n"
    "v_add_f32 v165, v165, v201\n"
    "v_mfma_f32_16x16x32_bf16 v[80:83], a[160:163], v[52:55], v[80:83]\n"
    "v_add_f32 v166, v166, v202\n"
    "s_waitcnt vmcnt(12)\n"
    "v_mfma_f32_16x16x32_bf16 v[80:83], a[192:195], v[56:59], v[80:83]\n"
    "buffer_load_dwordx4 v[32:35], v254, s[28:31], s4 offen offset:2048 sc1\n"
    "v_mfma_f32_16x16x32_bf16 v[80:83], a[224:227], v[60:63], v[80:83]\n"
    "v_add3_u32 v219, v48, v49, v219\n"
    "v_add3_u32 v219, v50, v51, v219\n"
    "v_mfma_f32_16x16x32_bf16 v[84:87], a[132:135], v[48:51], 0\n"
    "v_add3_u32 v219, v52, v53, v219\n"
    "v_add3_u32 v219, v54, v55, v219\n"
    "v_mfma_f32_16x16x32_bf16 v[84:87], a[164:167], v[52:55], v[84:87]\n"
    "v_add3_u32 v219, v56, v57, v219\n"
    "v_add3_u32 v219, v58, v59, v219\n"
    "v_mfma_f32_16x16x32_bf16 v[84:87], a[196:199], v[56:59], v[84:87]\n"
    "v_add3_u32 v219, v60, v61, v219\n"
    "v_add3_u32 v219, v62, v63, v219\n"
    "v_mfma_f32_16x16x32_bf16 v[84:87], a[228:231], v[60:63], v[84:87]\n"
    "buffer_load_dwordx4 v[36:39], v254, s[28:31], s5 offen offset:2048 sc1\n"
    "v_mfma_f32_16x16x32_bf16 v[88:91], a[136:139], v[48:51], 0\n"
    "v_add_f32 v167, v167, v203\n"
    "s_waitcnt lgkmcnt(1)\n"
    "v_add_f32 v168, v168, v204\n"
    "v_mfma_f32_16x16x32_bf16 v[88:91], a[168:171], v[52:55], v[88:91]\n"
    "v_add_f32 v169, v169, v205\n"
    "v_add_f32 v170, v170, v206\n"
    "v_mfma_f32_16x16x32_bf16 v[88:91], a[200:203], v[56:59], v[88:91]\n"
    "v_add_f32 v171, v171, v207\n"
    "s_waitcnt lgkmcnt(0)\n"
    "v_add_f32 v172, v172, v208\n"
    "v_mfma_f32_16x16x32_bf16 v[88:91], a[232:235], v[60:63], v[88:91]\n"
    "v_add_f32 v173, v173, v209\n"
    "v_add_f32 v174, v174, v210\n"
    "v_mfma_f32_16x16x32_bf16 v[92:95], a[140:143], v[48:51], 0\n"
    "buffer_load_dwordx4 v[40:43], v254, s[28:31], s6 offen offset:2048 sc1\n"
    "v_mfma_f32_16x16x32_bf16 v[92:95], a[172:175], v[52:55], v[92:95]\n"
    "v_add_f32 v175, v175, v211\n"
    "v_add_f32 v144, v144, v112\n"
    "v_mfma_f32_16x16x32_bf16 v[92:95], a[204:207], v[56:59], v[92:95]\n"
    "v_add_f32 v145, v145, v113\n"
    "v_add_f32 v146, v146, v114\n"
    "v_mfma_f32_16x16x32_bf16 v[92:95], a[236:239], v[60:63], v[92:95]\n"
    "v_add_f32 v147, v147, v115\n"
    "v_add_f32 v148, v148, v116\n"
    "v_mfma_f32_16x16x32_bf16 v[96:99], a[144:147], v[48:51], 0\n"
    "v_add_f32 v149, v149, v117\n"
    "v_add_f32 v150, v150, v118\n"
    "v_mfma_f32_16x16x32_bf16 v[96:99], a[176:179], v[52:55], v[96:99]\n"
    "buffer_load_dwordx4 v[44:47], v254, s[28:31], s7 offen offset:2048 sc1\n"
    "v_mfma_f32_16x16x32_bf16 v[96:99], a[208:211], v[56:59], v[96:99]\n"
    "v_add_f32 v151, v151, v119\n"
    "v_cvt_pk_bf16_f32 v144, v144, v145\n"
    "v_mfma_f32_16x16x32_bf16 v[96:99], a[240:243], v[60:63], v[96:99]\n"
    "v_cvt_pk_bf16_f32 v145, v146, v147\n"
    "v_cvt_pk_bf16_f32 v146, v148, v149\n"
    "v_mfma_f32_16x16x32_bf16 v[100:103], a[148:151], v[48:51], 0\n"
    "v_cvt_pk_bf16_f32 v147, v150, v151\n"
    "v_add3_u32 v232, v144, v145, v250\n"
    "v_mfma_f32_16x16x32_bf16 v[100:103], a[180:183], v[52:55], v[100:103]\n"
    "v_add3_u32 v232, v146, v147, v232\n"
    "v_add_f32 v152, v152, v120\n"
    "v_mfma_f32_16x16x32_bf16 v[100:103], a[212:215], v[56:59], v[100:103]\n"
    "v_add_f32 v153, v153, v121\n"
    "v_add_f32 v154, v154, v122\n"
    "v_mfma_f32_16x16x32_bf16 v[100:103], a[244:247], v[60:63], v[100:103]\n"
    "v_add_f32 v155, v155, v123\n"
    "v_add_f32 v156, v156, v124\n"
    "v_mfma_f32_16x16x32_bf16 v[104:107], a[152:155], v[48:51], 0\n"
    "v_add_f32 v157, v157, v125\n"
    "v_add_f32 v158, v158, v126\n"
    "v_mfma_f32_16x16x32_bf16 v[104:107], a[184:187], v[52:55], v[104:107]\n"
    "v_add_f32 v159, v159, v127\n"
    "v_cvt_pk_bf16_f32 v152, v152, v153\n"
    "v_mfma_f32_16x16x32_bf16 v[104:107], a[216:219], v[56:59], v[104:107]\n"
    "v_cvt_pk_bf16_f32 v153, v154, v155\n"
    "v_cvt_pk_bf16_f32 v154, v156, v157\n"
    "v_mfma_f32_16x16x32_bf16 v[104:107], a[248:251], v[60:63], v[104:107]\n"
    "v_cvt_pk_bf16_f32 v155, v158, v159\n"
    "v_add3_u32 v232, v152, v153, v232\n"
    "v_mfma_f32_16x16x32_bf16 v[108:111], a[156:159], v[48:51], 0\n"
    "v_add3_u32 v232, v154, v155, v232\n"
    "v_add_f32 v160, v160, v128\n"
    "v_mfma_f32_16x16x32_bf16 v[108:111], a[188:191], v[52:55], v[108:111]\n"
    "v_add_f32 v161, v161, v129\n"
    "v_add_f32 v162, v162, v130\n"
    "v_mfma_f32_16x16x32_bf16 v[108:111], a[220:223], v[56:59], v[108:111]\n"
    "v_add_f32 v163, v163, v131\n"
    "v_add_f32 v164, v164, v132\n"
    "v_mfma_f32_16x16x32_bf16 v[108:111], a[252:255], v[60:63], v[108:111]\n"
    "v_add_f32 v165, v165, v133\n"
    "s_waitcnt vmcnt(14)\n"
    "v_mfma_f32_16x16x32_bf16 v[80:83], a[0:3], v[64:67], v[80:83]\n"
    "v_add_f32 v166, v166, v134\n"
    "v_add_f32 v167, v167, v135\n"
    "v_mfma_f32_16x16x32_bf16 v[80:83], a[16:19], v[68:71], v[80:83]\n"
    "v_cvt_pk_bf16_f32 v160, v160, v161\n"
    "s_waitcnt vmcnt(12)\n"
    "v_mfma_f32_16x16x32_bf16 v[80:83], a[64:67], v[72:75], v[80:83]\n"
    "buffer_load_dwordx4 v[48:51], v254, s[28:31], s8 offen offset:2048 sc1\n"
    "v_mfma_f32_16x16x32_bf16 v[80:83], a[96:99], v[76:79], v[80:83]\n"
    "buffer_load_dwordx4 v[52:55], v254, s[28:31], s9 offen offset:2048 sc1\n"
    "v_mfma_f32_16x16x32_bf16 v[84:87], a[4:7], v[64:67], v[84:87]\n"
    "v_add3_u32 v219, v64, v65, v219\n"
    "v_add3_u32 v219, v66, v67, v219\n"
    "v_mfma_f32_16x16x32_bf16 v[84:87], a[20:23], v[68:71], v[84:87]\n"
    "buffer_load_dwordx4 v[56:59], v254, s[28:31], s10 offen offset:2048 sc1\n"
    "v_mfma_f32_16x16x32_bf16 v[84:87], a[68:71], v[72:75], v[84:87]\n"
    "v_add3_u32 v219, v68, v69, v219\n"
    "v_add3_u32 v219, v70, v71, v219\n"
    "v_mfma_f32_16x16x32_bf16 v[84:87], a[100:103], v[76:79], v[84:87]\n"
    "buffer_load_dwordx4 v[60:63], v254, s[28:31], s11 offen offset:2048 sc1\n"
    "v_mfma_f32_16x16x32_bf16 v[88:91], a[8:11], v[64:67], v[88:91]\n"
    "v_add3_u32 v219, v72, v73, v219\n"
    "v_add3_u32 v219, v74, v75, v219\n"
    "v_mfma_f32_16x16x32_bf16 v[88:91], a[24:27], v[68:71], v[88:91]\n"
    "v_add3_u32 v219, v76, v77, v219\n"
    "v_add3_u32 v219, v78, v79, v219\n"
    "v_mfma_f32_16x16x32_bf16 v[88:91], a[72:75], v[72:75], v[88:91]\n"
    "v_cvt_pk_bf16_f32 v161, v162, v163\n"
    "v_cvt_pk_bf16_f32 v162, v164, v165\n"
    "v_mfma_f32_16x16x32_bf16 v[88:91], a[104:107], v[76:79], v[88:91]\n"
    "v_cvt_pk_bf16_f32 v163, v166, v167\n"
    "v_add3_u32 v232, v160, v161, v232\n"
    "v_mfma_f32_16x16x32_bf16 v[92:95], a[12:15], v[64:67], v[92:95]\n"
    "v_add3_u32 v232, v162, v163, v232\n"
    "v_add_f32 v168, v168, v136\n"
    "v_mfma_f32_16x16x32_bf16 v[92:95], a[28:31], v[68:71], v[92:95]\n"
    "v_add_f32 v169, v169, v137\n"
    "v_add_f32 v170, v170, v138\n"
    "v_mfma_f32_16x16x32_bf16 v[92:95], a[76:79], v[72:75], v[92:95]\n"
    "v_add_f32 v171, v171, v139\n"
    "v_add_f32 v172, v172, v140\n"
    "v_mfma_f32_16x16x32_bf16 v[92:95], a[108:111], v[76:79], v[92:95]\n"
    "v_add_f32 v173, v173, v141\n"
    "v_add_f32 v174, v174, v142\n"
    "v_mfma_f32_16x16x32_bf16 v[96:99], a[48:51], v[64:67], v[96:99]\n"
    "v_add_f32 v175, v175, v143\n"
    "v_cvt_pk_bf16_f32 v168, v168, v169\n"
    "v_mfma_f32_16x16x32_bf16 v[96:99], a[32:35], v[68:71], v[96:99]\n"
    "v_cvt_pk_bf16_f32 v169, v170, v171\n"
    "v_cvt_pk_bf16_f32 v170, v172, v173\n"
    "v_mfma_f32_16x16x32_bf16 v[96:99], a[80:83], v[72:75], v[96:99]\n"
    "v_cvt_pk_bf16_f32 v171, v174, v175\n"
    "v_add3_u32 v232, v168, v169, v232\n"
    "v_mfma_f32_16x16x32_bf16 v[96:99], a[40:43], v[76:79], v[96:99]\n"
    "v_add3_u32 v232, v170, v171, v232\n"
    "v_sub_u32 v232, 0, v232\n"
    "v_mfma_f32_16x16x32_bf16 v[100:103], a[52:55], v[64:67], v[100:103]\n"
    "v_mfma_f32_16x16x32_bf16 v[100:103], a[36:39], v[68:71], v[100:103]\n"
    "buffer_store_dwordx4 v[144:147], v216, s[28:31], s12 offen offset:0 sc1\n"
    "v_mfma_f32_16x16x32_bf16 v[100:103], a[84:87], v[72:75], v[100:103]\n"
    "v_mfma_f32_16x16x32_bf16 v[100:103], a[44:47], v[76:79], v[100:103]\n"
    "buffer_store_dwordx4 v[152:155], v216, s[28:31], s12 offen offset:1024 sc1\n"
    "v_mfma_f32_16x16x32_bf16 v[104:107], a[56:59], v[64:67], v[104:107]\n"
    "v_mfma_f32_16x16x32_bf16 v[104:107], a[112:115], v[68:71], v[104:107]\n"
    "buffer_store_dwordx4 v[160:163], v216, s[28:31], s12 offen offset:2048 sc1\n"
    "v_mfma_f32_16x16x32_bf16 v[104:107], a[88:91], v[72:75], v[104:107]\n"
    "v_mfma_f32_16x16x32_bf16 v[104:107], a[120:123], v[76:79], v[104:107]\n"
    "buffer_store_dwordx4 v[168:171], v216, s[28:31], s12 offen offset:3072 sc1\n"
    "v_mfma_f32_16x16x32_bf16 v[108:111], a[60:63], v[64:67], v[108:111]\n"
    "buffer_store_dword v232, v225, s[28:31], s16 offen sc1\n"
    "v_mfma_f32_16x16x32_bf16 v[108:111], a[116:119], v[68:71], v[108:111]\n"
    "v_mfma_f32_16x16x32_bf16 v[108:111], a[92:95], v[72:75], v[108:111]\n"
    "v_mfma_f32_16x16x32_bf16 v[108:111], a[124:127], v[76:79], v[108:111]\n"
    "s_waitcnt vmcnt(19)\n"
    "v_mfma_f32_16x16x32_bf16 v[112:115], a[128:131], v[0:3], 0\n"
    "ds_write_b64 v226, v[80:81] offset:0\n"
    "v_mfma_f32_16x16x32_bf16 v[112:115], a[160:163], v[4:7], v[112:115]\n"
    "s_waitcnt vmcnt(17)\n"
    "v_mfma_f32_16x16x32_bf16 v[112:115], a[192:195], v[8:11], v[112:115]\n"
    "v_mfma_f32_16x16x32_bf16 v[112:115], a[224:227], v[12:15], v[112:115]\n"
    "ds_write_b64 v226, v[82:83] offset:8\n"
    "v_mfma_f32_16x16x32_bf16 v[116:119], a[132:135], v[0:3], 0\n"
    "v_add3_u32 v219, v0, v1, v219\n"
    "v_add3_u32 v219, v2, v3, v219\n"
    "v_mfma_f32_16x16x32_bf16 v[116:119], a[164:167], v[4:7], v[116:119]\n"
    "v_add3_u32 v219, v4, v5, v219\n"
    "v_add3_u32 v219, v6, v7, v219\n"
    "v_mfma_f32_16x16x32_bf16 v[116:119], a[196:199], v[8:11], v[116:119]\n"
    "v_add3_u32 v219, v8, v9, v219\n"
    "v_add3_u32 v219, v10, v11, v219\n"
    "v_mfma_f32_16x16x32_bf16 v[116:119], a[228:231], v[12:15], v[116:119]\n"
    "ds_write_b64 v226, v[84:85] offset:1024\n"
    "v_mfma_f32_16x16x32_bf16 v[120:123], a[136:139], v[0:3], 0\n"
    "v_add3_u32 v219, v12, v13, v219\n"
    "v_add3_u32 v219, v14, v15, v219\n"
    "v_mfma_f32_16x16x32_bf16 v[120:123], a[168:171], v[4:7], v[120:123]\n"
    "v_mfma_f32_16x16x32_bf16 v[120:123], a[200:203], v[8:11], v[120:123]\n"
    "buffer_load_dwordx4 v[64:67], v254, s[28:31], s4 offen offset:3072 sc1\n"
    "v_mfma_f32_16x16x32_bf16 v[120:123], a[232:235], v[12:15], v[120:123]\n"
    "ds_write_b64 v226, v[86:87] offset:1032\n"
    "v_mfma_f32_16x16x32_bf16 v[124:127], a[140:143], v[0:3], 0\n"
    "v_mfma_f32_16x16x32_bf16 v[124:127], a[172:175], v[4:7], v[124:127]\n"
    "buffer_load_dwordx4 v[68:71], v254, s[28:31], s5 offen offset:3072 sc1\n"
    "v_mfma_f32_16x16x32_bf16 v[124:127], a[204:207], v[8:11], v[124:127]\n"
    "v_mfma_f32_16x16x32_bf16 v[124:127], a[236:239], v[12:15], v[124:127]\n"
    "ds_write_b64 v226, v[88:89] offset:2048\n"
    "v_mfma_f32_16x16x32_bf16 v[128:131], a[144:147], v[0:3], 0\n"
    "buffer_load_dwordx4 v[72:75], v254, s[28:31], s6 offen offset:3072 sc1\n"
    "v_mfma_f32_16x16x32_bf16 v[128:131], a[176:179], v[4:7], v[128:131]\n"
    "v_mfma_f32_16x16x32_bf16 v[128:131], a[208:211], v[8:11], v[128:131]\n"
    "ds_write_b64 v226, v[90:91] offset:2056\n"
    "v_mfma_f32_16x16x32_bf16 v[128:131], a[240:243], v[12:15], v[128:131]\n"
    "buffer_load_dwordx4 v[76:79], v254, s[28:31], s7 offen offset:3072 sc1\n"
    "v_mfma_f32_16x16x32_bf16 v[132:135], a[148:151], v[0:3], 0\n"
    "v_mfma_f32_16x16x32_bf16 v[132:135], a[180:183], v[4:7], v[132:135]\n"
    "v_mfma_f32_16x16x32_bf16 v[132:135], a[212:215], v[8:11], v[132:135]\n"
    "ds_write_b64 v226, v[92:93] offset:3072\n"
    "v_mfma_f32_16x16x32_bf16 v[132:135], a[244:247], v[12:15], v[132:135]\n"
    "v_mfma_f32_16x16x32_bf16 v[136:139], a[152:155], v[0:3], 0\n"
    "v_mfma_f32_16x16x32_bf16 v[136:139], a[184:187], v[4:7], v[136:139]\n"
    "v_mfma_f32_16x16x32_bf16 v[136:139], a[216:219], v[8:11], v[136:139]\n"
    "ds_write_b64 v226, v[94:95] offset:3080\n"
    "v_mfma_f32_16x16x32_bf16 v[136:139], a[248:251], v[12:15], v[136:139]\n"
    "v_mfma_f32_16x16x32_bf16 v[140:143], a[156:159], v[0:3], 0\n"
    "v_mfma_f32_16x16x32_bf16 v[140:143], a[188:191], v[4:7], v[140:143]\n"
    "v_mfma_f32_16x16x32_bf16 v[140:143], a[220:223], v[8:11], v[140:143]\n"
    "ds_write_b64 v226, v[96:97] offset:4096\n"
    "v_mfma_f32_16x16x32_bf16 v[140:143], a[252:255], v[12:15], v[140:143]\n"
    "s_waitcnt vmcnt(19)\n"
    "v_mfma_f32_16x16x32_bf16 v[112:115], a[0:3], v[16:19], v[112:115]\n"
    "v_mfma_f32_16x16x32_bf16 v[112:115], a[16:19], v[20:23], v[112:115]\n"
    "s_waitcnt vmcnt(17)\n"
    "v_mfma_f32_16x16x32_bf16 v[112:115], a[64:67], v[24:27], v[112:115]\n"
    "ds_write_b64 v226, v[98:99] offset:4104\n"
    "v_mfma_f32_16x16x32_bf16 v[112:115], a[96:99], v[28:31], v[112:115]\n"
    "v_add3_u32 v219, v16, v17, v219\n"
    "v_add3_u32 v219, v18, v19, v219\n"
    "v_mfma_f32_16x16x32_bf16 v[116:119], a[4:7], v[16:19], v[116:119]\n"
    "v_add3_u32 v219, v20, v21, v219\n"
    "v_add3_u32 v219, v22, v23, v219\n"
    "v_mfma_f32_16x16x32_bf16 v[116:119], a[20:23], v[20:23], v[116:119]\n"
    "ds_write_b64 v226, v[100:101] offset:5120\n"
    "v_mfma_f32_16x16x32_bf16 v[116:119], a[68:71], v[24:27], v[116:119]\n"
    "v_add3_u32 v219, v24, v25, v219\n"
    "v_add3_u32 v219, v26, v27, v219\n"
    "v_mfma_f32_16x16x32_bf16 v[116:119], a[100:103], v[28:31], v[116:119]\n"
    "v_add3_u32 v219, v28, v29, v219\n"
    "v_add3_u32 v219, v30, v31, v219\n"
    "v_mfma_f32_16x16x32_bf16 v[120:123], a[8:11], v[16:19], v[120:123]\n"
    "v_mfma_f32_16x16x32_bf16 v[120:123], a[24:27], v[20:23], v[120:123]\n"
    "ds_write_b64 v226, v[102:103] offset:5128\n"
    "v_mfma_f32_16x16x32_bf16 v[120:123], a[72:75], v[24:27], v[120:123]\n"
    "v_mfma_f32_16x16x32_bf16 v[120:123], a[104:107], v[28:31], v[120:123]\n"
    "v_mfma_f32_16x16x32_bf16 v[124:127], a[12:15], v[16:19], v[124:127]\n"
    "v_mfma_f32_16x16x32_bf16 v[124:127], a[28:31], v[20:23], v[124:127]\n"
    "ds_write_b64 v226, v[104:105] offset:6144\n"
    "v_mfma_f32_16x16x32_bf16 v[124:127], a[76:79], v[24:27], v[124:127]\n"
    "v_mfma_f32_16x16x32_bf16 v[124:127], a[108:111], v[28:31], v[124:127]\n"
    "v_mfma_f32_16x16x32_bf16 v[128:131], a[48:51], v[16:19], v[128:131]\n"
    "ds_write_b64 v226, v[106:107] offset:6152\n"
    "v_mfma_f32_16x16x32_bf16 v[128:131], a[32:35], v[20:23], v[128:131]\n"
    "v_mfma_f32_16x16x32_bf16 v[128:131], a[80:83], v[24:27], v[128:131]\n"
    "v_mfma_f32_16x16x32_bf16 v[128:131], a[40:43], v[28:31], v[128:131]\n"
    "v_mfma_f32_16x16x32_bf16 v[132:135], a[52:55], v[16:19], v[132:135]\n"
    "ds_write_b64 v226, v[108:109] offset:7168\n"
    "v_mfma_f32_16x16x32_bf16 v[132:135], a[36:39], v[20:23], v[132:135]\n"
    "v_mfma_f32_16x16x32_bf16 v[132:135], a[84:87], v[24:27], v[132:135]\n"
    "v_mfma_f32_16x16x32_bf16 v[132:135], a[44:47], v[28:31], v[132:135]\n"
    "v_mfma_f32_16x16x32_bf16 v[136:139], a[56:59], v[16:19], v[136:139]\n"
    "ds_write_b64 v226, v[110:111] offset:7176\n"
    "v_mfma_f32_16x16x32_bf16 v[136:139], a[112:115], v[20:23], v[136:139]\n"
    "ds_write_b64 v227, v[112:113] offset:0\n"
    "v_mfma_f32_16x16x32_bf16 v[136:139], a[88:91], v[24:27], v[136:139]\n"
    "ds_write_b64 v227, v[114:115] offset:8\n"
    "v_mfma_f32_16x16x32_bf16 v[136:139], a[120:123], v[28:31], v[136:139]\n"
    "ds_write_b64 v227, v[116:117] offset:1024\n"
    "v_mfma_f32_16x16x32_bf16 v[140:143], a[60:63], v[16:19], v[140:143]\n"
    "ds_write_b64 v227, v[118:119] offset:1032\n"
    "v_mfma_f32_16x16x32_bf16 v[140:143], a[116:119], v[20:23], v[140:143]\n"
    "ds_write_b64 v227, v[120:121] offset:2048\n"
    "v_mfma_f32_16x16x32_bf16 v[140:143], a[92:95], v[24:27], v[140:143]\n"
    "v_mfma_f32_16x16x32_bf16 v[140:143], a[124:127], v[28:31], v[140:143]\n"
    "s_waitcnt vmcnt(15)\n"
    "v_mfma_f32_16x16x32_bf16 v[80:83], a[128:131], v[32:35], 0\n"
    "ds_write_b64 v227, v[122:123] offset:2056\n"
    "v_mfma_f32_16x16x32_bf16 v[80:83], a[160:163], v[36:39], v[80:83]\n"
    "s_waitcnt vmcnt(13)\n"
    "v_mfma_f32_16x16x32_bf16 v[80:83], a[192:195], v[40:43], v[80:83]\n"
    "v_mfma_f32_16x16x32_bf16 v[80:83], a[224:227], v[44:47], v[80:83]\n"
    "ds_write_b64 v227, v[124:125] offset:3072\n"
    "v_mfma_f32_16x16x32_bf16 v[84:87], a[132:135], v[32:35], 0\n"
    "v_add3_u32 v219, v32, v33, v219\n"
    "v_add3_u32 v219, v34, v35, v219\n"
    "v_mfma_f32_16x16x32_bf16 v[84:87], a[164:167], v[36:39], v[84:87]\n"
    "v_add3_u32 v219, v36, v37, v219\n"
    "v_add3_u32 v219, v38, v39, v219\n"
    "v_mfma_f32_16x16x32_bf16 v[84:87], a[196:199], v[40:43], v[84:87]\n"
    "v_add3_u32 v219, v40, v41, v219\n"
    "v_add3_u32 v219, v42, v43, v219\n"
    "v_mfma_f32_16x16x32_bf16 v[84:87], a[228:231], v[44:47], v[84:87]\n"
    "ds_write_b64 v227, v[126:127] offset:3080\n"
    "v_mfma_f32_16x16x32_bf16 v[88:91], a[136:139], v[32:35], 0\n"
    "v_add3_u32 v219, v44, v45, v219\n"
    "v_add3_u32 v219, v46, v47, v219\n"
    "v_mfma_f32_16x16x32_bf16 v[88:91], a[168:171], v[36:39], v[88:91]\n"
    "v_mfma_f32_16x16x32_bf16 v[88:91], a[200:203], v[40:43], v[88:91]\n"
    "v_mfma_f32_16x16x32_bf16 v[88:91], a[232:235], v[44:47], v[88:91]\n"
    "buffer_load_dwordx4 v[0:3], v254, s[28:31], s8 offen offset:3072 sc1\n"
    "v_mfma_f32_16x16x32_bf16 v[92:95], a[140:143], v[32:35], 0\n"
    "ds_write_b64 v227, v[128:129] offset:4096\n"
    "v_mfma_f32_16x16x32_bf16 v[92:95], a[172:175], v[36:39], v[92:95]\n"
    "v_mfma_f32_16x16x32_bf16 v[92:95], a[204:207], v[40:43], v[92:95]\n"
    "buffer_load_dwordx4 v[4:7], v254, s[28:31], s9 offen offset:3072 sc1\n"
    "v_mfma_f32_16x16x32_bf16 v[92:95], a[236:239], v[44:47], v[92:95]\n"
    "ds_write_b64 v227, v[130:131] offset:4104\n"
    "v_mfma_f32_16x16x32_bf16 v[96:99], a[144:147], v[32:35], 0\n"
    "v_mfma_f32_16x16x32_bf16 v[96:99], a[176:179], v[36:39], v[96:99]\n"
    "buffer_load_dwordx4 v[8:11], v254, s[28:31], s10 offen offset:3072 sc1\n"
    "v_mfma_f32_16x16x32_bf16 v[96:99], a[208:211], v[40:43], v[96:99]\n"
    "ds_write_b64 v227, v[132:133] offset:5120\n"
    "v_mfma_f32_16x16x32_bf16 v[96:99], a[240:243], v[44:47], v[96:99]\n"
    "v_mfma_f32_16x16x32_bf16 v[100:103], a[148:151], v[32:35], 0\n"
    "buffer_load_dwordx4 v[12:15], v254, s[28:31], s11 offen offset:3072 sc1\n"
    "v_mfma_f32_16x16x32_bf16 v[100:103], a[180:183], v[36:39], v[100:103]\n"
    "v_mfma_f32_16x16x32_bf16 v[100:103], a[212:215], v[40:43], v[100:103]\n"
    "ds_write_b64 v227, v[134:135] offset:5128\n"
    "v_mfma_f32_16x16x32_bf16 v[100:103], a[244:247], v[44:47], v[100:103]\n"
    "v_mfma_f32_16x16x32_bf16 v[104:107], a[152:155], v[32:35], 0\n"
    "v_mfma_f32_16x16x32_bf16 v[104:107], a[184:187], v[36:39], v[104:107]\n"
    "v_mfma_f32_16x16x32_bf16 v[104:107], a[216:219], v[40:43], v[104:107]\n"
    "ds_write_b64 v227, v[136:137] offset:6144\n"
    "v_mfma_f32_16x16x32_bf16 v[104:107], a[248:251], v[44:47], v[104:107]\n"
    "v_mfma_f32_16x16x32_bf16 v[108:111], a[156:159], v[32:35], 0\n"
    "v_mfma_f32_16x16x32_bf16 v[108:111], a[188:191], v[36:39], v[108:111]\n"
    "v_mfma_f32_16x16x32_bf16 v[108:111], a[220:223], v[40:43], v[108:111]\n"
    "ds_write_b64 v227, v[138:139] offset:6152\n"
    "v_mfma_f32_16x16x32_bf16 v[108:111], a[252:255], v[44:47], v[108:111]\n"
    "s_waitcnt vmcnt(15)\n"
    "v_mfma_f32_16x16x32_bf16 v[80:83], a[0:3], v[48:51], v[80:83]\n"
    "v_mfma_f32_16x16x32_bf16 v[80:83], a[16:19], v[52:55], v[80:83]\n"
    "s_waitcnt vmcnt(13)\n"
    "v_mfma_f32_16x16x32_bf16 v[80:83], a[64:67], v[56:59], v[80:83]\n"
    "ds_write_b64 v227, v[140:141] offset:7168\n"
    "v_mfma_f32_16x16x32_bf16 v[80:83], a[96:99], v[60:63], v[80:83]\n"
    "v_add3_u32 v219, v48, v49, v219\n"
    "v_add3_u32 v219, v50, v51, v219\n"
    "v_mfma_f32_16x16x32_bf16 v[84:87], a[4:7], v[48:51], v[84:87]\n"
    "v_add3_u32 v219, v52, v53, v219\n"
    "v_add3_u32 v219, v54, v55, v219\n"
    "v_mfma_f32_16x16x32_bf16 v[84:87], a[20:23], v[52:55], v[84:87]\n"
    "v_add3_u32 v219, v56, v57, v219\n"
    "v_add3_u32 v219, v58, v59, v219\n"
    "v_mfma_f32_16x16x32_bf16 v[84:87], a[68:71], v[56:59], v[84:87]\n"
    "ds_write_b64 v227, v[142:143] offset:7176\n"
    "v_mfma_f32_16x16x32_bf16 v[84:87], a[100:103], v[60:63], v[84:87]\n"
    "v_add3_u32 v219, v60, v61, v219\n"
    "v_add3_u32 v219, v62, v63, v219\n"
    "v_mfma_f32_16x16x32_bf16 v[88:91], a[8:11], v[48:51], v[88:91]\n"
    "v_mfma_f32_16x16x32_bf16 v[88:91], a[24:27], v[52:55], v[88:91]\n"
    "buffer_load_dwordx4 v[16:19], v216, s[28:31], s14 offen offset:0 sc1\n"
    "v_mfma_f32_16x16x32_bf16 v[88:91], a[72:75], v[56:59], v[88:91]\n"
    "v_mfma_f32_16x16x32_bf16 v[88:91], a[104:107], v[60:63], v[88:91]\n"
    "v_mfma_f32_16x16x32_bf16 v[92:95], a[12:15], v[48:51], v[92:95]\n"
    "buffer_load_dwordx4 v[20:23], v216, s[28:31], s14 offen offset:1024 sc1\n"
    "v_mfma_f32_16x16x32_bf16 v[92:95], a[28:31], v[52:55], v[92:95]\n"
    "v_mfma_f32_16x16x32_bf16 v[92:95], a[76:79], v[56:59], v[92:95]\n"
    "v_mfma_f32_16x16x32_bf16 v[92:95], a[108:111], v[60:63], v[92:95]\n"
    "s_waitcnt lgkmcnt(0)\n"
    "s_barrier\n"
    "v_mfma_f32_16x16x32_bf16 v[96:99], a[48:51], v[48:51], v[96:99]\n"
    "ds_read_b128 v[112:115], v230 offset:0\n"
    "v_mfma_f32_16x16x32_bf16 v[96:99], a[32:35], v[52:55], v[96:99]\n"
    "ds_read_b128 v[116:119], v230 offset:1024\n"
    "v_mfma_f32_16x16x32_bf16 v[96:99], a[80:83], v[56:59], v[96:99]\n"
    "ds_read_b128 v[120:123], v230 offset:2048\n"
    "v_mfma_f32_16x16x32_bf16 v[96:99], a[40:43], v[60:63], v[96:99]\n"
    "ds_read_b128 v[124:127], v230 offset:3072\n"
    "v_mfma_f32_16x16x32_bf16 v[100:103], a[52:55], v[48:51], v[100:103]\n"
    "ds_write_b128 v228, v[80:83] offset:0\n"
    "v_mfma_f32_16x16x32_bf16 v[100:103], a[36:39], v[52:55], v[100:103]\n"
    "ds_read_b128 v[128:131], v230 offset:4096\n"
    "ds_read_b128 v[132:135], v230 offset:5120\n"
    "v_mfma_f32_16x16x32_bf16 v[100:103], a[84:87], v[56:59], v[100:103]\n"
    "ds_read_b128 v[136:139], v230 offset:6144\n"
    "v_mfma_f32_16x16x32_bf16 v[100:103], a[44:47], v[60:63], v[100:103]\n"
    "ds_read_b128 v[140:143], v230 offset:7168\n"
    "v_mfma_f32_16x16x32_bf16 v[104:107], a[56:59], v[48:51], v[104:107]\n"
    "buffer_load_dwordx4 v[24:27], v216, s[28:31], s14 offen offset:2048 sc1\n"
    "v_mfma_f32_16x16x32_bf16 v[104:107], a[112:115], v[52:55], v[104:107]\n"
    "ds_write_b128 v228, v[84:87] offset:1024\n"
    "v_mfma_f32_16x16x32_bf16 v[104:107], a[88:91], v[56:59], v[104:107]\n"
    "buffer_load_dwordx4 v[28:31], v216, s[28:31], s14 offen offset:3072 sc1\n"
    "v_mfma_f32_16x16x32_bf16 v[104:107], a[120:123], v[60:63], v[104:107]\n"
    "buffer_load_dword v233, v225, s[28:31], s17 offen sc1\n"
    "v_mfma_f32_16x16x32_bf16 v[108:111], a[60:63], v[48:51], v[108:111]\n"
    "v_mfma_f32_16x16x32_bf16 v[108:111], a[116:119], v[52:55], v[108:111]\n"
    "v_mfma_f32_16x16x32_bf16 v[108:111], a[92:95], v[56:59], v[108:111]\n"
    "ds_write_b128 v228, v[88:91] offset:2048\n"
    "v_mfma_f32_16x16x32_bf16 v[108:111], a[124:127], v[60:63], v[108:111]\n"
    "s_waitcnt vmcnt(11)\n"
    "s_waitcnt lgkmcnt(10)\n"
    "v_mfma_f32_16x16x32_bf16 v[112:115], a[128:131], v[64:67], v[112:115]\n"
    "ds_read_b128 v[144:147], v230 offset:32768\n"
    "ds_read_b128 v[148:151], v230 offset:33792\n"
    "v_mfma_f32_16x16x32_bf16 v[112:115], a[160:163], v[68:71], v[112:115]\n"
    "ds_read_b128 v[152:155], v230 offset:34816\n"
    "s_waitcnt vmcnt(9)\n"
    "v_mfma_f32_16x16x32_bf16 v[112:115], a[192:195], v[72:75], v[112:115]\n"
    "ds_read_b128 v[156:159], v230 offset:35840\n"
    "ds_read_b128 v[160:163], v230 offset:36864\n"
    "v_mfma_f32_16x16x32_bf16 v[112:115], a[224:227], v[76:79], v[112:115]\n"
    "ds_write_b128 v228, v[92:95] offset:3072\n"
    "s_waitcnt lgkmcnt(14)\n"
    "v_mfma_f32_16x16x32_bf16 v[116:119], a[132:135], v[64:67], v[116:119]\n"
    "v_add3_u32 v219, v64, v65, v219\n"
    "v_add3_u32 v219, v66, v67, v219\n"
    "v_mfma_f32_16x16x32_bf16 v[116:119], a[164:167], v[68:71], v[116:119]\n"
    "v_add3_u32 v219, v68, v69, v219\n"
    "v_add3_u32 v219, v70, v71, v219\n"
    "v_mfma_f32_16x16x32_bf16 v[116:119], a[196:199], v[72:75], v[116:119]\n"
    "v_add3_u32 v219, v72, v73, v219\n"
    "v_add3_u32 v219, v74, v75, v219\n"
    "v_mfma_f32_16x16x32_bf16 v[116:119], a[228:231], v[76:79], v[116:119]\n"
    "v_add3_u32 v219, v76, v77, v219\n"
    "v_add3_u32 v219, v78, v79, v219\n"
    "v_mfma_f32_16x16x32_bf16 v[120:123], a[136:139], v[64:67], v[120:123]\n"
    "ds_write_b128 v228, v[96:99] offset:4096\n"
    "v_mfma_f32_16x16x32_bf16 v[120:123], a[168:171], v[68:71], v[120:123]\n"
    "ds_read_b128 v[164:167], v230 offset:37888\n"
    "ds_read_b128 v[168:171], v230 offset:38912\n"
    "v_mfma_f32_16x16x32_bf16 v[120:123], a[200:203], v[72:75], v[120:123]\n"
    "ds_read_b128 v[172:175], v230 offset:39936\n"
    "v_mfma_f32_16x16x32_bf16 v[120:123], a[232:235], v[76:79], v[120:123]\n"
    "s_waitcnt lgkmcnt(14)\n"
    "v_mfma_f32_16x16x32_bf16 v[124:127], a[140:143], v[64:67], v[124:127]\n"
    "v_mfma_f32_16x16x32_bf16 v[124:127], a[172:175], v[68:71], v[124:127]\n"
    "ds_write_b128 v228, v[100:103] offset:5120\n"
    "v_mfma_f32_16x16x32_bf16 v[124:127], a[204:207], v[72:75], v[124:127]\n"
    "v_mfma_f32_16x16x32_bf16 v[124:127], a[236:239], v[76:79], v[124:127]\n"
    "v_mfma_f32_16x16x32_bf16 v[128:131], a[144:147], v[64:67], v[128:131]\n"
    "s_waitcnt vmcnt(0)\n"
    "v_add3_u32 v233, v16, v17, v233\n"
    "v_add3_u32 v233, v18, v19, v233\n"
    "v_mfma_f32_16x16x32_bf16 v[128:131], a[176:179], v[68:71], v[128:131]\n"
    "v_add3_u32 v233, v20, v21, v233\n"
    "v_add3_u32 v233, v22, v23, v233\n"
    "v_mfma_f32_16x16x32_bf16 v[128:131], a[208:211], v[72:75], v[128:131]\n"
    "ds_write_b128 v228, v[104:107] offset:6144\n"
    "v_mfma_f32_16x16x32_bf16 v[128:131], a[240:243], v[76:79], v[128:131]\n"
    "v_add3_u32 v233, v24, v25, v233\n"
    "v_add3_u32 v233, v26, v27, v233\n"
    "v_mfma_f32_16x16x32_bf16 v[132:135], a[148:151], v[64:67], v[132:135]\n"
    "v_add3_u32 v233, v28, v29, v233\n"
    "v_add3_u32 v233, v30, v31, v233\n"
    "v_mfma_f32_16x16x32_bf16 v[132:135], a[180:183], v[68:71], v[132:135]\n"
    "v_add_u32 v233, v233, v250\n"
    "v_cmp_ne_u32 vcc, 0, v233\n"
    "v_mfma_f32_16x16x32_bf16 v[132:135], a[212:215], v[72:75], v[132:135]\n"
    "v_mfma_f32_16x16x32_bf16 v[132:135], a[244:247], v[76:79], v[132:135]\n"
    "ds_write_b128 v228, v[108:111] offset:7168\n"
    "s_waitcnt lgkmcnt(14)\n"
    "v_mfma_f32_16x16x32_bf16 v[136:139], a[152:155], v[64:67], v[136:139]\n"
    "s_cbranch_vccnz Lxs%=\n"
    "Lxok%=:\n"
    "v_mfma_f32_16x16x32_bf16 v[136:139], a[184:187], v[68:71], v[136:139]\n"
    "v_mfma_f32_16x16x32_bf16 v[136:139], a[216:219], v[72:75], v[136:139]\n"
    "v_mfma_f32_16x16x32_bf16 v[136:139], a[248:251], v[76:79], v[136:139]\n"
    "v_mfma_f32_16x16x32_bf16 v[140:143], a[156:159], v[64:67], v[140:143]\n"
    "v_mfma_f32_16x16x32_bf16 v[140:143], a[188:191], v[68:71], v[140:143]\n"
    "v_mfma_f32_16x16x32_bf16 v[140:143], a[220:223], v[72:75], v[140:143]\n"
    "v_mfma_f32_16x16x32_bf16 v[140:143], a[252:255], v[76:79], v[140:143]\n"
    "s_waitcnt lgkmcnt(0)\n"
    "s_barrier\n"
    "v_mfma_f32_16x16x32_bf16 v[112:115], v[244:247], v[16:19], v[112:115]\n"
    "v_mfma_f32_16x16x32_bf16 v[112:115], a[0:3], v[0:3], v[112:115]\n"
    "ds_read_b128 v[176:179], v231 offset:0\n"
    "ds_read_b128 v[180:183], v231 offset:1024\n"
    "v_mfma_f32_16x16x32_bf16 v[112:115], a[16:19], v[4:7], v[112:115]\n"
    "ds_read_b128 v[184:187], v231 offset:2048\n"
    "v_mfma_f32_16x16x32_bf16 v[112:115], a[64:67], v[8:11], v[112:115]\n"
    "ds_read_b128 v[188:191], v231 offset:3072\n"
    "ds_read_b128 v[196:199], v231 offset:4096\n"
    "v_mfma_f32_16x16x32_bf16 v[112:115], a[96:99], v[12:15], v[112:115]\n"
    "v_add3_u32 v219, v0, v1, v219\n"
    "v_add3_u32 v219, v2, v3, v219\n"
    "v_mfma_f32_16x16x32_bf16 v[116:119], v[242:245], v[16:19], v[116:119]\n"
    "v_mfma_f32_16x16x32_bf16 v[116:119], a[4:7], v[0:3], v[116:119]\n"
    "v_add3_u32 v219, v4, v5, v219\n"
    "v_add3_u32 v219, v6, v7, v219\n"
    "v_mfma_f32_16x16x32_bf16 v[116:119], a[20:23], v[4:7], v[116:119]\n"
    "v_add3_u32 v219, v8, v9, v219\n"
    "v_add3_u32 v219, v10, v11, v219\n"
    "v_mfma_f32_16x16x32_bf16 v[116:119], a[68:71], v[8:11], v[116:119]\n"
    "v_add3_u32 v219, v12, v13, v219\n"
    "v_add3_u32 v219, v14, v15, v219\n"
    "v_mfma_f32_16x16x32_bf16 v[116:119], a[100:103], v[12:15], v[116:119]\n"
    "ds_read_b128 v[200:203], v231 offset:5120\n"
    "ds_read_b128 v[204:207], v231 offset:6144\n"
    "v_mfma_f32_16x16x32_bf16 v[120:123], v[244:247], v[20:23], v[120:123]\n"
    "v_mfma_f32_16x16x32_bf16 v[120:123], a[8:11], v[0:3], v[120:123]\n"
    "ds_read_b128 v[208:211], v231 offset:7168\n"
    "v_mfma_f32_16x16x32_bf16 v[120:123], a[24:27], v[4:7], v[120:123]\n"
    "v_mfma_f32_16x16x32_bf16 v[120:123], a[72:75], v[8:11], v[120:123]\n"
    "s_waitcnt lgkmcnt(7)\n"
    "v_add_f32 v144, v144, v176\n"
    "v_add_f32 v145, v145, v177\n"
    "v_mfma_f32_16x16x32_bf16 v[120:123], a[104:107], v[12:15], v[120:123]\n"
    "v_add_f32 v146, v146, v178\n"
    "v_add_f32 v147, v147, v179\n"
    "v_mfma_f32_16x16x32_bf16 v[124:127], v[242:245], v[20:23], v[124:127]\n"
    "s_waitcnt lgkmcnt(6)\n"
    "v_add_f32 v148, v148, v180\n"
    "v_add_f32 v149, v149, v181\n"
    "v_mfma_f32_16x16x32_bf16 v[124:127], a[12:15], v[0:3], v[124:127]\n"
    "v_add_f32 v150, v150, v182\n"
    "v_add_f32 v151, v151, v183\n"
    "v_mfma_f32_16x16x32_bf16 v[124:127], a[28:31], v[4:7], v[124:127]\n"
    "v_add_f32 v112, v112, v144\n"
    "v_add_f32 v113, v113, v145\n"
    "v_mfma_f32_16x16x32_bf16 v[124:127], a[76:79], v[8:11], v[124:127]\n"
    "v_add_f32 v114, v114, v146\n"
    "v_add_f32 v115, v115, v147\n"
    "v_mfma_f32_16x16x32_bf16 v[124:127], a[108:111], v[12:15], v[124:127]\n"
    "v_add_f32 v116, v116, v148\n"
    "v_add_f32 v117, v117, v149\n"
    "v_mfma_f32_16x16x32_bf16 v[128:131], v[244:247], v[24:27], v[128:131]\n"
    "v_add_f32 v118, v118, v150\n"
    "v_add_f32 v119, v119, v151\n"
    "v_mfma_f32_16x16x32_bf16 v[128:131], a[48:51], v[0:3], v[128:131]\n"
    "v_cvt_pk_bf16_f32 v112, v112, v113\n"
    "v_cvt_pk_bf16_f32 v113, v114, v115\n"
    "v_mfma_f32_16x16x32_bf16 v[128:131], a[32:35], v[4:7], v[128:131]\n"
    "v_cvt_pk_bf16_f32 v114, v116, v117\n"
    "v_cvt_pk_bf16_f32 v115, v118, v119\n"
    "v_mfma_f32_16x16x32_bf16 v[128:131], a[80:83], v[8:11], v[128:131]\n"
    "buffer_store_dwordx4 v[112:115], v229, s[36:39], s90 offen offset:0\n"
    "v_mfma_f32_16x16x32_bf16 v[128:131], a[40:43], v[12:15], v[128:131]\n"
    "s_waitcnt lgkmcnt(5)\n"
    "v_add_f32 v152, v152, v184\n"
    "v_add_f32 v153, v153, v185\n"
    "v_mfma_f32_16x16x32_bf16 v[132:135], v[242:245], v[24:27], v[132:135]\n"
    "v_add_f32 v154, v154, v186\n"
    "v_add_f32 v155, v155, v187\n"
    "v_mfma_f32_16x16x32_bf16 v[132:135], a[52:55], v[0:3], v[132:135]\n"
    "s_waitcnt lgkmcnt(4)\n"
    "v_add_f32 v156, v156, v188\n"
    "v_add_f32 v157, v157, v189\n"
    "v_mfma_f32_16x16x32_bf16 v[132:135], a[36:39], v[4:7], v[132:135]\n"
    "v_add_f32 v158, v158, v190\n"
    "v_add_f32 v159, v159, v191\n"
    "v_mfma_f32_16x16x32_bf16 v[132:135], a[84:87], v[8:11], v[132:135]\n"
    "v_add_f32 v120, v120, v152\n"
    "v_add_f32 v121, v121, v153\n"
    "v_mfma_f32_16x16x32_bf16 v[132:135], a[44:47], v[12:15], v[132:135]\n"
    "v_add_f32 v122, v122, v154\n"
    "v_add_f32 v123, v123, v155\n"
    "v_mfma_f32_16x16x32_bf16 v[136:139], v[244:247], v[28:31], v[136:139]\n"
    "v_add_f32 v124, v124, v156\n"
    "v_add_f32 v125, v125, v157\n"
    "v_mfma_f32_16x16x32_bf16 v[136:139], a[56:59], v[0:3], v[136:139]\n"
    "v_add_f32 v126, v126, v158\n"
    "v_add_f32 v127, v127, v159\n"
    "v_mfma_f32_16x16x32_bf16 v[136:139], a[112:115], v[4:7], v[136:139]\n"
    "v_cvt_pk_bf16_f32 v120, v120, v121\n"
    "v_cvt_pk_bf16_f32 v121, v122, v123\n"
    "v_mfma_f32_16x16x32_bf16 v[136:139], a[88:91], v[8:11], v[136:139]\n"
    "v_cvt_pk_bf16_f32 v122, v124, v125\n"
    "v_cvt_pk_bf16_f32 v123, v126, v127\n"
    "v_mfma_f32_16x16x32_bf16 v[136:139], a[120:123], v[12:15], v[136:139]\n"
    "buffer_store_dwordx4 v[120:123], v229, s[36:39], s90 offen offset:64\n"
    "v_mfma_f32_16x16x32_bf16 v[140:143], v[242:245], v[28:31], v[140:143]\n"
    "s_waitcnt lgkmcnt(3)\n"
    "v_add_f32 v160, v160, v196\n"
    "v_add_f32 v161, v161, v197\n"
    "v_mfma_f32_16x16x32_bf16 v[140:143], a[60:63], v[0:3], v[140:143]\n"
    "v_add_f32 v162, v162, v198\n"
    "v_add_f32 v163, v163, v199\n"
    "v_mfma_f32_16x16x32_bf16 v[140:143], a[116:119], v[4:7], v[140:143]\n"
    "s_waitcnt lgkmcnt(2)\n"
    "v_add_f32 v164, v164, v200\n"
    "v_add_f32 v165, v165, v201\n"
    "v_mfma_f32_16x16x32_bf16 v[140:143], a[92:95], v[8:11], v[140:143]\n"
    "v_add_f32 v166, v166, v202\n"
    "v_add_f32 v167, v167, v203\n"
    "v_mfma_f32_16x16x32_bf16 v[140:143], a[124:127], v[12:15], v[140:143]\n"
    "v_add_f32 v128, v128, v160\n"
    "v_add_f32 v129, v129, v161\n"
    "v_pk_add_f32 v[130:131], v[130:131], v[162:163]\n"
    "v_pk_add_f32 v[132:133], v[132:133], v[164:165]\n"
    "v_pk_add_f32 v[134:135], v[134:135], v[166:167]\n"
    "v_cvt_pk_bf16_f32 v128, v128, v129\n"
    "v_cvt_pk_bf16_f32 v129, v130, v131\n"
    "v_cvt_pk_bf16_f32 v130, v132, v133\n"
    "v_cvt_pk_bf16_f32 v131, v134, v135\n"
    "buffer_store_dwordx4 v[128:131], v229, s[36:39], s90 offen offset:128\n"
    "s_waitcnt lgkmcnt(1)\n"
    "v_pk_add_f32 v[168:169], v[168:169], v[204:205]\n"
    "v_pk_add_f32 v[170:171], v[170:171], v[206:207]\n"
    "s_waitcnt lgkmcnt(0)\n"
    "v_pk_add_f32 v[172:173], v[172:173], v[208:209]\n"
    "v_pk_add_f32 v[174:175], v[174:175], v[210:211]\n"
    "v_pk_add_f32 v[136:137], v[136:137], v[168:169]\n"
    "v_pk_add_f32 v[138:139], v[138:139], v[170:171]\n"
    "v_pk_add_f32 v[140:141], v[140:141], v[172:173]\n"
    "v_pk_add_f32 v[142:143], v[142:143], v[174:175]\n"
    "v_cvt_pk_bf16_f32 v136, v136, v137\n"
    "v_cvt_pk_bf16_f32 v137, v138, v139\n"
    "v_cvt_pk_bf16_f32 v138, v140, v141\n"
    "v_cvt_pk_bf16_f32 v139, v142, v143\n"
    "buffer_store_dwordx4 v[136:139], v229, s[36:39], s90 offen offset:192\n"
    "v_add_u32_dpp v219, v219, v219 row_shr:1 row_mask:0xf bank_mask:0xf\n"
    "s_nop 1\n"
    "v_add_u32_dpp v219, v219, v219 row_shr:2 row_mask:0xf bank_mask:0xf\n"
    "s_nop 1\n"
    "v_add_u32_dpp v219, v219, v219 row_shr:4 row_mask:0xf bank_mask:0xf\n"
    "s_nop 1\n"
    "v_add_u32_dpp v219, v219, v219 row_shr:8 row_mask:0xf bank_mask:0xf\n"
    "s_nop 1\n"
    "v_add_u32_dpp v219, v219, v219 row_bcast:15 row_mask:0xa bank_mask:0xf\n"
    "s_nop 1\n"
    "v_add_u32_dpp v219, v219, v219 row_bcast:31 row_mask:0xc bank_mask:0xf\n"
    "s_nop 0\n"
    "v_readlane_b32 s75, v219, 63\n"
    "s_cmp_eq_u32 s74, 0\n"
    "s_nop 0\n"
    "s_cselect_b32 s75, 0, s75\n"
    "v_mov_b32 v218, s75\n"
    "ds_write_b32 v222, v218 offset:16\n"
    "s_waitcnt lgkmcnt(0)\n"
    "s_barrier\n"
    "ds_read_b128 v[234:237], v223 offset:16\n"
    "s_waitcnt lgkmcnt(0)\n"
    "v_or3_b32 v234, v234, v235, v236\n"
    "v_or_b32 v234, v234, v237\n"
    "v_cmp_ne_u32 vcc, 0, v234\n"
    "s_nop 4\n"
    "s_cbranch_vccnz Lredo%=\n"
    "s_or_b32 s75, s53, s48\n"
    "s_cmp_lg_u32 s75, 0\n"
    "s_cbranch_scc1 Lend%=\n"
    "buffer_store_dword v250, v251, s[28:31], 0 offen sc1\n"
    "Lend%=:\n"
    "s_branch Lfin%=\n"
    "Lgs%=:\n"
    "v_lshlrev_b32 v194, 4, v253\n"
    "s_lshr_b32 s75, s93, 1\n"
    "s_lshl_b32 s75, s75, 4\n"
    "v_or_b32 v194, s75, v194\n"
    "v_xor_b32 v195, 0, v194\n"
    "buffer_load_dwordx4 v[226:229], v195, s[28:31], s89 offen sc1\n"
    "v_xor_b32 v195, 16, v194\n"
    "buffer_load_dwordx4 v[230:233], v195, s[28:31], s89 offen sc1\n"
    "v_xor_b32 v195, 32, v194\n"
    "buffer_load_dwordx4 v[234:237], v195, s[28:31], s89 offen sc1\n"
    "v_xor_b32 v195, 48, v194\n"
    "buffer_load_dwordx4 v[238:241], v195, s[28:31], s89 offen sc1\n"
    "s_waitcnt vmcnt(0)\n"
    "v_or3_b32 v194, v227, v229, v231\n"
    "v_bitop3_b32 v195, v227, v229, v231 bitop3:0x80\n"
    "v_or3_b32 v194, v194, v233, v235\n"
    "v_bitop3_b32 v195, v195, v233, v235 bitop3:0x80\n"
    "v_or3_b32 v194, v194, v237, v239\n"
    "v_bitop3_b32 v195, v195, v237, v239 bitop3:0x80\n"
    "v_or3_b32 v194, v194, v241, v250\n"
    "v_bitop3_b32 v195, v195, v241, v250 bitop3:0x80\n"
    "v_cmp_ne_u32 vcc, v194, v195\n"
    "s_nop 4\n"
    "s_cbranch_vccz Lgok%=\n"
    "s_sub_u32 s74, s74, 1\n"
    "s_max_i32 s74, s74, 0\n"
    "s_cmp_lg_u32 s74, 0\n"
    "s_cbranch_scc1 Lgs%=\n"
    "s_branch Lgok%=\n"
    "Lks%=:\n"
    "s_cmp_eq_u32 s74, 0\n"
    "s_cbranch_scc1 Lkok%=\n"
    "s_mov_b32 s92, 0\n"
    "v_and_b32 v195, 7, v192\n"
    "v_mul_u32_u24 v195, 3072, v195\n"
    "buffer_load_dword v194, v195, s[28:31], s84 offen offset:1528 sc1\n"
    "v_cmp_ne_u32 vcc, 0, v4\n"
    "s_nop 4\n"
    "s_cmp_lg_u64 vcc, 0\n"
    "s_cselect_b32 s75, 1, 0\n"
    "s_or_b32 s92, s92, s75\n"
    "v_cmp_ne_u32 vcc, 0, v5\n"
    "s_nop 4\n"
    "s_cmp_lg_u64 vcc, 0\n"
    "s_cselect_b32 s75, 2, 0\n"
    "s_or_b32 s92, s92, s75\n"
    "v_cmp_ne_u32 vcc, 0, v6\n"
    "s_nop 4\n"
    "s_cmp_lg_u64 vcc, 0\n"
    "s_cselect_b32 s75, 4, 0\n"
    "s_or_b32 s92, s92, s75\n"
    "v_cmp_ne_u32 vcc, 0, v7\n"
    "s_nop 4\n"
    "s_cmp_lg_u64 vcc, 0\n"
    "s_cselect_b32 s75, 8, 0\n"
    "s_or_b32 s92, s92, s75\n"
    "v_cmp_ne_u32 vcc, 0, v28\n"
    "s_nop 4\n"
    "s_cmp_lg_u64 vcc, 0\n"
    "s_cselect_b32 s75, 16, 0\n"
    "s_or_b32 s92, s92, s75\n"
    "v_cmp_ne_u32 vcc, 0, v29\n"
    "s_nop 4\n"
    "s_cmp_lg_u64 vcc, 0\n"
    "s_cselect_b32 s75, 32, 0\n"
    "s_or_b32 s92, s92, s75\n"
    "v_cmp_ne_u32 vcc, 0, v30\n"
    "s_nop 4\n"
    "s_cmp_lg_u64 vcc, 0\n"
    "s_cselect_b32 s75, 64, 0\n"
    "s_or_b32 s92, s92, s75\n"
    "v_cmp_ne_u32 vcc, 0, v31\n"
    "s_nop 4\n"
    "s_cmp_lg_u64 vcc, 0\n"
    "s_cselect_b32 s75, 128, 0\n"
    "s_or_b32 s92, s92, s75\n"
    "s_branch Lkw%=\n"
    "Lkz%=:\n"
    "buffer_load_dword v194, v195, s[28:31], s84 offen offset:1528 sc1\n"
    "Lkw%=:\n"
    "s_waitcnt vmcnt(0)\n"
    "v_cmp_ne_u32 vcc, v194, v250\n"
    "s_sub_u32 s74, s74, 1\n"
    "s_max_i32 s74, s74, 0\n"
    "s_nop 2\n"
    "s_cbranch_vccz Lkr%=\n"
    "s_cmp_lg_u32 s74, 0\n"
    "s_cbranch_scc1 Lkz%=\n"
    "Lkr%=:\n"
    "s_bitcmp1_b32 s92, 0\n"
    "s_cbranch_scc0 Lkp0%=\n"
    "s_add_u32 s63, s84, 0\n"
    "buffer_load_dwordx4 v[8:11], v216, s[28:31], s63 offen offset:0 sc1\n"
    "buffer_load_dwordx2 v[112:113], v252, s[28:31], s63 offen offset:1024 sc1\n"
    "Lkp0%=:\n"
    "s_bitcmp1_b32 s92, 1\n"
    "s_cbranch_scc0 Lkp1%=\n"
    "s_add_u32 s63, s84, 3072\n"
    "buffer_load_dwordx4 v[32:35], v216, s[28:31], s63 offen offset:0 sc1\n"
    "buffer_load_dwordx2 v[114:115], v252, s[28:31], s63 offen offset:1024 sc1\n"
    "Lkp1%=:\n"
    "s_bitcmp1_b32 s92, 2\n"
    "s_cbranch_scc0 Lkp2%=\n"
    "s_add_u32 s63, s84, 6144\n"
    "buffer_load_dwordx4 v[56:59], v216, s[28:31], s63 offen offset:0 sc1\n"
    "buffer_load_dwordx2 v[116:117], v252, s[28:31], s63 offen offset:1024 sc1\n"
    "Lkp2%=:\n"
    "s_bitcmp1_b32 s92, 3\n"
    "s_cbranch_scc0 Lkp3%=\n"
    "s_add_u32 s63, s84, 9216\n"
    "buffer_load_dwordx4 v[80:83], v216, s[28:31], s63 offen offset:0 sc1\n"
    "buffer_load_dwordx2 v[118:119], v252, s[28:31], s63 offen offset:1024 sc1\n"
    "Lkp3%=:\n"
    "s_bitcmp1_b32 s92, 4\n"
    "s_cbranch_scc0 Lkp4%=\n"
    "s_add_u32 s63, s84, 12288\n"
    "buffer_load_dwordx4 v[104:107], v216, s[28:31], s63 offen offset:0 sc1\n"
    "buffer_load_dwordx2 v[136:137], v252, s[28:31], s63 offen offset:1024 sc1\n"
    "Lkp4%=:\n"
    "s_bitcmp1_b32 s92, 5\n"
    "s_cbranch_scc0 Lkp5%=\n"
    "s_add_u32 s63, s84, 15360\n"
    "buffer_load_dwordx4 v[128:131], v216, s[28:31], s63 offen offset:0 sc1\n"
    "buffer_load_dwordx2 v[138:139], v252, s[28:31], s63 offen offset:1024 sc1\n"
    "Lkp5%=:\n"
    "s_bitcmp1_b32 s92, 6\n"
    "s_cbranch_scc0 Lkp6%=\n"
    "s_add_u32 s63, s84, 18432\n"
    "buffer_load_dwordx4 v[152:155], v216, s[28:31], s63 offen offset:0 sc1\n"
    "buffer_load_dwordx2 v[140:141], v252, s[28:31], s63 offen offset:1024 sc1\n"
    "Lkp6%=:\n"
    "s_bitcmp1_b32 s92, 7\n"
    "s_cbranch_scc0 Lkp7%=\n"
    "s_add_u32 s63, s84, 21504\n"
    "buffer_load_dwordx4 v[176:179], v216, s[28:31], s63 offen offset:0 sc1\n"
    "buffer_load_dwordx2 v[142:143], v252, s[28:31], s63 offen offset:1024 sc1\n"
    "Lkp7%=:\n"
    "s_waitcnt vmcnt(0)\n"
    "s_branch Lsum%=\n"
    "Lh%=:\n"
    "s_add_u32 s92, s92, 1\n"
    "s_cmp_eq_u32 s92, 3\n"
    "s_cbranch_scc0 Lhp%=\n"
    "buffer_store_dwordx4 v[116:119], v216, s[20:23], s88 offen offset:0 sc1\n"
    "buffer_store_dwordx4 v[140:143], v216, s[20:23], s88 offen offset:1024 sc1\n"
    "buffer_store_dwordx4 v[164:167], v216, s[20:23], s88 offen offset:2048 sc1\n"
    "buffer_store_dwordx4 v[188:191], v216, s[20:23], s88 offen offset:3072 sc1\n"
    "buffer_store_dwordx3 v[204:206], v251, s[20:23], s97 offen sc1\n"
    "s_mov_b32 s22, 0\n"
    "Lhp%=:\n"
    "buffer_load_dword v194, v195, s[28:31], s86 offen sc1\n"
    "s_sub_u32 s74, s74, 1\n"
    "s_max_i32 s74, s74, 0\n"
    "s_waitcnt vmcnt(0)\n"
    "v_cmp_ne_u32 vcc, v194, v250\n"
    "s_nop 4\n"
    "s_cbranch_vccz Lhl%=\n"
    "s_cmp_lg_u32 s74, 0\n"
    "s_cbranch_scc1 Lh%=\n"
    "Lhl%=:\n"
    "buffer_load_dwordx4 v[0:3], v216, s[28:31], s4 offen offset:0 sc1\n"
    "buffer_load_dwordx4 v[4:7], v216, s[28:31], s5 offen offset:0 sc1\n"
    "buffer_load_dwordx4 v[8:11], v216, s[28:31], s6 offen offset:0 sc1\n"
    "buffer_load_dwordx4 v[12:15], v216, s[28:31], s7 offen offset:0 sc1\n"
    "buffer_load_dwordx4 v[16:19], v216, s[28:31], s8 offen offset:0 sc1\n"
    "buffer_load_dwordx4 v[20:23], v216, s[28:31], s9 offen offset:0 sc1\n"
    "buffer_load_dwordx4 v[24:27], v216, s[28:31], s10 offen offset:0 sc1\n"
    "buffer_load_dwordx4 v[28:31], v216, s[28:31], s11 offen offset:0 sc1\n"
    "s_branch Lhm%=\n"
    "Lcs%=:\n"
    "buffer_load_dwordx3 v[238:240], v224, s[28:31], s79 offen sc1\n"
    "s_waitcnt vmcnt(0)\n"
    "v_cmp_ne_u32 vcc, v240, v250\n"
    "s_nop 4\n"
    "s_cbranch_vccz Lcok%=\n"
    "s_sub_u32 s74, s74, 1\n"
    "s_max_i32 s74, s74, 0\n"
    "s_cmp_lg_u32 s74, 0\n"
    "s_cbranch_scc1 Lcs%=\n"
    "s_branch Lcok%=\n"
    "Lxs%=:\n"
    "buffer_load_dwordx4 v[16:19], v216, s[28:31], s14 offen offset:0 sc1\n"
    "buffer_load_dwordx4 v[20:23], v216, s[28:31], s14 offen offset:1024 sc1\n"
    "buffer_load_dwordx4 v[24:27], v216, s[28:31], s14 offen offset:2048 sc1\n"
    "buffer_load_dwordx4 v[28:31], v216, s[28:31], s14 offen offset:3072 sc1\n"
    "buffer_load_dword v233, v225, s[28:31], s17 offen sc1\n"
    "s_waitcnt vmcnt(0)\n"
    "v_add3_u32 v233, v16, v17, v233\n"
    "v_add3_u32 v233, v18, v19, v233\n"
    "v_add3_u32 v233, v20, v21, v233\n"
    "v_add3_u32 v233, v22, v23, v233\n"
    "v_add3_u32 v233, v24, v25, v233\n"
    "v_add3_u32 v233, v26, v27, v233\n"
    "v_add3_u32 v233, v28, v29, v233\n"
    "v_add3_u32 v233, v30, v31, v233\n"
    "v_add_u32 v233, v233, v250\n"
    "v_cmp_ne_u32 vcc, 0, v233\n"
    "s_nop 4\n"
    "s_cbranch_vccz Lxok%=\n"
    "s_sub_u32 s74, s74, 1\n"
    "s_max_i32 s74, s74, 0\n"
    "s_cmp_lg_u32 s74, 0\n"
    "s_cbranch_scc1 Lxs%=\n"
    "s_branch Lxok%=\n"
    "Lredo%=:\n"
    "s_sub_u32 s74, s74, 256\n"
    "s_max_i32 s74, s74, 0\n"
    "s_waitcnt vmcnt(0) lgkmcnt(0)\n"
    "s_nop 15\n"
    "s_branch Lhl%=\n"
    "Lfin%=:\n"
    :
    : [ka] "s"(__builtin_amdgcn_kernarg_segment_ptr()), [wid] "s"(wid), [bid] "s"(blockIdx.x), "s"((unsigned)(size_t)lds)
    : "v0","v1","v2","v3","v4","v5","v6","v7","v8","v9","v10","v11","v12","v13","v14","v15","v16","v17","v18","v19","v20","v21","v22","v23","v24","v25","v26","v27","v28","v29","v30","v31","v32","v33","v34","v35","v36","v37","v38","v39","v40","v41","v42","v43","v44","v45","v46","v47","v48","v49","v50","v51","v52","v53","v54","v55","v56","v57","v58","v59","v60","v61","v62","v63","v64","v65","v66","v67","v68","v69","v70","v71","v72","v73","v74","v75","v76","v77","v78","v79","v80","v81","v82","v83","v84","v85","v86","v87","v88","v89","v90","v91","v92","v93","v94","v95","v96","v97","v98","v99","v100","v101","v102","v103","v104","v105","v106","v107","v108","v109","v110","v111","v112","v113","v114","v115","v116","v117","v118","v119","v120","v121","v122","v123","v124","v125","v126","v127","v128","v129","v130","v131","v132","v133","v134","v135","v136","v137","v138","v139","v140","v141","v142","v143","v144","v145","v146","v147","v148","v149","v150","v151","v152","v153","v154","v155","v156","v157","v158","v159","v160","v161","v162","v163","v164","v165","v166","v167","v168","v169","v170","v171","v172","v173","v174","v175","v176","v177","v178","v179","v180","v181","v182","v183","v184","v185","v186","v187","v188","v189","v190","v191","v192","v193","v194","v195","v196","v197","v198","v199","v200","v201","v202","v203","v204","v205","v206","v207","v208","v209","v210","v211","v212","v213","v214","v215","v216","v217","v218","v219","v220","v221","v222","v223","v224","v225","v226","v227","v228","v229","v230","v231","v232","v233","v234","v235","v236","v237","v238","v239","v240","v241","v242","v243","v244","v245","v246","v247","v248","v249","v250","v251","v252","v253","v254","v255","a0","a1","a2","a3","a4","a5","a6","a7","a8","a9","a10","a11","a12","a13","a14","a15","a16","a17","a18","a19","a20","a21","a22","a23","a24","a25","a26","a27","a28","a29","a30","a31","a32","a33","a34","a35","a36","a37","a38","a39","a40","a41","a42","a43","a44","a45","a46","a47","a48","a49","a50","a51","a52","a53","a54","a55","a56","a57","a58","a59","a60","a61","a62","a63","a64","a65","a66","a67","a68","a69","a70","a71","a72","a73","a74","a75","a76","a77","a78","a79","a80","a81","a82","a83","a84","a85","a86","a87","a88","a89","a90","a91","a92","a93","a94","a95","a96","a97","a98","a99","a100","a101","a102","a103","a104","a105","a106","a107","a108","a109","a110","a111","a112","a113","a114","a115","a116","a117","a118","a119","a120","a121","a122","a123","a124","a125","a126","a127","a128","a129","a130","a131","a132","a133","a134","a135","a136","a137","a138","a139","a140","a141","a142","a143","a144","a145","a146","a147","a148","a149","a150","a151","a152","a153","a154","a155","a156","a157","a158","a159","a160","a161","a162","a163","a164","a165","a166","a167","a168","a169","a170","a171","a172","a173","a174","a175","a176","a177","a178","a179","a180","a181","a182","a183","a184","a185","a186","a187","a188","a189","a190","a191","a192","a193","a194","a195","a196","a197","a198","a199","a200","a201","a202","a203","a204","a205","a206","a207","a208","a209","a210","a211","a212","a213","a214","a215","a216","a217","a218","a219","a220","a221","a222","a223","a224","a225","a226","a227","a228","a229","a230","a231","a232","a233","a234","a235","a236","a237","a238","a239","a240","a241","a242","a243","a244","a245","a246","a247","a248","a249","a250","a251","a252","a253","a254","a255","s4","s5","s6","s7","s8","s9","s10","s11","s12","s13","s14","s15","s16","s17","s18","s19","s20","s21","s22","s23","s24","s25","s26","s27","s28","s29","s30","s31","s33","s34","s35","s36","s37","s38","s39","s40","s41","s42","s43","s44","s45","s46","s47","s48","s49","s50","s51","s52","s53","s54","s55","s56","s57","s58","s59","s60","s61","s62","s63","s64","s65","s66","s67","s68","s69","s70","s71","s72","s73","s74","s75","s76","s77","s78","s79","s80","s81","s82","s83","s84","s85","s86","s87","s88","s89","s90","s91","s92","s93","s94","s95","s96","s97","vcc","scc","memory");
}

extern "C" void kernel_launch(void* const* d_in, const int* in_sizes, int n_in, void* d_out, int out_size, void* d_ws,
                              size_t ws_size, hipStream_t stream) {
  gmla_fused<<<256, 256, 0, stream>>>(d_in[0], d_in[1], d_in[2], d_in[4], d_in[5], d_in[6], d_out, d_ws);
}
```
